```python
import torch

def _softmax(x, axis=-1):
    return torch.softmax(x, dim=axis)
def _gelu(x):
    return torch.nn.functional.gelu(x, approximate="tanh")
def _silu(x):
    return torch.nn.functional.silu(x)
def _sigmoid(x):
    return torch.sigmoid(x)
def _layernorm(x, axis=-1, eps=1e-5):
    m = x.mean(dim=axis, keepdim=True)
    v = x.var(dim=axis, keepdim=True, unbiased=False)
    return (x - m) / torch.sqrt(v + eps)
def _scatter_add(tgt, idx, src, axis=0):
    return tgt.clone().index_add_(axis, idx, src)
def _segment_sum(src, seg, n):
    out = torch.zeros((n,) + src.shape[1:], dtype=src.dtype)
    return out.index_add_(0, seg, src)
def _pad(x, pw):
    flat = tuple(v for pair in reversed(pw) for v in pair)
    return torch.nn.functional.pad(x, flat)

kernel_name = 'parallel_sum__softmax_full_mha__glu_gated_glu__prompt'

def setup_inputs():

    _w = (lambda a: (lambda t: t.to(torch.bfloat16) if t.is_floating_point() else t)(torch.as_tensor(a)))
    def _A_setup():
        B, S, D = 1, 2048, 512
        return {
            "x": _w((lambda *s: torch.randn(*s, dtype=torch.bfloat16) * 0.05)(B, S, D)),
            "wqkv": _w((lambda *s: torch.randn(*s, dtype=torch.bfloat16) * 0.2)(D, 3 * D)),
            "wo": _w((lambda *s: torch.randn(*s, dtype=torch.bfloat16) * 0.1)(D, D)),
        }
    _A_in = _A_setup()

    def _B_setup():
        B, S, D, F = 1, 2048, 512, 1792
        return {
            "x": _w((lambda *s: torch.randn(*s, dtype=torch.bfloat16) * 0.05)(B, S, D)),
            "w_gate": _w((lambda *s: torch.randn(*s, dtype=torch.bfloat16) * 0.8)(D, F)),
            "w_up": _w((lambda *s: torch.randn(*s, dtype=torch.bfloat16) * 0.2)(D, F)),
            "w_down": _w((lambda *s: torch.randn(*s, dtype=torch.bfloat16) * 0.2)(F, D)),
        }
    _B_in = _B_setup()

    return {
        "A_x": _A_in["x"],
        "A_wqkv": _A_in["wqkv"],
        "A_wo": _A_in["wo"],
        "B_x": _B_in["x"],
        "B_w_gate": _B_in["w_gate"],
        "B_w_up": _B_in["w_up"],
        "B_w_down": _B_in["w_down"],
    }

def reference(A_x, A_wqkv, A_wo, B_x, B_w_gate, B_w_up, B_w_down):
    def _A_ref(x, wqkv, wo):
        B, S, D = x.shape
        H = 32; Dh = D // H
        qkv = (lambda x, *d: x.permute(*d))((x @ wqkv).reshape(B, S, 3, H, Dh), 2, 0, 3, 1, 4)
        q, k, v = qkv[0], qkv[1], qkv[2]
        scale = Dh ** -0.5
        scores = torch.einsum("bhqd,bhkd->bhqk", q, k) * scale
        p = _softmax(scores, axis=-1)
        o = torch.einsum("bhqk,bhkd->bhqd", p, v)
        return (lambda x, *d: x.permute(*d))(o, 0, 2, 1, 3).reshape(B, S, D) @ wo

    def _B_ref(x, w_gate, w_up, w_down):
        g = torch.einsum("bsd,df->bsf", x, w_gate)
        u = torch.einsum("bsd,df->bsf", x, w_up)
        h = _sigmoid(g) * u
        return torch.einsum("bsf,fd->bsd", h, w_down)

    x = A_x
    xn = _layernorm(x, axis=-1)
    return x + _A_ref(xn, A_wqkv, A_wo) + _B_ref(xn, B_w_gate, B_w_up, B_w_down)
```

```cpp
#include <hip/hip_runtime.h>
#include <hip/hip_bf16.h>
typedef unsigned short u16;
__device__ __forceinline__ float bf2f(u16 v){ return __uint_as_float(((unsigned)v)<<16); }
__device__ __forceinline__ u16 f2bf(float f){ unsigned u=__float_as_uint(f); u += 0x7FFF + ((u>>16)&1); return (u16)(u>>16); }
constexpr int S=2048, D=512, F=1792, H=32, DH=16;

__global__ void k_ln(const u16* x, u16* xn){
  int row=blockIdx.x, t=threadIdx.x;
  float v[8]; float s=0;
  for(int i=0;i<8;i++){ v[i]=bf2f(x[row*D+t*8+i]); s+=v[i]; }
  for(int o=32;o;o>>=1) s+=__shfl_xor(s,o);
  float m=s/D; float q=0;
  for(int i=0;i<8;i++){ float d=v[i]-m; q+=d*d; }
  for(int o=32;o;o>>=1) q+=__shfl_xor(q,o);
  float r=1.0f/sqrtf(q/D+1e-5f);
  for(int i=0;i<8;i++) xn[row*D+t*8+i]=f2bf((v[i]-m)*r);
}
__global__ void k_gemm(const u16* A, const u16* W, float* C, int M, int N, int K){
  __shared__ float a[32][33], w[32][33];
  int tx=threadIdx.x&31, ty=threadIdx.x>>5;
  int m0=blockIdx.y*32, n0=blockIdx.x*32;
  float acc[4]={0,0,0,0};
  for(int k0=0;k0<K;k0+=32){
    for(int i=0;i<4;i++){ a[ty+8*i][tx]=bf2f(A[(m0+ty+8*i)*K+k0+tx]); w[ty+8*i][tx]=bf2f(W[(k0+ty+8*i)*N+n0+tx]); }
    __syncthreads();
    for(int k=0;k<32;k++){ float b=w[k][tx]; for(int i=0;i<4;i++) acc[i]+=a[ty+8*i][k]*b; }
    __syncthreads();
  }
  for(int i=0;i<4;i++) C[(m0+ty+8*i)*N+n0+tx]=acc[i];
}
__global__ void k_glu(const float* g, const float* u, u16* h){
  int i=blockIdx.x*256+threadIdx.x;
  h[i]=f2bf(u[i]/(1.0f+__expf(-g[i])));
}
__global__ void k_attn(const float* qkv, u16* o){
  int h=blockIdx.y; int q=blockIdx.x*256+threadIdx.x;
  __shared__ float ks[256][16], vs[256][16];
  float qv[16];
  for(int d=0;d<16;d++) qv[d]=bf2f(f2bf(qkv[q*1536+h*16+d]*0.25f*1.4426950408889634f));
  float m=-1e30f,l=0,acc[16]; for(int d=0;d<16;d++)acc[d]=0;
  for(int k0=0;k0<S;k0+=256){
    __syncthreads();
    for(int d=0;d<16;d++){ ks[threadIdx.x][d]=bf2f(f2bf(qkv[(k0+threadIdx.x)*1536+512+h*16+d])); vs[threadIdx.x][d]=bf2f(f2bf(qkv[(k0+threadIdx.x)*1536+1024+h*16+d])); }
    __syncthreads();
    for(int k=0;k<256;k++){
      float s=0; for(int d=0;d<16;d++) s+=qv[d]*ks[k][d];
      float mn=fmaxf(m,s); float a=exp2f(m-mn); float p=exp2f(s-mn);
      p=__uint_as_float(__float_as_uint(p)&0xffff0000u);
      l=l*a+p; for(int d=0;d<16;d++) acc[d]=acc[d]*a+p*vs[k][d];
      m=mn;
    }
  }
  for(int d=0;d<16;d++) o[q*D+h*16+d]=f2bf(acc[d]/l);
}
__global__ void k_fin(const u16* x, const float* a, const float* b, u16* out){
  int i=blockIdx.x*256+threadIdx.x;
  out[i]=f2bf(bf2f(x[i])+a[i]+b[i]);
}
extern "C" void kernel_launch(void* const* d_in, const int* in_sizes, int n_in, void* d_out, int out_size, void* d_ws, size_t ws_size, hipStream_t stream){
  const u16* x=(const u16*)d_in[0]; const u16* wqkv=(const u16*)d_in[1]; const u16* wo=(const u16*)d_in[2];
  const u16* wg=(const u16*)d_in[4]; const u16* wu=(const u16*)d_in[5]; const u16* wd=(const u16*)d_in[6];
  char* ws=(char*)d_ws;
  u16* xn=(u16*)ws; ws+=S*D*2;
  float* qkv=(float*)ws; ws+=S*1536*4;
  float* g=(float*)ws; ws+=S*F*4;
  float* u=(float*)ws; ws+=S*F*4;
  u16* h=(u16*)ws; ws+=S*F*2;
  u16* o=(u16*)ws; ws+=S*D*2;
  float* ya=(float*)ws; ws+=S*D*4;
  float* yb=(float*)ws; ws+=S*D*4;
  k_ln<<<S,64,0,stream>>>(x,xn);
  k_gemm<<<dim3(1536/32,S/32),256,0,stream>>>(xn,wqkv,qkv,S,1536,D);
  k_gemm<<<dim3(F/32,S/32),256,0,stream>>>(xn,wg,g,S,F,D);
  k_gemm<<<dim3(F/32,S/32),256,0,stream>>>(xn,wu,u,S,F,D);
  k_glu<<<S*F/256,256,0,stream>>>(g,u,h);
  k_attn<<<dim3(S/256,H),256,0,stream>>>(qkv,o);
  k_gemm<<<dim3(D/32,S/32),256,0,stream>>>(o,wo,ya,S,D,D);
  k_gemm<<<dim3(D/32,S/32),256,0,stream>>>(h,wd,yb,S,D,F);
  k_fin<<<S*D/256,256,0,stream>>>(x,ya,yb,(u16*)d_out);
}
```

```cpp
#include <hip/hip_runtime.h>
#include <hip/hip_bf16.h>

__global__ void __launch_bounds__(512, 2)
fused_block_kernel(const void* x, const void* wqkv, const void* wo, const void* wg, const void* wu, const void* wd, void* out, void* ws) {
    __shared__ __attribute__((aligned(16))) char smem[163840];
    unsigned tid = threadIdx.x, wgid = blockIdx.x;
    unsigned long long ka = (unsigned long long)__builtin_amdgcn_kernarg_segment_ptr();
    unsigned lds0 = (unsigned)(unsigned long long)(__attribute__((address_space(3))) char*)smem;
    asm volatile(R"ASM(
.Lk_start:
s_load_dwordx16 s[4:19], s[0:1], 0x0
v_lshrrev_b32 v1, 6, v0
s_and_b32 s57, s2, 7
s_lshr_b32 s58, s2, 3
v_readfirstlane_b32 s52, v1
s_and_b32 s62, s57, 1
s_lshl_b32 s62, s62, 3
s_and_b32 s63, s58, 7
s_add_u32 s54, s62, s63
s_lshr_b32 s62, s57, 1
s_lshl_b32 s62, s62, 2
s_lshr_b32 s63, s58, 3
s_add_u32 s55, s62, s63
s_bfe_u32 s62, s57, 0x10001
s_lshl_b32 s62, s62, 3
s_and_b32 s63, s58, 7
s_add_u32 s54, s62, s63
s_and_b32 s62, s57, 5
s_lshl_b32 s55, s62, 1
s_lshr_b32 s63, s58, 4
s_lshl_b32 s63, s63, 2
s_add_u32 s55, s55, s63
s_bfe_u32 s63, s58, 0x10003
s_add_u32 s55, s55, s63
s_lshr_b32 s56, s62, 1
s_add_u32 s56, s56, s62
s_add_u32 s56, s56, s54
s_and_b32 s56, s56, 3
s_and_b32 s53, s52, 3
v_bfe_u32 v1, v0, 3, 3
v_bfe_u32 v2, v0, 4, 2
v_xor_b32 v2, v2, v0
v_and_b32 v2, 7, v2
v_lshlrev_b32 v1, 10, v1
v_lshl_add_u32 v225, v2, 4, v1
v_xor_b32 v226, 64, v225
s_lshl_b32 s98, s52, 14
s_lshl_b32 s97, s54, 2
s_add_u32 s97, s97, s53
s_lshl_b32 s64, s97, 15
s_lshr_b32 s63, s52, 1
s_and_b32 s63, s63, 2
s_add_u32 s63, s63, s56
s_add_u32 s62, s63, 0
s_and_b32 s62, s62, 3
s_lshl_b32 s62, s62, 8
s_add_u32 s84, s64, s62
s_add_u32 s62, s63, 1
s_and_b32 s62, s62, 3
s_lshl_b32 s62, s62, 8
s_add_u32 s85, s64, s62
s_mov_b32 s22, 2097152
s_mov_b32 s23, 0x20000
s_cmp_lt_u32 s52, 4
s_cbranch_scc1 .Lk_x_1
s_getpc_b64 s[64:65]
.Lk_pc:
s_sub_u32 s64, s64, .Lk_pc-.Lk_start
s_subb_u32 s65, s65, 0
s_and_b32 s65, s65, 0xffff
s_mov_b32 s66, .Lk_end-.Lk_start
s_mov_b32 s67, 0x20000
s_lshl_b32 s62, s53, 14
s_lshl_b32 s63, s58, 7
s_add_u32 s62, s62, s63
v_and_b32 v1, 3, v0
v_lshl_add_u32 v1, v1, 12, s62
s_mov_b64 exec, 15
buffer_load_dword v247, v1, s[64:67], 0 offen
s_mov_b64 exec, -1
.Lk_x_1:
v_and_b32 v224, 63, v0
v_mov_b32 v250, 0
v_mov_b32 v251, 0
v_mov_b32 v227, 0
v_mov_b32 v232, 0
v_mov_b32 v233, 0x3f803f80
v_and_b32 v1, 31, v0
v_bfe_u32 v2, v0, 1, 3
v_bfe_u32 v3, v0, 5, 1
v_xor_b32 v2, v2, v3
v_lshlrev_b32 v2, 4, v2
v_lshl_add_u32 v1, v1, 7, v2
v_add_u32 v136, s98, v1
v_xor_b32 v137, 32, v136
v_xor_b32 v138, 64, v136
v_xor_b32 v139, 96, v136
v_lshlrev_b32 v245, 2, v224
v_add_u32 v245, 131072, v245
s_lshl_b32 s62, s53, 9
s_add_u32 s62, s62, 132096
v_lshl_add_u32 v252, v224, 3, s62
v_lshrrev_b32 v1, 3, v224
v_mul_u32_u24 v222, 3072, v1
v_lshrrev_b32 v1, 2, v224
v_and_b32 v1, 4, v1
v_xor_b32 v1, v1, v224
v_and_b32 v1, 7, v1
v_lshl_add_u32 v222, v1, 4, v222
s_mov_b32 s26, 1572864
s_mov_b32 s27, 0x20000
s_lshl_b32 s63, s53, 5
s_lshl_b32 s64, s55, 7
s_add_u32 s62, s56, 0
s_and_b32 s62, s62, 3
s_lshl_b32 s62, s62, 7
s_add_u32 s62, s62, s63
s_mul_i32 s62, s62, 3072
s_add_u32 s88, s62, s64
s_add_u32 s62, s56, 1
s_and_b32 s62, s62, 3
s_lshl_b32 s62, s62, 7
s_add_u32 s62, s62, s63
s_mul_i32 s62, s62, 3072
s_add_u32 s89, s62, s64
s_add_u32 s62, s56, 2
s_and_b32 s62, s62, 3
s_lshl_b32 s62, s62, 7
s_add_u32 s62, s62, s63
s_mul_i32 s62, s62, 3072
s_add_u32 s90, s62, s64
s_add_u32 s62, s56, 3
s_and_b32 s62, s62, 3
s_lshl_b32 s62, s62, 7
s_add_u32 s62, s62, s63
s_mul_i32 s62, s62, 3072
s_add_u32 s91, s62, s64
s_lshl_b32 s96, s53, 14
v_mov_b32 v246, 1
s_waitcnt lgkmcnt(0)
s_mov_b32 s20, s4
s_and_b32 s21, s5, 0xffff
s_setprio 1
s_add_u32 m0, s98, 0
s_add_u32 s62, s84, 0
buffer_load_dwordx4 v225, s[20:23], s62 offen  lds
s_add_u32 m0, s98, 1024
s_add_u32 s62, s84, 8192
buffer_load_dwordx4 v226, s[20:23], s62 offen  lds
s_add_u32 m0, s98, 2048
s_add_u32 s62, s84, 16384
buffer_load_dwordx4 v225, s[20:23], s62 offen  lds
s_add_u32 m0, s98, 3072
s_add_u32 s62, s84, 24576
buffer_load_dwordx4 v226, s[20:23], s62 offen  lds
s_add_u32 m0, s98, 4096
s_add_u32 s62, s84, 128
buffer_load_dwordx4 v225, s[20:23], s62 offen  lds
s_add_u32 m0, s98, 5120
s_add_u32 s62, s84, 8320
buffer_load_dwordx4 v226, s[20:23], s62 offen  lds
s_add_u32 m0, s98, 6144
s_add_u32 s62, s84, 16512
buffer_load_dwordx4 v225, s[20:23], s62 offen  lds
s_add_u32 m0, s98, 7168
s_add_u32 s62, s84, 24704
buffer_load_dwordx4 v226, s[20:23], s62 offen  lds
s_add_u32 m0, s98, 8192
s_add_u32 s62, s85, 0
buffer_load_dwordx4 v225, s[20:23], s62 offen  lds
s_add_u32 m0, s98, 9216
s_add_u32 s62, s85, 8192
buffer_load_dwordx4 v226, s[20:23], s62 offen  lds
s_add_u32 m0, s98, 10240
s_add_u32 s62, s85, 16384
buffer_load_dwordx4 v225, s[20:23], s62 offen  lds
s_add_u32 m0, s98, 11264
s_add_u32 s62, s85, 24576
buffer_load_dwordx4 v226, s[20:23], s62 offen  lds
s_add_u32 m0, s98, 12288
s_add_u32 s62, s85, 128
buffer_load_dwordx4 v225, s[20:23], s62 offen  lds
s_add_u32 m0, s98, 13312
s_add_u32 s62, s85, 8320
buffer_load_dwordx4 v226, s[20:23], s62 offen  lds
s_add_u32 m0, s98, 14336
s_add_u32 s62, s85, 16512
buffer_load_dwordx4 v225, s[20:23], s62 offen  lds
s_add_u32 m0, s98, 15360
s_add_u32 s62, s85, 24704
buffer_load_dwordx4 v226, s[20:23], s62 offen  lds
s_mov_b32 s24, s6
s_and_b32 s25, s7, 0xffff
s_cmp_ge_u32 s52, 4
s_cbranch_scc1 .Lk_att
v_bfe_u32 v128, v224, 2, 2
v_lshrrev_b32 v129, 5, v224
v_lshl_add_u32 v130, v129, 3, v128
v_bfe_u32 v131, v224, 4, 1
v_and_b32 v134, 3, v224
v_lshlrev_b32 v134, 3, v134
v_lshl_add_u32 v132, v131, 5, v134
v_bfe_u32 v133, v224, 3, 1
v_lshl_add_u32 v133, v133, 6, v132
v_lshl_add_u32 v234, v130, 7, v133
v_xor_b32 v235, 64, v234
v_add_u32 v236, 0x10000, v234
v_add_u32 v237, 0x10000, v235
v_lshl_add_u32 v238, v130, 6, v132
)ASM" R"ASM(
v_add_u32 v238, 0x10000, v238
v_mul_u32_u24 v239, 4224, v131
v_lshl_add_u32 v239, v130, 5, v239
v_add_u32 v239, v134, v239
v_and_b32 v243, 0x33, v224
v_bfe_u32 v128, v224, 2, 1
v_bfe_u32 v129, v224, 3, 1
v_lshl_add_u32 v243, v128, 3, v243
v_lshl_add_u32 v243, v129, 2, v243
v_lshrrev_b32 v130, 5, v224
v_bfe_u32 v134, v224, 4, 1
v_xor_b32 v130, v130, v134
v_lshl_add_u32 v133, v128, 1, v129
v_lshl_add_u32 v133, v130, 2, v133
v_and_b32 v131, 3, v224
v_cmp_ne_u32 vcc, 3, v131
v_mov_b32 v132, 0x3f80
v_lshlrev_b32 v221, 4, v224
v_cndmask_b32 v132, 1.0, v132, vcc
v_cndmask_b32 v131, 0, v131, vcc
v_or_b32 v131, 12, v131
v_mul_u32_u24 v220, 0x1111, v131
v_cmp_eq_u32 vcc, 0, v133
s_nop 1
v_cndmask_b32 v216, 0, v132, vcc
v_cmp_eq_u32 vcc, 1, v133
s_nop 1
v_cndmask_b32 v217, 0, v132, vcc
v_cmp_eq_u32 vcc, 2, v133
s_nop 1
v_cndmask_b32 v218, 0, v132, vcc
v_cmp_eq_u32 vcc, 3, v133
s_nop 1
v_cndmask_b32 v219, 0, v132, vcc
s_lshl_b32 s62, s53, 10
s_add_u32 s62, s62, 135168
v_add_u32 v221, s62, v221
v_lshlrev_b32 v243, 4, v243
s_cmp_lt_u32 s55, 8
s_cbranch_scc1 .Lk_k_2
v_and_b32 v243, 15, v224
v_lshrrev_b32 v129, 5, v224
v_lshlrev_b32 v243, 4, v243
v_lshl_add_u32 v243, v134, 17, v243
v_lshl_add_u32 v243, v129, 8, v243
.Lk_k_2:
v_add_u32 v140, 0x10000, v136
v_add_u32 v141, 0x10000, v137
v_add_u32 v142, 0x10000, v138
v_add_u32 v143, 0x10000, v139
s_waitcnt vmcnt(12)
ds_read_b128 v[0:3], v136 offset:0
ds_read_b128 v[4:7], v137 offset:0
ds_read_b128 v[8:11], v138 offset:0
ds_read_b128 v[12:15], v139 offset:0
s_waitcnt vmcnt(8)
ds_read_b128 v[16:19], v136 offset:4096
ds_read_b128 v[20:23], v137 offset:4096
ds_read_b128 v[24:27], v138 offset:4096
ds_read_b128 v[28:31], v139 offset:4096
s_waitcnt lgkmcnt(4)
v_dot2c_f32_bf16 v250, v233, v0
v_dot2c_f32_bf16 v227, v0, v0
v_dot2c_f32_bf16 v251, v233, v1
v_dot2c_f32_bf16 v232, v1, v1
v_dot2c_f32_bf16 v250, v233, v2
v_dot2c_f32_bf16 v227, v2, v2
v_dot2c_f32_bf16 v251, v233, v3
v_dot2c_f32_bf16 v232, v3, v3
v_dot2c_f32_bf16 v250, v233, v4
v_dot2c_f32_bf16 v227, v4, v4
v_dot2c_f32_bf16 v251, v233, v5
v_dot2c_f32_bf16 v232, v5, v5
v_dot2c_f32_bf16 v250, v233, v6
v_dot2c_f32_bf16 v227, v6, v6
v_dot2c_f32_bf16 v251, v233, v7
v_dot2c_f32_bf16 v232, v7, v7
v_dot2c_f32_bf16 v250, v233, v8
v_dot2c_f32_bf16 v227, v8, v8
v_dot2c_f32_bf16 v251, v233, v9
v_dot2c_f32_bf16 v232, v9, v9
v_dot2c_f32_bf16 v250, v233, v10
v_dot2c_f32_bf16 v227, v10, v10
v_dot2c_f32_bf16 v251, v233, v11
v_dot2c_f32_bf16 v232, v11, v11
v_dot2c_f32_bf16 v250, v233, v12
v_dot2c_f32_bf16 v227, v12, v12
v_dot2c_f32_bf16 v251, v233, v13
v_dot2c_f32_bf16 v232, v13, v13
v_dot2c_f32_bf16 v250, v233, v14
v_dot2c_f32_bf16 v227, v14, v14
v_dot2c_f32_bf16 v251, v233, v15
v_dot2c_f32_bf16 v232, v15, v15
s_waitcnt vmcnt(4)
ds_read_b128 v[32:35], v136 offset:8192
ds_read_b128 v[36:39], v137 offset:8192
ds_read_b128 v[40:43], v138 offset:8192
ds_read_b128 v[44:47], v139 offset:8192
s_waitcnt lgkmcnt(4)
v_dot2c_f32_bf16 v250, v233, v16
v_dot2c_f32_bf16 v227, v16, v16
v_dot2c_f32_bf16 v251, v233, v17
v_dot2c_f32_bf16 v232, v17, v17
v_dot2c_f32_bf16 v250, v233, v18
v_dot2c_f32_bf16 v227, v18, v18
v_dot2c_f32_bf16 v251, v233, v19
v_dot2c_f32_bf16 v232, v19, v19
v_dot2c_f32_bf16 v250, v233, v20
v_dot2c_f32_bf16 v227, v20, v20
v_dot2c_f32_bf16 v251, v233, v21
v_dot2c_f32_bf16 v232, v21, v21
v_dot2c_f32_bf16 v250, v233, v22
v_dot2c_f32_bf16 v227, v22, v22
v_dot2c_f32_bf16 v251, v233, v23
v_dot2c_f32_bf16 v232, v23, v23
v_dot2c_f32_bf16 v250, v233, v24
v_dot2c_f32_bf16 v227, v24, v24
v_dot2c_f32_bf16 v251, v233, v25
v_dot2c_f32_bf16 v232, v25, v25
v_dot2c_f32_bf16 v250, v233, v26
v_dot2c_f32_bf16 v227, v26, v26
v_dot2c_f32_bf16 v251, v233, v27
v_dot2c_f32_bf16 v232, v27, v27
v_dot2c_f32_bf16 v250, v233, v28
v_dot2c_f32_bf16 v227, v28, v28
v_dot2c_f32_bf16 v251, v233, v29
v_dot2c_f32_bf16 v232, v29, v29
v_dot2c_f32_bf16 v250, v233, v30
v_dot2c_f32_bf16 v227, v30, v30
v_dot2c_f32_bf16 v251, v233, v31
v_dot2c_f32_bf16 v232, v31, v31
s_waitcnt vmcnt(0)
ds_read_b128 v[48:51], v136 offset:12288
ds_read_b128 v[52:55], v137 offset:12288
ds_read_b128 v[56:59], v138 offset:12288
ds_read_b128 v[60:63], v139 offset:12288
s_waitcnt lgkmcnt(4)
v_dot2c_f32_bf16 v250, v233, v32
v_dot2c_f32_bf16 v227, v32, v32
v_dot2c_f32_bf16 v251, v233, v33
v_dot2c_f32_bf16 v232, v33, v33
v_dot2c_f32_bf16 v250, v233, v34
v_dot2c_f32_bf16 v227, v34, v34
v_dot2c_f32_bf16 v251, v233, v35
v_dot2c_f32_bf16 v232, v35, v35
v_dot2c_f32_bf16 v250, v233, v36
v_dot2c_f32_bf16 v227, v36, v36
v_dot2c_f32_bf16 v251, v233, v37
v_dot2c_f32_bf16 v232, v37, v37
v_dot2c_f32_bf16 v250, v233, v38
v_dot2c_f32_bf16 v227, v38, v38
v_dot2c_f32_bf16 v251, v233, v39
v_dot2c_f32_bf16 v232, v39, v39
v_dot2c_f32_bf16 v250, v233, v40
v_dot2c_f32_bf16 v227, v40, v40
v_dot2c_f32_bf16 v251, v233, v41
v_dot2c_f32_bf16 v232, v41, v41
v_dot2c_f32_bf16 v250, v233, v42
v_dot2c_f32_bf16 v227, v42, v42
v_dot2c_f32_bf16 v251, v233, v43
v_dot2c_f32_bf16 v232, v43, v43
v_dot2c_f32_bf16 v250, v233, v44
v_dot2c_f32_bf16 v227, v44, v44
v_dot2c_f32_bf16 v251, v233, v45
v_dot2c_f32_bf16 v232, v45, v45
v_dot2c_f32_bf16 v250, v233, v46
v_dot2c_f32_bf16 v227, v46, v46
v_dot2c_f32_bf16 v251, v233, v47
v_dot2c_f32_bf16 v232, v47, v47
s_waitcnt lgkmcnt(0)
v_dot2c_f32_bf16 v250, v233, v48
v_dot2c_f32_bf16 v227, v48, v48
v_dot2c_f32_bf16 v251, v233, v49
v_dot2c_f32_bf16 v232, v49, v49
v_dot2c_f32_bf16 v250, v233, v50
v_dot2c_f32_bf16 v227, v50, v50
v_dot2c_f32_bf16 v251, v233, v51
v_dot2c_f32_bf16 v232, v51, v51
v_dot2c_f32_bf16 v250, v233, v52
v_dot2c_f32_bf16 v227, v52, v52
v_dot2c_f32_bf16 v251, v233, v53
v_dot2c_f32_bf16 v232, v53, v53
v_dot2c_f32_bf16 v250, v233, v54
v_dot2c_f32_bf16 v227, v54, v54
v_dot2c_f32_bf16 v251, v233, v55
v_dot2c_f32_bf16 v232, v55, v55
v_dot2c_f32_bf16 v250, v233, v56
v_dot2c_f32_bf16 v227, v56, v56
v_dot2c_f32_bf16 v251, v233, v57
v_dot2c_f32_bf16 v232, v57, v57
v_dot2c_f32_bf16 v250, v233, v58
v_dot2c_f32_bf16 v227, v58, v58
v_dot2c_f32_bf16 v251, v233, v59
v_dot2c_f32_bf16 v232, v59, v59
v_dot2c_f32_bf16 v250, v233, v60
v_dot2c_f32_bf16 v227, v60, v60
v_dot2c_f32_bf16 v251, v233, v61
v_dot2c_f32_bf16 v232, v61, v61
v_dot2c_f32_bf16 v250, v233, v62
v_dot2c_f32_bf16 v227, v62, v62
)ASM" R"ASM(
v_dot2c_f32_bf16 v251, v233, v63
v_dot2c_f32_bf16 v232, v63, v63
s_nop 1
v_add_f32 v250, v250, v251
v_add_f32 v227, v227, v232
v_mov_b32 v251, v250
v_mov_b32 v232, v227
s_nop 0
v_permlane32_swap_b32 v250, v251
v_permlane32_swap_b32 v227, v232
v_add_f32 v250, v250, v251
v_add_f32 v251, v227, v232
s_setprio 0
s_add_u32 m0, s96, 0
s_add_u32 s62, s88, 1024
buffer_load_dwordx4 v222, s[24:27], s62 offen  lds
s_add_u32 m0, s96, 1024
s_add_u32 s62, s88, 25600
buffer_load_dwordx4 v222, s[24:27], s62 offen  lds
s_add_u32 m0, s96, 2048
s_add_u32 s62, s88, 50176
buffer_load_dwordx4 v222, s[24:27], s62 offen  lds
s_add_u32 m0, s96, 3072
s_add_u32 s62, s88, 74752
buffer_load_dwordx4 v222, s[24:27], s62 offen  lds
s_mov_b32 s48, s18
s_and_b32 s49, s19, 0xffff
s_mov_b32 s50, 14680064
s_mov_b32 s51, 0x20000
s_mov_b32 s28, s10
s_and_b32 s29, s11, 0xffff
s_mov_b32 s30, 1835008
s_mov_b32 s31, 0x20000
s_add_u32 m0, s96, 4096
s_add_u32 s62, s89, 1024
buffer_load_dwordx4 v222, s[24:27], s62 offen  lds
s_add_u32 m0, s96, 5120
s_add_u32 s62, s89, 25600
buffer_load_dwordx4 v222, s[24:27], s62 offen  lds
s_add_u32 m0, s96, 6144
s_add_u32 s62, s89, 50176
buffer_load_dwordx4 v222, s[24:27], s62 offen  lds
s_add_u32 m0, s96, 7168
s_add_u32 s62, s89, 74752
buffer_load_dwordx4 v222, s[24:27], s62 offen  lds
s_mov_b32 s32, s12
s_and_b32 s33, s13, 0xffff
s_mov_b32 s34, 1835008
s_mov_b32 s35, 0x20000
s_mov_b32 s36, s14
s_and_b32 s37, s15, 0xffff
s_mov_b32 s38, 1835008
s_mov_b32 s39, 0x20000
s_mov_b32 s40, s8
s_and_b32 s41, s9, 0xffff
s_mov_b32 s42, 524288
s_mov_b32 s43, 0x20000
s_mov_b32 s44, s16
s_and_b32 s45, s17, 0xffff
s_mov_b32 s46, 2097152
s_mov_b32 s47, 0x20000
s_lshl_b32 s62, s55, 6
s_add_u32 s62, s62, s97
s_lshl_b32 s62, s62, 5
s_add_u32 s15, s62, 131072
s_and_b32 s62, s55, 7
s_lshl_b32 s62, s62, 19
s_lshl_b32 s63, s97, 11
s_add_u32 s62, s62, s63
s_add_u32 s99, s62, 1048576
s_lshr_b32 s64, s55, 2
s_mul_i32 s64, s64, 7
s_and_b32 s65, s55, 3
s_lshl_b32 s66, s65, 1
s_add_u32 s66, s66, s64
s_lshl_b32 s62, s53, 5
s_lshl_b32 s63, s66, 7
s_add_u32 s88, s56, 0
s_and_b32 s88, s88, 3
s_lshl_b32 s88, s88, 7
s_add_u32 s88, s88, s62
s_mul_i32 s88, s88, 3584
s_add_u32 s92, s88, s63
s_add_u32 s89, s56, 1
s_and_b32 s89, s89, 3
s_lshl_b32 s89, s89, 7
s_add_u32 s89, s89, s62
s_mul_i32 s89, s89, 3584
s_add_u32 s93, s89, s63
s_add_u32 s90, s56, 2
s_and_b32 s90, s90, 3
s_lshl_b32 s90, s90, 7
s_add_u32 s90, s90, s62
s_mul_i32 s90, s90, 3584
s_add_u32 s94, s90, s63
s_add_u32 s91, s56, 3
s_and_b32 s91, s91, 3
s_lshl_b32 s91, s91, 7
s_add_u32 s91, s91, s62
s_mul_i32 s91, s91, 3584
s_add_u32 s95, s91, s63
s_mul_i32 s67, s97, 147456
s_add_u32 s67, s67, 5242880
s_lshl_b32 s66, s66, 12
s_add_u32 s7, s67, s66
s_add_u32 s19, s7, 4096
s_lshl_b32 s65, s65, 1
s_add_u32 s65, s65, 4
s_lshl_b32 s64, s64, 2
s_add_u32 s64, s64, 4
s_add_u32 s62, s65, 0
s_and_b32 s63, s62, 12
s_add_u32 s62, s62, s63
s_add_u32 s62, s62, s64
s_lshl_b32 s4, s62, 5
s_lshl_b32 s62, s62, 10
s_add_u32 s8, s67, s62
s_add_u32 s62, s65, 1
s_and_b32 s63, s62, 12
s_add_u32 s62, s62, s63
s_add_u32 s62, s62, s64
s_lshl_b32 s5, s62, 5
s_lshl_b32 s62, s62, 10
s_add_u32 s9, s67, s62
s_add_u32 s62, s65, 2
s_and_b32 s63, s62, 12
s_add_u32 s62, s62, s63
s_add_u32 s62, s62, s64
s_lshl_b32 s6, s62, 5
s_lshl_b32 s62, s62, 10
s_add_u32 s10, s67, s62
s_add_u32 s5, s4, 57344
s_lshl_b32 s96, s53, 14
s_lshr_b32 s62, s58, 2
s_lshl_b32 s62, s62, 7
s_mul_i32 s11, s53, 458752
s_add_u32 s11, s11, s62
s_lshl_b32 s12, s53, 17
s_add_u32 s12, s12, s62
s_lshl_b32 s65, s57, 2
s_and_b32 s62, s58, 3
s_add_u32 s65, s65, s62
s_mul_i32 s63, s65, 294912
s_add_u32 s63, s63, 5242880
s_mul_i32 s13, s53, 28672
s_add_u32 s13, s13, s63
s_lshl_b32 s14, s53, 13
s_add_u32 s14, s14, s63
s_add_u32 s14, s14, 114688
s_mov_b32 s17, s65
s_lshl_b32 s62, s54, 4
s_add_u32 s62, s62, s55
s_lshl_b32 s62, s62, 7
s_lshl_b32 s63, s53, 4
s_add_u32 s62, s62, s63
s_add_u32 s16, s62, 393216
v_lshrrev_b32 v248, 3, v224
v_mul_u32_u24 v240, 3584, v248
v_lshrrev_b32 v248, 2, v224
v_and_b32 v248, 4, v248
v_xor_b32 v248, v248, v224
v_and_b32 v248, 7, v248
v_lshl_add_u32 v240, v248, 4, v240
v_bfe_u32 v242, v224, 3, 2
v_lshrrev_b32 v248, 5, v224
v_lshl_add_u32 v248, v248, 2, v242
v_mul_u32_u24 v242, 1024, v248
v_lshrrev_b32 v248, 3, v224
v_and_b32 v248, 6, v248
v_xor_b32 v248, v248, v224
v_and_b32 v248, 7, v248
v_lshl_add_u32 v242, v248, 4, v242
v_lshrrev_b32 v248, 2, v224
v_mul_u32_u24 v241, 3584, v248
v_and_b32 v248, 3, v224
v_lshl_add_u32 v241, v248, 4, v241
v_lshlrev_b32 v244, 4, v224
s_lshl_b32 s62, s57, 2
s_and_b32 s63, s58, 3
s_add_u32 s62, s62, s63
s_lshl_b32 s62, s62, 3
s_add_u32 s62, s62, s52
s_lshl_b32 s62, s62, 13
s_lshr_b32 s63, s58, 2
s_lshl_b32 s63, s63, 7
s_add_u32 s59, s62, s63
v_lshrrev_b32 v248, 3, v224
v_and_b32 v249, 7, v224
v_lshlrev_b32 v253, 10, v248
v_lshl_add_u32 v253, v249, 4, v253
v_lshlrev_b32 v254, 1, v249
v_xor_b32 v254, v254, v248
v_lshlrev_b32 v254, 4, v254
v_lshl_add_u32 v254, v248, 8, v254
s_lshr_b32 s62, s52, 2
s_lshl_b32 s62, s62, 13
s_lshl_b32 s63, s53, 11
s_add_u32 s62, s62, s63
v_add_u32 v254, s62, v254
v_xor_b32 v255, 16, v254
)ASM" R"ASM(
v_bfe_u32 v248, v224, 2, 1
v_and_b32 v225, 3, v224
v_lshlrev_b32 v248, 4, v248
s_lshr_b32 s62, s17, 1
s_lshl_b32 s62, s62, 2
s_add_u32 s62, s62, s53
s_lshl_b32 s62, s62, 9
s_and_b32 s63, s17, 1
s_lshl_b32 s63, s63, 5
s_add_u32 s62, s62, s63
s_add_u32 s62, s62, 393216
v_lshl_add_u32 v225, v225, 7, v248
v_add_u32 v225, s62, v225
s_lshl_b32 s62, s17, 2
s_add_u32 s62, s62, s53
s_lshl_b32 s62, s62, 7
s_add_u32 s62, s62, 262144
v_and_b32 v226, 7, v224
v_lshl_add_u32 v226, v226, 4, s62
s_lshr_b32 s62, s55, 2
s_lshl_b32 s63, s55, 1
s_sub_u32 s62, s63, s62
s_lshl_b32 s62, s62, 7
s_mul_i32 s18, s54, 114688
s_add_u32 s18, s18, s62
s_mov_b64 s[24:25], s[28:29]
s_mov_b64 s[26:27], s[30:31]
s_mov_b64 s[60:61], -1
s_movk_i32 s62, 3584
s_movk_i32 s63, 128
s_mul_i32 s64, s2, 7168
s_lshl_b32 s65, s2, 11
s_cmp_eq_u32 s53, 2
s_cselect_b64 s[24:25], s[32:33], s[24:25]
s_mov_b32 s18, s64
s_movk_i32 s62, 128
s_movk_i32 s63, 4096
s_mov_b32 s61, 0
s_mov_b32 s60, 1
s_cmp_eq_u32 s53, 3
s_cselect_b64 s[24:25], s[36:37], s[24:25]
s_cselect_b32 s26, s38, s26
s_cselect_b32 s18, s64, s18
s_cselect_b32 s62, 128, s62
s_cselect_b32 s63, 4096, s63
s_cselect_b32 s60, -1, s60
s_cselect_b32 s61, 16777215, s61
s_cmp_eq_u32 s53, 0
s_cselect_b64 s[24:25], s[40:41], s[24:25]
s_cselect_b32 s26, s42, s26
s_cselect_b32 s18, s65, s18
s_cselect_b32 s62, 128, s62
s_cselect_b32 s63, 4096, s63
s_cselect_b32 s60, 65535, s60
s_cselect_b32 s61, 0, s61
v_and_b32 v248, 31, v224
v_lshrrev_b32 v249, 5, v224
v_mul_u32_u24 v247, s62, v248
v_mad_u32_u24 v247, v249, s63, v247
s_mov_b32 s64, 0xffff0000
s_cmp_ge_u32 s55, 8
s_cbranch_scc1 .Lk_vproj
v_mov_b32 v124, v140
v_mov_b32 v125, v141
v_mov_b32 v126, v142
v_mov_b32 v127, v143
s_waitcnt vmcnt(4)
s_barrier
ds_read_b64 v[232:233], v252
ds_read_b64_tr_b16 v[192:193], v234 offset:0
ds_read_b64_tr_b16 v[194:195], v234 offset:512
ds_read_b64_tr_b16 v[196:197], v235 offset:0
ds_read_b64_tr_b16 v[198:199], v235 offset:512
ds_read_b64_tr_b16 v[200:201], v234 offset:2048
ds_read_b64_tr_b16 v[202:203], v234 offset:2560
ds_read_b64_tr_b16 v[204:205], v235 offset:2048
ds_read_b64_tr_b16 v[206:207], v235 offset:2560
s_waitcnt lgkmcnt(8)
v_add_f32 v250, v250, v232
v_add_f32 v227, v251, v233
v_mul_f32 v250, 0xbb000000, v250
v_cvt_pk_bf16_f32 v251, v250, v250
v_and_b32 v160, s64, v251
v_and_b32 v161, s64, v251
v_and_b32 v162, s64, v251
v_and_b32 v163, s64, v251
ds_write_b128 v221, v[160:163]
v_and_b32 v164, s64, v251
v_and_b32 v165, s64, v251
v_and_b32 v166, s64, v251
v_and_b32 v167, s64, v251
v_and_b32 v168, s64, v251
v_and_b32 v169, s64, v251
v_and_b32 v170, s64, v251
v_and_b32 v171, s64, v251
v_and_b32 v172, s64, v251
v_and_b32 v173, s64, v251
v_and_b32 v174, s64, v251
v_and_b32 v175, s64, v251
ds_read_b128 v[176:179], v221
ds_read_b128 v[180:183], v221
ds_read_b128 v[184:187], v221
ds_read_b128 v[188:191], v221
v_smfmac_f32_32x32x32_bf16 v[160:175], v[216:219], v[0:7], v220
v_mul_f32 v227, 0x3b000000, v227
v_fma_f32 v227, -v250, v250, v227
v_max_f32 v227, 0, v227
v_cvt_pk_bf16_f32 v227, v227, v227
v_and_b32 v227, s64, v227
v_add_f32 v227, 0x3727c5ac, v227
v_cvt_pk_bf16_f32 v227, v227, v227
v_and_b32 v227, s64, v227
v_sqrt_f32 v227, v227
s_nop 0
v_cvt_pk_bf16_f32 v230, v227, v227
v_and_b32 v230, s64, v230
v_rcp_f32 v228, v230
v_and_b32 v250, s64, v251
v_and_b32 v251, s64, v251
v_cvt_pk_bf16_f32 v0, v160, v161
v_cvt_pk_bf16_f32 v1, v162, v163
v_cvt_pk_bf16_f32 v2, v164, v165
v_cvt_pk_bf16_f32 v3, v166, v167
v_cvt_pk_bf16_f32 v4, v168, v169
v_cvt_pk_bf16_f32 v5, v170, v171
v_cvt_pk_bf16_f32 v6, v172, v173
v_cvt_pk_bf16_f32 v7, v174, v175
s_waitcnt lgkmcnt(0)
v_smfmac_f32_32x32x32_bf16 v[176:191], v[216:219], v[8:15], v220
ds_read_b128 v[160:163], v221
ds_read_b128 v[164:167], v221
v_mov_b32 v168, v250
v_mov_b32 v169, v250
v_mov_b32 v170, v250
v_mfma_f32_32x32x16_bf16 v[128:143], v[192:195], v[0:3], 0
ds_read_b64_tr_b16 v[208:209], v234 offset:16384
ds_read_b64_tr_b16 v[210:211], v234 offset:16896
v_mov_b32 v171, v250
v_mov_b32 v172, v250
v_mov_b32 v173, v250
ds_read_b128 v[64:67], v124 offset:0
ds_read_b128 v[68:71], v125 offset:0
ds_read_b128 v[72:75], v126 offset:0
v_mfma_f32_32x32x16_bf16 v[144:159], v[196:199], v[0:3], 0
ds_read_b64_tr_b16 v[212:213], v235 offset:16384
ds_read_b64_tr_b16 v[214:215], v235 offset:16896
v_mov_b32 v174, v250
v_mov_b32 v175, v250
ds_read_b128 v[76:79], v127 offset:0
ds_read_b128 v[80:83], v124 offset:4096
ds_read_b128 v[84:87], v125 offset:4096
v_mfma_f32_32x32x16_bf16 v[128:143], v[200:203], v[4:7], v[128:143]
ds_read_b64_tr_b16 v[192:193], v234 offset:18432
ds_read_b64_tr_b16 v[194:195], v234 offset:18944
v_cvt_pk_bf16_f32 v8, v176, v177
v_cvt_pk_bf16_f32 v9, v178, v179
v_cvt_pk_bf16_f32 v10, v180, v181
v_cvt_pk_bf16_f32 v11, v182, v183
v_mfma_f32_32x32x16_bf16 v[144:159], v[204:207], v[4:7], v[144:159]
ds_read_b64_tr_b16 v[196:197], v235 offset:18432
s_waitcnt lgkmcnt(13)
ds_read_b64_tr_b16 v[198:199], v235 offset:18944
v_cvt_pk_bf16_f32 v12, v184, v185
v_cvt_pk_bf16_f32 v13, v186, v187
v_cvt_pk_bf16_f32 v14, v188, v189
v_cvt_pk_bf16_f32 v15, v190, v191
v_smfmac_f32_32x32x32_bf16 v[160:175], v[216:219], v[16:23], v220
s_waitcnt lgkmcnt(7)
ds_read_b128 v[176:179], v221
ds_read_b128 v[180:183], v221
v_mov_b32 v184, v250
v_mov_b32 v185, v250
v_mov_b32 v186, v250
v_mfma_f32_32x32x16_bf16 v[128:143], v[208:211], v[8:11], v[128:143]
ds_read_b64_tr_b16 v[200:201], v234 offset:32768
ds_read_b64_tr_b16 v[202:203], v234 offset:33280
v_mov_b32 v187, v250
v_mov_b32 v188, v250
v_mov_b32 v189, v250
ds_read_b128 v[88:91], v126 offset:4096
ds_read_b128 v[92:95], v127 offset:4096
ds_read_b128 v[96:99], v124 offset:8192
v_mfma_f32_32x32x16_bf16 v[144:159], v[212:215], v[8:11], v[144:159]
ds_read_b64_tr_b16 v[204:205], v235 offset:32768
s_waitcnt lgkmcnt(13)
ds_read_b64_tr_b16 v[206:207], v235 offset:33280
v_mov_b32 v190, v250
v_mov_b32 v191, v250
ds_read_b128 v[100:103], v125 offset:8192
s_waitcnt lgkmcnt(13)
ds_read_b128 v[104:107], v126 offset:8192
ds_read_b128 v[108:111], v127 offset:8192
s_waitcnt lgkmcnt(12)
v_mfma_f32_32x32x16_bf16 v[128:143], v[192:195], v[12:15], v[128:143]
ds_read_b64_tr_b16 v[208:209], v234 offset:34816
ds_read_b64_tr_b16 v[210:211], v234 offset:35328
v_cvt_pk_bf16_f32 v16, v160, v161
v_cvt_pk_bf16_f32 v17, v162, v163
v_cvt_pk_bf16_f32 v18, v164, v165
v_cvt_pk_bf16_f32 v19, v166, v167
)ASM" R"ASM(
v_mfma_f32_32x32x16_bf16 v[144:159], v[196:199], v[12:15], v[144:159]
s_waitcnt vmcnt(0)
ds_add_u32 v245, v246 offset:256
s_waitcnt lgkmcnt(13)
ds_read_b64_tr_b16 v[212:213], v235 offset:34816
ds_read_b64_tr_b16 v[214:215], v235 offset:35328
v_cvt_pk_bf16_f32 v20, v168, v169
v_cvt_pk_bf16_f32 v21, v170, v171
v_cvt_pk_bf16_f32 v22, v172, v173
v_cvt_pk_bf16_f32 v23, v174, v175
v_smfmac_f32_32x32x32_bf16 v[176:191], v[216:219], v[24:31], v220
s_waitcnt lgkmcnt(8)
ds_read_b128 v[160:163], v221
ds_read_b128 v[164:167], v221
v_mov_b32 v168, v250
v_mov_b32 v169, v250
v_mov_b32 v170, v250
v_mfma_f32_32x32x16_bf16 v[128:143], v[200:203], v[16:19], v[128:143]
ds_read_b64_tr_b16 v[192:193], v234 offset:49152
ds_read_b64_tr_b16 v[194:195], v234 offset:49664
v_mov_b32 v171, v250
v_mov_b32 v172, v250
v_mov_b32 v173, v250
ds_read_b128 v[112:115], v124 offset:12288
ds_read_b128 v[116:119], v125 offset:12288
ds_read_b128 v[120:123], v126 offset:12288
v_mfma_f32_32x32x16_bf16 v[144:159], v[204:207], v[16:19], v[144:159]
s_waitcnt lgkmcnt(13)
ds_read_b64_tr_b16 v[196:197], v235 offset:49152
ds_read_b64_tr_b16 v[198:199], v235 offset:49664
v_mov_b32 v174, v250
v_mov_b32 v175, v250
s_waitcnt lgkmcnt(13)
ds_read_b128 v[124:127], v127 offset:12288
ds_add_u32 v245, v246 offset:512
v_mov_b32 v231, v230
s_waitcnt lgkmcnt(11)
v_mfma_f32_32x32x16_bf16 v[128:143], v[208:211], v[20:23], v[128:143]
ds_read_b32 v252, v245 offset:256
ds_read_b64_tr_b16 v[200:201], v234 offset:51200
ds_read_b64_tr_b16 v[202:203], v234 offset:51712
v_cvt_pk_bf16_f32 v24, v176, v177
v_cvt_pk_bf16_f32 v25, v178, v179
v_cvt_pk_bf16_f32 v26, v180, v181
v_cvt_pk_bf16_f32 v27, v182, v183
v_mfma_f32_32x32x16_bf16 v[144:159], v[212:215], v[20:23], v[144:159]
ds_read_b64_tr_b16 v[204:205], v235 offset:51200
s_waitcnt lgkmcnt(13)
ds_read_b64_tr_b16 v[206:207], v235 offset:51712
v_cvt_pk_bf16_f32 v28, v184, v185
v_cvt_pk_bf16_f32 v29, v186, v187
v_cvt_pk_bf16_f32 v30, v188, v189
v_cvt_pk_bf16_f32 v31, v190, v191
v_smfmac_f32_32x32x32_bf16 v[160:175], v[216:219], v[32:39], v220
s_waitcnt lgkmcnt(7)
ds_read_b128 v[176:179], v221
ds_read_b128 v[180:183], v221
v_mov_b32 v184, v250
v_mov_b32 v185, v250
v_mov_b32 v186, v250
v_mfma_f32_32x32x16_bf16 v[128:143], v[192:195], v[24:27], v[128:143]
s_waitcnt lgkmcnt(6)
v_cmp_gt_u32 vcc, 4, v252
s_nop 0
s_nop 0
s_cbranch_vccnz .Lk_ls_3
.Lk_ld_4:
ds_read_b64_tr_b16 v[208:209], v234 offset:4096
ds_read_b64_tr_b16 v[210:211], v234 offset:4608
v_mov_b32 v187, v250
v_mov_b32 v188, v250
v_mov_b32 v189, v250
v_mov_b32 v229, v228
v_mul_f32 v232, 0xbfb8aa3b, v228
v_mul_f32 v233, 0xbfb8aa3b, v228
v_mfma_f32_32x32x16_bf16 v[144:159], v[196:199], v[24:27], v[144:159]
ds_read_b64_tr_b16 v[212:213], v235 offset:4096
ds_read_b64_tr_b16 v[214:215], v235 offset:4608
v_mov_b32 v190, v250
v_mov_b32 v191, v250
s_waitcnt lgkmcnt(6)
v_mfma_f32_32x32x16_bf16 v[128:143], v[200:203], v[28:31], v[128:143]
ds_read_b64_tr_b16 v[192:193], v234 offset:6144
ds_read_b64_tr_b16 v[194:195], v234 offset:6656
v_cvt_pk_bf16_f32 v32, v160, v161
v_cvt_pk_bf16_f32 v33, v162, v163
v_cvt_pk_bf16_f32 v34, v164, v165
v_cvt_pk_bf16_f32 v35, v166, v167
v_mfma_f32_32x32x16_bf16 v[144:159], v[204:207], v[28:31], v[144:159]
ds_read_b64_tr_b16 v[196:197], v235 offset:6144
ds_read_b64_tr_b16 v[198:199], v235 offset:6656
v_cvt_pk_bf16_f32 v36, v168, v169
v_cvt_pk_bf16_f32 v37, v170, v171
v_cvt_pk_bf16_f32 v38, v172, v173
v_cvt_pk_bf16_f32 v39, v174, v175
s_waitcnt lgkmcnt(8)
v_smfmac_f32_32x32x32_bf16 v[176:191], v[216:219], v[40:47], v220
s_waitcnt lgkmcnt(4)
ds_read_b128 v[160:163], v221
ds_read_b128 v[164:167], v221
v_mov_b32 v168, v250
v_mov_b32 v169, v250
v_mov_b32 v170, v250
v_mfma_f32_32x32x16_bf16 v[128:143], v[208:211], v[32:35], v[128:143]
ds_read_b64_tr_b16 v[200:201], v234 offset:20480
ds_read_b64_tr_b16 v[202:203], v234 offset:20992
v_mov_b32 v171, v250
v_mov_b32 v172, v250
v_mov_b32 v173, v250
v_mfma_f32_32x32x16_bf16 v[144:159], v[212:215], v[32:35], v[144:159]
ds_read_b64_tr_b16 v[204:205], v235 offset:20480
ds_read_b64_tr_b16 v[206:207], v235 offset:20992
v_mov_b32 v174, v250
v_mov_b32 v175, v250
s_waitcnt lgkmcnt(6)
v_mfma_f32_32x32x16_bf16 v[128:143], v[192:195], v[36:39], v[128:143]
ds_read_b64_tr_b16 v[208:209], v234 offset:22528
ds_read_b64_tr_b16 v[210:211], v234 offset:23040
v_cvt_pk_bf16_f32 v40, v176, v177
v_cvt_pk_bf16_f32 v41, v178, v179
v_cvt_pk_bf16_f32 v42, v180, v181
v_cvt_pk_bf16_f32 v43, v182, v183
v_mfma_f32_32x32x16_bf16 v[144:159], v[196:199], v[36:39], v[144:159]
ds_read_b64_tr_b16 v[212:213], v235 offset:22528
ds_read_b64_tr_b16 v[214:215], v235 offset:23040
v_cvt_pk_bf16_f32 v44, v184, v185
v_cvt_pk_bf16_f32 v45, v186, v187
v_cvt_pk_bf16_f32 v46, v188, v189
v_cvt_pk_bf16_f32 v47, v190, v191
s_waitcnt lgkmcnt(8)
v_smfmac_f32_32x32x32_bf16 v[160:175], v[216:219], v[48:55], v220
s_waitcnt lgkmcnt(4)
ds_read_b128 v[176:179], v221
ds_read_b128 v[180:183], v221
v_mov_b32 v184, v250
v_mov_b32 v185, v250
v_mov_b32 v186, v250
v_mfma_f32_32x32x16_bf16 v[128:143], v[200:203], v[40:43], v[128:143]
ds_read_b64_tr_b16 v[192:193], v234 offset:36864
ds_read_b64_tr_b16 v[194:195], v234 offset:37376
v_mov_b32 v187, v250
v_mov_b32 v188, v250
v_mov_b32 v189, v250
v_mfma_f32_32x32x16_bf16 v[144:159], v[204:207], v[40:43], v[144:159]
ds_read_b64_tr_b16 v[196:197], v235 offset:36864
ds_read_b64_tr_b16 v[198:199], v235 offset:37376
v_mov_b32 v190, v250
v_mov_b32 v191, v250
s_waitcnt lgkmcnt(6)
v_mfma_f32_32x32x16_bf16 v[128:143], v[208:211], v[44:47], v[128:143]
ds_read_b64_tr_b16 v[200:201], v234 offset:38912
ds_read_b64_tr_b16 v[202:203], v234 offset:39424
v_cvt_pk_bf16_f32 v48, v160, v161
v_cvt_pk_bf16_f32 v49, v162, v163
v_cvt_pk_bf16_f32 v50, v164, v165
v_cvt_pk_bf16_f32 v51, v166, v167
v_mfma_f32_32x32x16_bf16 v[144:159], v[212:215], v[44:47], v[144:159]
ds_read_b64_tr_b16 v[204:205], v235 offset:38912
ds_read_b64_tr_b16 v[206:207], v235 offset:39424
v_cvt_pk_bf16_f32 v52, v168, v169
v_cvt_pk_bf16_f32 v53, v170, v171
v_cvt_pk_bf16_f32 v54, v172, v173
v_cvt_pk_bf16_f32 v55, v174, v175
s_waitcnt lgkmcnt(8)
v_smfmac_f32_32x32x32_bf16 v[176:191], v[216:219], v[56:63], v220
s_waitcnt lgkmcnt(4)
ds_read_b128 v[160:163], v221
ds_read_b128 v[164:167], v221
v_mov_b32 v168, v250
v_mov_b32 v169, v250
v_mov_b32 v170, v250
v_mfma_f32_32x32x16_bf16 v[128:143], v[192:195], v[48:51], v[128:143]
ds_read_b64_tr_b16 v[208:209], v234 offset:53248
ds_read_b64_tr_b16 v[210:211], v234 offset:53760
v_mov_b32 v171, v250
v_mov_b32 v172, v250
v_mov_b32 v173, v250
v_mfma_f32_32x32x16_bf16 v[144:159], v[196:199], v[48:51], v[144:159]
ds_read_b64_tr_b16 v[212:213], v235 offset:53248
ds_read_b64_tr_b16 v[214:215], v235 offset:53760
v_mov_b32 v174, v250
v_mov_b32 v175, v250
s_waitcnt lgkmcnt(6)
v_mfma_f32_32x32x16_bf16 v[128:143], v[200:203], v[52:55], v[128:143]
ds_read_b32 v252, v245 offset:768
ds_read_b64_tr_b16 v[192:193], v234 offset:55296
ds_read_b64_tr_b16 v[194:195], v234 offset:55808
v_cvt_pk_bf16_f32 v56, v176, v177
v_cvt_pk_bf16_f32 v57, v178, v179
v_cvt_pk_bf16_f32 v58, v180, v181
v_cvt_pk_bf16_f32 v59, v182, v183
v_mfma_f32_32x32x16_bf16 v[144:159], v[204:207], v[52:55], v[144:159]
ds_read_b64_tr_b16 v[196:197], v235 offset:55296
ds_read_b64_tr_b16 v[198:199], v235 offset:55808
v_cvt_pk_bf16_f32 v60, v184, v185
v_cvt_pk_bf16_f32 v61, v186, v187
v_cvt_pk_bf16_f32 v62, v188, v189
v_cvt_pk_bf16_f32 v63, v190, v191
s_waitcnt lgkmcnt(9)
v_smfmac_f32_32x32x32_bf16 v[160:175], v[216:219], v[64:71], v220
)ASM" R"ASM(
s_waitcnt lgkmcnt(5)
ds_read_b128 v[176:179], v221
ds_read_b128 v[180:183], v221
v_mov_b32 v184, v250
v_mov_b32 v185, v250
v_mov_b32 v186, v250
v_mfma_f32_32x32x16_bf16 v[128:143], v[208:211], v[56:59], v[128:143]
s_waitcnt lgkmcnt(6)
v_cmp_gt_u32 vcc, 4, v252
s_nop 0
s_nop 0
s_cbranch_vccnz .Lk_ls_5
.Lk_ld_6:
ds_read_b64_tr_b16 v[200:201], v234 offset:8192
ds_read_b64_tr_b16 v[202:203], v234 offset:8704
v_mov_b32 v187, v250
v_mov_b32 v188, v250
v_mov_b32 v189, v250
v_mfma_f32_32x32x16_bf16 v[144:159], v[212:215], v[56:59], v[144:159]
ds_read_b64_tr_b16 v[204:205], v235 offset:8192
ds_read_b64_tr_b16 v[206:207], v235 offset:8704
v_mov_b32 v190, v250
v_mov_b32 v191, v250
s_waitcnt lgkmcnt(6)
v_mfma_f32_32x32x16_bf16 v[128:143], v[192:195], v[60:63], v[128:143]
ds_read_b64_tr_b16 v[208:209], v234 offset:10240
ds_read_b64_tr_b16 v[210:211], v234 offset:10752
v_cvt_pk_bf16_f32 v64, v160, v161
v_cvt_pk_bf16_f32 v65, v162, v163
v_cvt_pk_bf16_f32 v66, v164, v165
v_cvt_pk_bf16_f32 v67, v166, v167
v_mfma_f32_32x32x16_bf16 v[144:159], v[196:199], v[60:63], v[144:159]
ds_add_u32 v245, v246 offset:512
ds_read_b64_tr_b16 v[212:213], v235 offset:10240
ds_read_b64_tr_b16 v[214:215], v235 offset:10752
v_cvt_pk_bf16_f32 v68, v168, v169
v_cvt_pk_bf16_f32 v69, v170, v171
v_cvt_pk_bf16_f32 v70, v172, v173
v_cvt_pk_bf16_f32 v71, v174, v175
s_waitcnt lgkmcnt(9)
v_smfmac_f32_32x32x32_bf16 v[176:191], v[216:219], v[72:79], v220
s_waitcnt lgkmcnt(5)
ds_read_b128 v[160:163], v221
ds_read_b128 v[164:167], v221
v_mov_b32 v168, v250
v_mov_b32 v169, v250
v_mov_b32 v170, v250
v_mfma_f32_32x32x16_bf16 v[128:143], v[200:203], v[64:67], v[128:143]
ds_read_b64_tr_b16 v[192:193], v234 offset:24576
ds_read_b64_tr_b16 v[194:195], v234 offset:25088
v_mov_b32 v171, v250
v_mov_b32 v172, v250
v_mov_b32 v173, v250
v_mfma_f32_32x32x16_bf16 v[144:159], v[204:207], v[64:67], v[144:159]
ds_read_b64_tr_b16 v[196:197], v235 offset:24576
ds_read_b64_tr_b16 v[198:199], v235 offset:25088
v_mov_b32 v174, v250
v_mov_b32 v175, v250
s_waitcnt lgkmcnt(6)
v_mfma_f32_32x32x16_bf16 v[128:143], v[208:211], v[68:71], v[128:143]
ds_read_b64_tr_b16 v[200:201], v234 offset:26624
ds_read_b64_tr_b16 v[202:203], v234 offset:27136
v_cvt_pk_bf16_f32 v72, v176, v177
v_cvt_pk_bf16_f32 v73, v178, v179
v_cvt_pk_bf16_f32 v74, v180, v181
v_cvt_pk_bf16_f32 v75, v182, v183
v_mfma_f32_32x32x16_bf16 v[144:159], v[212:215], v[68:71], v[144:159]
ds_read_b64_tr_b16 v[204:205], v235 offset:26624
ds_read_b64_tr_b16 v[206:207], v235 offset:27136
v_cvt_pk_bf16_f32 v76, v184, v185
v_cvt_pk_bf16_f32 v77, v186, v187
v_cvt_pk_bf16_f32 v78, v188, v189
v_cvt_pk_bf16_f32 v79, v190, v191
s_waitcnt lgkmcnt(8)
v_smfmac_f32_32x32x32_bf16 v[160:175], v[216:219], v[80:87], v220
s_waitcnt lgkmcnt(4)
ds_read_b128 v[176:179], v221
ds_read_b128 v[180:183], v221
v_mov_b32 v184, v250
v_mov_b32 v185, v250
v_mov_b32 v186, v250
v_mfma_f32_32x32x16_bf16 v[128:143], v[192:195], v[72:75], v[128:143]
ds_read_b64_tr_b16 v[208:209], v234 offset:40960
ds_read_b64_tr_b16 v[210:211], v234 offset:41472
v_mov_b32 v187, v250
v_mov_b32 v188, v250
v_mov_b32 v189, v250
v_mfma_f32_32x32x16_bf16 v[144:159], v[196:199], v[72:75], v[144:159]
ds_read_b64_tr_b16 v[212:213], v235 offset:40960
ds_read_b64_tr_b16 v[214:215], v235 offset:41472
v_mov_b32 v190, v250
v_mov_b32 v191, v250
s_waitcnt lgkmcnt(6)
v_mfma_f32_32x32x16_bf16 v[128:143], v[200:203], v[76:79], v[128:143]
ds_read_b64_tr_b16 v[192:193], v234 offset:43008
ds_read_b64_tr_b16 v[194:195], v234 offset:43520
v_cvt_pk_bf16_f32 v80, v160, v161
v_cvt_pk_bf16_f32 v81, v162, v163
v_cvt_pk_bf16_f32 v82, v164, v165
v_cvt_pk_bf16_f32 v83, v166, v167
v_mfma_f32_32x32x16_bf16 v[144:159], v[204:207], v[76:79], v[144:159]
ds_read_b64_tr_b16 v[196:197], v235 offset:43008
ds_read_b64_tr_b16 v[198:199], v235 offset:43520
v_cvt_pk_bf16_f32 v84, v168, v169
v_cvt_pk_bf16_f32 v85, v170, v171
v_cvt_pk_bf16_f32 v86, v172, v173
v_cvt_pk_bf16_f32 v87, v174, v175
s_waitcnt lgkmcnt(8)
v_smfmac_f32_32x32x32_bf16 v[176:191], v[216:219], v[88:95], v220
s_waitcnt lgkmcnt(4)
ds_read_b128 v[160:163], v221
ds_read_b128 v[164:167], v221
v_mov_b32 v168, v250
v_mov_b32 v169, v250
v_mov_b32 v170, v250
v_mfma_f32_32x32x16_bf16 v[128:143], v[208:211], v[80:83], v[128:143]
ds_read_b64_tr_b16 v[200:201], v234 offset:57344
ds_read_b64_tr_b16 v[202:203], v234 offset:57856
v_mov_b32 v171, v250
v_mov_b32 v172, v250
v_mov_b32 v173, v250
v_mfma_f32_32x32x16_bf16 v[144:159], v[212:215], v[80:83], v[144:159]
ds_read_b64_tr_b16 v[204:205], v235 offset:57344
ds_read_b64_tr_b16 v[206:207], v235 offset:57856
v_mov_b32 v174, v250
v_mov_b32 v175, v250
s_waitcnt lgkmcnt(6)
v_mfma_f32_32x32x16_bf16 v[128:143], v[192:195], v[84:87], v[128:143]
ds_read_b32 v252, v245 offset:0
ds_read_b64_tr_b16 v[208:209], v234 offset:59392
ds_read_b64_tr_b16 v[210:211], v234 offset:59904
v_cvt_pk_bf16_f32 v88, v176, v177
v_cvt_pk_bf16_f32 v89, v178, v179
v_cvt_pk_bf16_f32 v90, v180, v181
v_cvt_pk_bf16_f32 v91, v182, v183
v_mfma_f32_32x32x16_bf16 v[144:159], v[196:199], v[84:87], v[144:159]
ds_read_b64_tr_b16 v[212:213], v235 offset:59392
ds_read_b64_tr_b16 v[214:215], v235 offset:59904
v_cvt_pk_bf16_f32 v92, v184, v185
v_cvt_pk_bf16_f32 v93, v186, v187
v_cvt_pk_bf16_f32 v94, v188, v189
v_cvt_pk_bf16_f32 v95, v190, v191
s_waitcnt lgkmcnt(9)
v_smfmac_f32_32x32x32_bf16 v[160:175], v[216:219], v[96:103], v220
s_waitcnt lgkmcnt(5)
ds_read_b128 v[176:179], v221
ds_read_b128 v[180:183], v221
v_mov_b32 v184, v250
v_mov_b32 v185, v250
v_mov_b32 v186, v250
v_mfma_f32_32x32x16_bf16 v[128:143], v[200:203], v[88:91], v[128:143]
s_waitcnt lgkmcnt(6)
v_cmp_gt_u32 vcc, 4, v252
s_nop 0
s_nop 0
s_cbranch_vccnz .Lk_ls_7
.Lk_ld_8:
ds_read_b64_tr_b16 v[192:193], v234 offset:12288
ds_read_b64_tr_b16 v[194:195], v234 offset:12800
v_mov_b32 v187, v250
v_mov_b32 v188, v250
v_mov_b32 v189, v250
v_mfma_f32_32x32x16_bf16 v[144:159], v[204:207], v[88:91], v[144:159]
ds_read_b64_tr_b16 v[196:197], v235 offset:12288
ds_read_b64_tr_b16 v[198:199], v235 offset:12800
v_mov_b32 v190, v250
v_mov_b32 v191, v250
s_waitcnt lgkmcnt(6)
v_mfma_f32_32x32x16_bf16 v[128:143], v[208:211], v[92:95], v[128:143]
ds_read_b64_tr_b16 v[200:201], v234 offset:14336
ds_read_b64_tr_b16 v[202:203], v234 offset:14848
v_cvt_pk_bf16_f32 v96, v160, v161
v_cvt_pk_bf16_f32 v97, v162, v163
v_cvt_pk_bf16_f32 v98, v164, v165
v_cvt_pk_bf16_f32 v99, v166, v167
v_mfma_f32_32x32x16_bf16 v[144:159], v[212:215], v[92:95], v[144:159]
ds_read_b64_tr_b16 v[204:205], v235 offset:14336
ds_read_b64_tr_b16 v[206:207], v235 offset:14848
v_cvt_pk_bf16_f32 v100, v168, v169
v_cvt_pk_bf16_f32 v101, v170, v171
v_cvt_pk_bf16_f32 v102, v172, v173
v_cvt_pk_bf16_f32 v103, v174, v175
s_waitcnt lgkmcnt(8)
v_smfmac_f32_32x32x32_bf16 v[176:191], v[216:219], v[104:111], v220
s_waitcnt lgkmcnt(4)
ds_read_b128 v[160:163], v221
ds_read_b128 v[164:167], v221
v_mov_b32 v168, v250
v_mov_b32 v169, v250
v_mov_b32 v170, v250
v_mfma_f32_32x32x16_bf16 v[128:143], v[192:195], v[96:99], v[128:143]
ds_read_b64_tr_b16 v[208:209], v234 offset:28672
ds_read_b64_tr_b16 v[210:211], v234 offset:29184
v_mov_b32 v171, v250
v_mov_b32 v172, v250
v_mov_b32 v173, v250
v_mfma_f32_32x32x16_bf16 v[144:159], v[196:199], v[96:99], v[144:159]
ds_read_b64_tr_b16 v[212:213], v235 offset:28672
ds_read_b64_tr_b16 v[214:215], v235 offset:29184
v_mov_b32 v174, v250
)ASM" R"ASM(
v_mov_b32 v175, v250
s_waitcnt lgkmcnt(6)
v_mfma_f32_32x32x16_bf16 v[128:143], v[200:203], v[100:103], v[128:143]
ds_read_b64_tr_b16 v[192:193], v234 offset:30720
ds_read_b64_tr_b16 v[194:195], v234 offset:31232
v_cvt_pk_bf16_f32 v104, v176, v177
v_cvt_pk_bf16_f32 v105, v178, v179
v_cvt_pk_bf16_f32 v106, v180, v181
v_cvt_pk_bf16_f32 v107, v182, v183
v_mfma_f32_32x32x16_bf16 v[144:159], v[204:207], v[100:103], v[144:159]
ds_read_b64_tr_b16 v[196:197], v235 offset:30720
ds_read_b64_tr_b16 v[198:199], v235 offset:31232
v_cvt_pk_bf16_f32 v108, v184, v185
v_cvt_pk_bf16_f32 v109, v186, v187
v_cvt_pk_bf16_f32 v110, v188, v189
v_cvt_pk_bf16_f32 v111, v190, v191
s_waitcnt lgkmcnt(8)
v_smfmac_f32_32x32x32_bf16 v[160:175], v[216:219], v[112:119], v220
s_waitcnt lgkmcnt(4)
ds_read_b128 v[176:179], v221
ds_read_b128 v[180:183], v221
v_mov_b32 v184, v250
v_mov_b32 v185, v250
v_mov_b32 v186, v250
v_mfma_f32_32x32x16_bf16 v[128:143], v[208:211], v[104:107], v[128:143]
ds_read_b64_tr_b16 v[200:201], v234 offset:45056
ds_read_b64_tr_b16 v[202:203], v234 offset:45568
v_mov_b32 v187, v250
v_mov_b32 v188, v250
v_mov_b32 v189, v250
v_mfma_f32_32x32x16_bf16 v[144:159], v[212:215], v[104:107], v[144:159]
ds_read_b64_tr_b16 v[204:205], v235 offset:45056
ds_read_b64_tr_b16 v[206:207], v235 offset:45568
v_mov_b32 v190, v250
v_mov_b32 v191, v250
s_waitcnt lgkmcnt(6)
v_mfma_f32_32x32x16_bf16 v[128:143], v[192:195], v[108:111], v[128:143]
ds_read_b64_tr_b16 v[208:209], v234 offset:47104
ds_read_b64_tr_b16 v[210:211], v234 offset:47616
v_cvt_pk_bf16_f32 v112, v160, v161
v_cvt_pk_bf16_f32 v113, v162, v163
v_cvt_pk_bf16_f32 v114, v164, v165
v_cvt_pk_bf16_f32 v115, v166, v167
v_mfma_f32_32x32x16_bf16 v[144:159], v[196:199], v[108:111], v[144:159]
ds_read_b64_tr_b16 v[212:213], v235 offset:47104
ds_read_b64_tr_b16 v[214:215], v235 offset:47616
v_cvt_pk_bf16_f32 v116, v168, v169
v_cvt_pk_bf16_f32 v117, v170, v171
v_cvt_pk_bf16_f32 v118, v172, v173
v_cvt_pk_bf16_f32 v119, v174, v175
s_waitcnt lgkmcnt(8)
v_smfmac_f32_32x32x32_bf16 v[176:191], v[216:219], v[120:127], v220
s_waitcnt lgkmcnt(4)
v_mfma_f32_32x32x16_bf16 v[128:143], v[200:203], v[112:115], v[128:143]
ds_read_b64_tr_b16 v[192:193], v234 offset:61440
ds_read_b64_tr_b16 v[194:195], v234 offset:61952
v_mfma_f32_32x32x16_bf16 v[144:159], v[204:207], v[112:115], v[144:159]
ds_read_b64_tr_b16 v[196:197], v235 offset:61440
ds_read_b64_tr_b16 v[198:199], v235 offset:61952
s_waitcnt lgkmcnt(4)
v_mfma_f32_32x32x16_bf16 v[128:143], v[208:211], v[116:119], v[128:143]
ds_read_b64_tr_b16 v[200:201], v234 offset:63488
ds_read_b64_tr_b16 v[202:203], v234 offset:64000
s_nop 0
v_cvt_pk_bf16_f32 v120, v176, v177
v_cvt_pk_bf16_f32 v121, v178, v179
v_cvt_pk_bf16_f32 v122, v180, v181
v_cvt_pk_bf16_f32 v123, v182, v183
v_mfma_f32_32x32x16_bf16 v[144:159], v[212:215], v[116:119], v[144:159]
ds_read_b64_tr_b16 v[204:205], v235 offset:63488
ds_read_b64_tr_b16 v[206:207], v235 offset:64000
v_cvt_pk_bf16_f32 v124, v184, v185
v_cvt_pk_bf16_f32 v125, v186, v187
v_cvt_pk_bf16_f32 v126, v188, v189
v_cvt_pk_bf16_f32 v127, v190, v191
s_waitcnt lgkmcnt(4)
v_mfma_f32_32x32x16_bf16 v[128:143], v[192:195], v[120:123], v[128:143]
v_mfma_f32_32x32x16_bf16 v[144:159], v[196:199], v[120:123], v[144:159]
s_waitcnt lgkmcnt(0)
v_mfma_f32_32x32x16_bf16 v[128:143], v[200:203], v[124:127], v[128:143]
v_mfma_f32_32x32x16_bf16 v[144:159], v[204:207], v[124:127], v[144:159]
s_waitcnt lgkmcnt(0)
s_add_u32 s68, s99, 0
s_nop 8
v_pk_mul_f32 v[128:129], v[128:129], v[228:229]
v_pk_mul_f32 v[130:131], v[130:131], v[228:229]
v_pk_mul_f32 v[132:133], v[132:133], v[228:229]
v_pk_mul_f32 v[134:135], v[134:135], v[228:229]
v_cvt_pk_bf16_f32 v128, v128, v129
v_cvt_pk_bf16_f32 v129, v130, v131
v_cvt_pk_bf16_f32 v130, v132, v133
v_cvt_pk_bf16_f32 v131, v134, v135
buffer_store_dwordx4 v[128:131], v243, s[48:51], s68 offen offset:0 sc1
s_add_u32 s68, s99, 131072
v_pk_mul_f32 v[136:137], v[136:137], v[228:229]
v_pk_mul_f32 v[138:139], v[138:139], v[228:229]
v_pk_mul_f32 v[140:141], v[140:141], v[228:229]
v_pk_mul_f32 v[142:143], v[142:143], v[228:229]
v_cvt_pk_bf16_f32 v136, v136, v137
v_cvt_pk_bf16_f32 v137, v138, v139
v_cvt_pk_bf16_f32 v138, v140, v141
v_cvt_pk_bf16_f32 v139, v142, v143
buffer_store_dwordx4 v[136:139], v243, s[48:51], s68 offen offset:0 sc1
s_add_u32 s68, s99, 262144
v_pk_mul_f32 v[144:145], v[144:145], v[228:229]
v_pk_mul_f32 v[146:147], v[146:147], v[228:229]
v_pk_mul_f32 v[148:149], v[148:149], v[228:229]
v_pk_mul_f32 v[150:151], v[150:151], v[228:229]
v_cvt_pk_bf16_f32 v144, v144, v145
v_cvt_pk_bf16_f32 v145, v146, v147
v_cvt_pk_bf16_f32 v146, v148, v149
v_cvt_pk_bf16_f32 v147, v150, v151
buffer_store_dwordx4 v[144:147], v243, s[48:51], s68 offen offset:0 sc1
s_add_u32 s68, s99, 393216
v_pk_mul_f32 v[152:153], v[152:153], v[228:229]
v_pk_mul_f32 v[154:155], v[154:155], v[228:229]
v_pk_mul_f32 v[156:157], v[156:157], v[228:229]
v_pk_mul_f32 v[158:159], v[158:159], v[228:229]
v_cvt_pk_bf16_f32 v152, v152, v153
v_cvt_pk_bf16_f32 v153, v154, v155
v_cvt_pk_bf16_f32 v154, v156, v157
v_cvt_pk_bf16_f32 v155, v158, v159
buffer_store_dwordx4 v[152:155], v243, s[48:51], s68 offen offset:0 sc1
s_branch .Lk_qproj
.Lk_vproj:
s_lshl_b32 s62, s53, 7
s_add_u32 s62, s62, 162816
v_and_b32 v248, 31, v224
v_lshl_add_u32 v248, v248, 2, s62
v_lshrrev_b32 v249, 5, v224
v_lshl_add_u32 v249, v249, 4, s62
v_mov_b32 v124, v140
v_mov_b32 v125, v141
v_mov_b32 v126, v142
v_mov_b32 v127, v143
s_waitcnt vmcnt(4)
s_barrier
ds_read_b64 v[232:233], v252
ds_read_b64_tr_b16 v[192:193], v234 offset:0
ds_read_b64_tr_b16 v[194:195], v234 offset:512
ds_read_b64_tr_b16 v[196:197], v235 offset:0
ds_read_b64_tr_b16 v[198:199], v235 offset:512
ds_read_b64_tr_b16 v[200:201], v234 offset:2048
ds_read_b64_tr_b16 v[202:203], v234 offset:2560
ds_read_b64_tr_b16 v[204:205], v235 offset:2048
ds_read_b64_tr_b16 v[206:207], v235 offset:2560
s_waitcnt lgkmcnt(8)
v_add_f32 v250, v250, v232
v_add_f32 v227, v251, v233
v_mul_f32 v250, 0xbb000000, v250
v_cvt_pk_bf16_f32 v251, v250, v250
v_and_b32 v160, s64, v251
v_and_b32 v161, s64, v251
v_and_b32 v162, s64, v251
v_and_b32 v163, s64, v251
ds_write_b128 v221, v[160:163]
v_and_b32 v164, s64, v251
v_and_b32 v165, s64, v251
v_and_b32 v166, s64, v251
v_and_b32 v167, s64, v251
v_and_b32 v168, s64, v251
v_and_b32 v169, s64, v251
v_and_b32 v170, s64, v251
v_and_b32 v171, s64, v251
v_and_b32 v172, s64, v251
v_and_b32 v173, s64, v251
v_and_b32 v174, s64, v251
v_and_b32 v175, s64, v251
ds_read_b128 v[176:179], v221
ds_read_b128 v[180:183], v221
ds_read_b128 v[184:187], v221
ds_read_b128 v[188:191], v221
v_smfmac_f32_32x32x32_bf16 v[160:175], v[216:219], v[0:7], v220
v_mul_f32 v227, 0x3b000000, v227
v_fma_f32 v227, -v250, v250, v227
v_max_f32 v227, 0, v227
v_cvt_pk_bf16_f32 v227, v227, v227
v_and_b32 v227, s64, v227
v_add_f32 v227, 0x3727c5ac, v227
v_cvt_pk_bf16_f32 v227, v227, v227
v_and_b32 v227, s64, v227
v_sqrt_f32 v227, v227
s_nop 0
v_cvt_pk_bf16_f32 v230, v227, v227
v_and_b32 v230, s64, v230
v_rcp_f32 v228, v230
v_and_b32 v250, s64, v251
v_and_b32 v251, s64, v251
v_cvt_pk_bf16_f32 v0, v160, v161
v_cvt_pk_bf16_f32 v1, v162, v163
v_cvt_pk_bf16_f32 v2, v164, v165
v_cvt_pk_bf16_f32 v3, v166, v167
v_cvt_pk_bf16_f32 v4, v168, v169
v_cvt_pk_bf16_f32 v5, v170, v171
v_cvt_pk_bf16_f32 v6, v172, v173
v_cvt_pk_bf16_f32 v7, v174, v175
s_waitcnt lgkmcnt(0)
v_smfmac_f32_32x32x32_bf16 v[176:191], v[216:219], v[8:15], v220
ds_read_b128 v[160:163], v221
ds_read_b128 v[164:167], v221
)ASM" R"ASM(
v_mov_b32 v168, v250
v_mov_b32 v169, v250
v_mov_b32 v170, v250
v_mfma_f32_32x32x16_bf16 v[128:143], v[0:3], v[192:195], 0
ds_read_b64_tr_b16 v[208:209], v234 offset:16384
ds_read_b64_tr_b16 v[210:211], v234 offset:16896
v_mov_b32 v171, v250
v_mov_b32 v172, v250
v_mov_b32 v173, v250
ds_read_b128 v[64:67], v124 offset:0
ds_read_b128 v[68:71], v125 offset:0
ds_read_b128 v[72:75], v126 offset:0
v_mfma_f32_32x32x16_bf16 v[144:159], v[0:3], v[196:199], 0
ds_read_b64_tr_b16 v[212:213], v235 offset:16384
ds_read_b64_tr_b16 v[214:215], v235 offset:16896
v_mov_b32 v174, v250
v_mov_b32 v175, v250
ds_read_b128 v[76:79], v127 offset:0
ds_read_b128 v[80:83], v124 offset:4096
ds_read_b128 v[84:87], v125 offset:4096
v_mfma_f32_32x32x16_bf16 v[128:143], v[4:7], v[200:203], v[128:143]
ds_read_b64_tr_b16 v[192:193], v234 offset:18432
ds_read_b64_tr_b16 v[194:195], v234 offset:18944
v_cvt_pk_bf16_f32 v8, v176, v177
v_cvt_pk_bf16_f32 v9, v178, v179
v_cvt_pk_bf16_f32 v10, v180, v181
v_cvt_pk_bf16_f32 v11, v182, v183
v_mfma_f32_32x32x16_bf16 v[144:159], v[4:7], v[204:207], v[144:159]
ds_read_b64_tr_b16 v[196:197], v235 offset:18432
s_waitcnt lgkmcnt(13)
ds_read_b64_tr_b16 v[198:199], v235 offset:18944
v_cvt_pk_bf16_f32 v12, v184, v185
v_cvt_pk_bf16_f32 v13, v186, v187
v_cvt_pk_bf16_f32 v14, v188, v189
v_cvt_pk_bf16_f32 v15, v190, v191
v_smfmac_f32_32x32x32_bf16 v[160:175], v[216:219], v[16:23], v220
s_waitcnt lgkmcnt(7)
ds_read_b128 v[176:179], v221
ds_read_b128 v[180:183], v221
v_mov_b32 v184, v250
v_mov_b32 v185, v250
v_mov_b32 v186, v250
v_mfma_f32_32x32x16_bf16 v[128:143], v[8:11], v[208:211], v[128:143]
ds_read_b64_tr_b16 v[200:201], v234 offset:32768
ds_read_b64_tr_b16 v[202:203], v234 offset:33280
v_mov_b32 v187, v250
v_mov_b32 v188, v250
v_mov_b32 v189, v250
ds_read_b128 v[88:91], v126 offset:4096
ds_read_b128 v[92:95], v127 offset:4096
ds_read_b128 v[96:99], v124 offset:8192
v_mfma_f32_32x32x16_bf16 v[144:159], v[8:11], v[212:215], v[144:159]
ds_read_b64_tr_b16 v[204:205], v235 offset:32768
s_waitcnt lgkmcnt(13)
ds_read_b64_tr_b16 v[206:207], v235 offset:33280
v_mov_b32 v190, v250
v_mov_b32 v191, v250
ds_read_b128 v[100:103], v125 offset:8192
s_waitcnt lgkmcnt(13)
ds_read_b128 v[104:107], v126 offset:8192
ds_read_b128 v[108:111], v127 offset:8192
s_waitcnt lgkmcnt(12)
v_mfma_f32_32x32x16_bf16 v[128:143], v[12:15], v[192:195], v[128:143]
ds_read_b64_tr_b16 v[208:209], v234 offset:34816
ds_read_b64_tr_b16 v[210:211], v234 offset:35328
v_cvt_pk_bf16_f32 v16, v160, v161
v_cvt_pk_bf16_f32 v17, v162, v163
v_cvt_pk_bf16_f32 v18, v164, v165
v_cvt_pk_bf16_f32 v19, v166, v167
v_mfma_f32_32x32x16_bf16 v[144:159], v[12:15], v[196:199], v[144:159]
s_waitcnt vmcnt(0)
ds_add_u32 v245, v246 offset:256
s_waitcnt lgkmcnt(13)
ds_read_b64_tr_b16 v[212:213], v235 offset:34816
ds_read_b64_tr_b16 v[214:215], v235 offset:35328
v_cvt_pk_bf16_f32 v20, v168, v169
v_cvt_pk_bf16_f32 v21, v170, v171
v_cvt_pk_bf16_f32 v22, v172, v173
v_cvt_pk_bf16_f32 v23, v174, v175
v_smfmac_f32_32x32x32_bf16 v[176:191], v[216:219], v[24:31], v220
s_waitcnt lgkmcnt(8)
ds_read_b128 v[160:163], v221
ds_read_b128 v[164:167], v221
v_mov_b32 v168, v250
v_mov_b32 v169, v250
v_mov_b32 v170, v250
v_mfma_f32_32x32x16_bf16 v[128:143], v[16:19], v[200:203], v[128:143]
ds_read_b64_tr_b16 v[192:193], v234 offset:49152
ds_read_b64_tr_b16 v[194:195], v234 offset:49664
v_mov_b32 v171, v250
v_mov_b32 v172, v250
v_mov_b32 v173, v250
ds_read_b128 v[112:115], v124 offset:12288
ds_read_b128 v[116:119], v125 offset:12288
ds_read_b128 v[120:123], v126 offset:12288
v_mfma_f32_32x32x16_bf16 v[144:159], v[16:19], v[204:207], v[144:159]
s_waitcnt lgkmcnt(13)
ds_read_b64_tr_b16 v[196:197], v235 offset:49152
ds_read_b64_tr_b16 v[198:199], v235 offset:49664
v_mov_b32 v174, v250
v_mov_b32 v175, v250
s_waitcnt lgkmcnt(13)
ds_read_b128 v[124:127], v127 offset:12288
ds_add_u32 v245, v246 offset:512
v_mov_b32 v231, v230
s_waitcnt lgkmcnt(11)
v_mfma_f32_32x32x16_bf16 v[128:143], v[20:23], v[208:211], v[128:143]
ds_read_b32 v252, v245 offset:256
ds_read_b64_tr_b16 v[200:201], v234 offset:51200
ds_read_b64_tr_b16 v[202:203], v234 offset:51712
v_cvt_pk_bf16_f32 v24, v176, v177
v_cvt_pk_bf16_f32 v25, v178, v179
v_cvt_pk_bf16_f32 v26, v180, v181
v_cvt_pk_bf16_f32 v27, v182, v183
v_mfma_f32_32x32x16_bf16 v[144:159], v[20:23], v[212:215], v[144:159]
ds_read_b64_tr_b16 v[204:205], v235 offset:51200
s_waitcnt lgkmcnt(13)
ds_read_b64_tr_b16 v[206:207], v235 offset:51712
v_cvt_pk_bf16_f32 v28, v184, v185
v_cvt_pk_bf16_f32 v29, v186, v187
v_cvt_pk_bf16_f32 v30, v188, v189
v_cvt_pk_bf16_f32 v31, v190, v191
v_smfmac_f32_32x32x32_bf16 v[160:175], v[216:219], v[32:39], v220
s_waitcnt lgkmcnt(7)
ds_read_b128 v[176:179], v221
ds_read_b128 v[180:183], v221
v_mov_b32 v184, v250
v_mov_b32 v185, v250
v_mov_b32 v186, v250
v_mfma_f32_32x32x16_bf16 v[128:143], v[24:27], v[192:195], v[128:143]
s_waitcnt lgkmcnt(6)
v_cmp_gt_u32 vcc, 4, v252
s_nop 0
s_nop 0
s_cbranch_vccnz .Lk_ls_9
.Lk_ld_10:
ds_read_b64_tr_b16 v[208:209], v234 offset:4096
ds_read_b64_tr_b16 v[210:211], v234 offset:4608
v_mov_b32 v187, v250
v_mov_b32 v188, v250
v_mov_b32 v189, v250
v_mov_b32 v229, v228
v_mul_f32 v232, 0xbfb8aa3b, v228
v_mul_f32 v233, 0xbfb8aa3b, v228
v_mfma_f32_32x32x16_bf16 v[144:159], v[24:27], v[196:199], v[144:159]
ds_read_b64_tr_b16 v[212:213], v235 offset:4096
ds_read_b64_tr_b16 v[214:215], v235 offset:4608
v_mov_b32 v190, v250
v_mov_b32 v191, v250
ds_write_b32 v248, v228
s_waitcnt lgkmcnt(7)
v_mfma_f32_32x32x16_bf16 v[128:143], v[28:31], v[200:203], v[128:143]
ds_read_b64_tr_b16 v[192:193], v234 offset:6144
ds_read_b64_tr_b16 v[194:195], v234 offset:6656
v_cvt_pk_bf16_f32 v32, v160, v161
v_cvt_pk_bf16_f32 v33, v162, v163
v_cvt_pk_bf16_f32 v34, v164, v165
v_cvt_pk_bf16_f32 v35, v166, v167
v_mfma_f32_32x32x16_bf16 v[144:159], v[28:31], v[204:207], v[144:159]
ds_read_b64_tr_b16 v[196:197], v235 offset:6144
ds_read_b64_tr_b16 v[198:199], v235 offset:6656
v_cvt_pk_bf16_f32 v36, v168, v169
v_cvt_pk_bf16_f32 v37, v170, v171
v_cvt_pk_bf16_f32 v38, v172, v173
v_cvt_pk_bf16_f32 v39, v174, v175
s_waitcnt lgkmcnt(9)
v_smfmac_f32_32x32x32_bf16 v[176:191], v[216:219], v[40:47], v220
s_waitcnt lgkmcnt(5)
ds_read_b128 v[160:163], v221
ds_read_b128 v[164:167], v221
v_mov_b32 v168, v250
v_mov_b32 v169, v250
v_mov_b32 v170, v250
v_mfma_f32_32x32x16_bf16 v[128:143], v[32:35], v[208:211], v[128:143]
ds_read_b64_tr_b16 v[200:201], v234 offset:20480
ds_read_b64_tr_b16 v[202:203], v234 offset:20992
v_mov_b32 v171, v250
v_mov_b32 v172, v250
v_mov_b32 v173, v250
v_mfma_f32_32x32x16_bf16 v[144:159], v[32:35], v[212:215], v[144:159]
ds_read_b64_tr_b16 v[204:205], v235 offset:20480
ds_read_b64_tr_b16 v[206:207], v235 offset:20992
v_mov_b32 v174, v250
v_mov_b32 v175, v250
s_waitcnt lgkmcnt(6)
v_mfma_f32_32x32x16_bf16 v[128:143], v[36:39], v[192:195], v[128:143]
ds_read_b64_tr_b16 v[208:209], v234 offset:22528
ds_read_b64_tr_b16 v[210:211], v234 offset:23040
v_cvt_pk_bf16_f32 v40, v176, v177
v_cvt_pk_bf16_f32 v41, v178, v179
v_cvt_pk_bf16_f32 v42, v180, v181
v_cvt_pk_bf16_f32 v43, v182, v183
v_mfma_f32_32x32x16_bf16 v[144:159], v[36:39], v[196:199], v[144:159]
ds_read_b64_tr_b16 v[212:213], v235 offset:22528
ds_read_b64_tr_b16 v[214:215], v235 offset:23040
v_cvt_pk_bf16_f32 v44, v184, v185
v_cvt_pk_bf16_f32 v45, v186, v187
v_cvt_pk_bf16_f32 v46, v188, v189
v_cvt_pk_bf16_f32 v47, v190, v191
s_waitcnt lgkmcnt(8)
)ASM" R"ASM(
v_smfmac_f32_32x32x32_bf16 v[160:175], v[216:219], v[48:55], v220
s_waitcnt lgkmcnt(4)
ds_read_b128 v[176:179], v221
ds_read_b128 v[180:183], v221
v_mov_b32 v184, v250
v_mov_b32 v185, v250
v_mov_b32 v186, v250
v_mfma_f32_32x32x16_bf16 v[128:143], v[40:43], v[200:203], v[128:143]
ds_read_b64_tr_b16 v[192:193], v234 offset:36864
ds_read_b64_tr_b16 v[194:195], v234 offset:37376
v_mov_b32 v187, v250
v_mov_b32 v188, v250
v_mov_b32 v189, v250
v_mfma_f32_32x32x16_bf16 v[144:159], v[40:43], v[204:207], v[144:159]
ds_read_b64_tr_b16 v[196:197], v235 offset:36864
ds_read_b64_tr_b16 v[198:199], v235 offset:37376
v_mov_b32 v190, v250
v_mov_b32 v191, v250
s_waitcnt lgkmcnt(6)
v_mfma_f32_32x32x16_bf16 v[128:143], v[44:47], v[208:211], v[128:143]
ds_read_b64_tr_b16 v[200:201], v234 offset:38912
ds_read_b64_tr_b16 v[202:203], v234 offset:39424
v_cvt_pk_bf16_f32 v48, v160, v161
v_cvt_pk_bf16_f32 v49, v162, v163
v_cvt_pk_bf16_f32 v50, v164, v165
v_cvt_pk_bf16_f32 v51, v166, v167
v_mfma_f32_32x32x16_bf16 v[144:159], v[44:47], v[212:215], v[144:159]
ds_read_b64_tr_b16 v[204:205], v235 offset:38912
ds_read_b64_tr_b16 v[206:207], v235 offset:39424
v_cvt_pk_bf16_f32 v52, v168, v169
v_cvt_pk_bf16_f32 v53, v170, v171
v_cvt_pk_bf16_f32 v54, v172, v173
v_cvt_pk_bf16_f32 v55, v174, v175
s_waitcnt lgkmcnt(8)
v_smfmac_f32_32x32x32_bf16 v[176:191], v[216:219], v[56:63], v220
s_waitcnt lgkmcnt(4)
ds_read_b128 v[160:163], v221
ds_read_b128 v[164:167], v221
v_mov_b32 v168, v250
v_mov_b32 v169, v250
v_mov_b32 v170, v250
v_mfma_f32_32x32x16_bf16 v[128:143], v[48:51], v[192:195], v[128:143]
ds_read_b64_tr_b16 v[208:209], v234 offset:53248
ds_read_b64_tr_b16 v[210:211], v234 offset:53760
v_mov_b32 v171, v250
v_mov_b32 v172, v250
v_mov_b32 v173, v250
v_mfma_f32_32x32x16_bf16 v[144:159], v[48:51], v[196:199], v[144:159]
ds_read_b64_tr_b16 v[212:213], v235 offset:53248
ds_read_b64_tr_b16 v[214:215], v235 offset:53760
v_mov_b32 v174, v250
v_mov_b32 v175, v250
s_waitcnt lgkmcnt(6)
v_mfma_f32_32x32x16_bf16 v[128:143], v[52:55], v[200:203], v[128:143]
ds_read_b32 v252, v245 offset:768
ds_read_b64_tr_b16 v[192:193], v234 offset:55296
ds_read_b64_tr_b16 v[194:195], v234 offset:55808
v_cvt_pk_bf16_f32 v56, v176, v177
v_cvt_pk_bf16_f32 v57, v178, v179
v_cvt_pk_bf16_f32 v58, v180, v181
v_cvt_pk_bf16_f32 v59, v182, v183
v_mfma_f32_32x32x16_bf16 v[144:159], v[52:55], v[204:207], v[144:159]
ds_read_b64_tr_b16 v[196:197], v235 offset:55296
ds_read_b64_tr_b16 v[198:199], v235 offset:55808
v_cvt_pk_bf16_f32 v60, v184, v185
v_cvt_pk_bf16_f32 v61, v186, v187
v_cvt_pk_bf16_f32 v62, v188, v189
v_cvt_pk_bf16_f32 v63, v190, v191
s_waitcnt lgkmcnt(9)
v_smfmac_f32_32x32x32_bf16 v[160:175], v[216:219], v[64:71], v220
s_waitcnt lgkmcnt(5)
ds_read_b128 v[176:179], v221
ds_read_b128 v[180:183], v221
v_mov_b32 v184, v250
v_mov_b32 v185, v250
v_mov_b32 v186, v250
v_mfma_f32_32x32x16_bf16 v[128:143], v[56:59], v[208:211], v[128:143]
s_waitcnt lgkmcnt(6)
v_cmp_gt_u32 vcc, 4, v252
s_nop 0
s_nop 0
s_cbranch_vccnz .Lk_ls_11
.Lk_ld_12:
ds_read_b64_tr_b16 v[200:201], v234 offset:8192
ds_read_b64_tr_b16 v[202:203], v234 offset:8704
v_mov_b32 v187, v250
v_mov_b32 v188, v250
v_mov_b32 v189, v250
v_mfma_f32_32x32x16_bf16 v[144:159], v[56:59], v[212:215], v[144:159]
ds_read_b64_tr_b16 v[204:205], v235 offset:8192
ds_read_b64_tr_b16 v[206:207], v235 offset:8704
v_mov_b32 v190, v250
v_mov_b32 v191, v250
s_waitcnt lgkmcnt(6)
v_mfma_f32_32x32x16_bf16 v[128:143], v[60:63], v[192:195], v[128:143]
ds_read_b64_tr_b16 v[208:209], v234 offset:10240
ds_read_b64_tr_b16 v[210:211], v234 offset:10752
v_cvt_pk_bf16_f32 v64, v160, v161
v_cvt_pk_bf16_f32 v65, v162, v163
v_cvt_pk_bf16_f32 v66, v164, v165
v_cvt_pk_bf16_f32 v67, v166, v167
v_mfma_f32_32x32x16_bf16 v[144:159], v[60:63], v[196:199], v[144:159]
ds_add_u32 v245, v246 offset:512
ds_read_b64_tr_b16 v[212:213], v235 offset:10240
ds_read_b64_tr_b16 v[214:215], v235 offset:10752
v_cvt_pk_bf16_f32 v68, v168, v169
v_cvt_pk_bf16_f32 v69, v170, v171
v_cvt_pk_bf16_f32 v70, v172, v173
v_cvt_pk_bf16_f32 v71, v174, v175
s_waitcnt lgkmcnt(9)
v_smfmac_f32_32x32x32_bf16 v[176:191], v[216:219], v[72:79], v220
s_waitcnt lgkmcnt(5)
ds_read_b128 v[160:163], v221
ds_read_b128 v[164:167], v221
v_mov_b32 v168, v250
v_mov_b32 v169, v250
v_mov_b32 v170, v250
v_mfma_f32_32x32x16_bf16 v[128:143], v[64:67], v[200:203], v[128:143]
ds_read_b64_tr_b16 v[192:193], v234 offset:24576
ds_read_b64_tr_b16 v[194:195], v234 offset:25088
v_mov_b32 v171, v250
v_mov_b32 v172, v250
v_mov_b32 v173, v250
v_mfma_f32_32x32x16_bf16 v[144:159], v[64:67], v[204:207], v[144:159]
ds_read_b64_tr_b16 v[196:197], v235 offset:24576
ds_read_b64_tr_b16 v[198:199], v235 offset:25088
v_mov_b32 v174, v250
v_mov_b32 v175, v250
s_waitcnt lgkmcnt(6)
v_mfma_f32_32x32x16_bf16 v[128:143], v[68:71], v[208:211], v[128:143]
ds_read_b64_tr_b16 v[200:201], v234 offset:26624
ds_read_b64_tr_b16 v[202:203], v234 offset:27136
v_cvt_pk_bf16_f32 v72, v176, v177
v_cvt_pk_bf16_f32 v73, v178, v179
v_cvt_pk_bf16_f32 v74, v180, v181
v_cvt_pk_bf16_f32 v75, v182, v183
v_mfma_f32_32x32x16_bf16 v[144:159], v[68:71], v[212:215], v[144:159]
ds_read_b64_tr_b16 v[204:205], v235 offset:26624
ds_read_b64_tr_b16 v[206:207], v235 offset:27136
v_cvt_pk_bf16_f32 v76, v184, v185
v_cvt_pk_bf16_f32 v77, v186, v187
v_cvt_pk_bf16_f32 v78, v188, v189
v_cvt_pk_bf16_f32 v79, v190, v191
s_waitcnt lgkmcnt(8)
v_smfmac_f32_32x32x32_bf16 v[160:175], v[216:219], v[80:87], v220
s_waitcnt lgkmcnt(4)
ds_read_b128 v[176:179], v221
ds_read_b128 v[180:183], v221
v_mov_b32 v184, v250
v_mov_b32 v185, v250
v_mov_b32 v186, v250
v_mfma_f32_32x32x16_bf16 v[128:143], v[72:75], v[192:195], v[128:143]
ds_read_b64_tr_b16 v[208:209], v234 offset:40960
ds_read_b64_tr_b16 v[210:211], v234 offset:41472
v_mov_b32 v187, v250
v_mov_b32 v188, v250
v_mov_b32 v189, v250
v_mfma_f32_32x32x16_bf16 v[144:159], v[72:75], v[196:199], v[144:159]
ds_read_b64_tr_b16 v[212:213], v235 offset:40960
ds_read_b64_tr_b16 v[214:215], v235 offset:41472
v_mov_b32 v190, v250
v_mov_b32 v191, v250
s_waitcnt lgkmcnt(6)
v_mfma_f32_32x32x16_bf16 v[128:143], v[76:79], v[200:203], v[128:143]
ds_read_b64_tr_b16 v[192:193], v234 offset:43008
ds_read_b64_tr_b16 v[194:195], v234 offset:43520
v_cvt_pk_bf16_f32 v80, v160, v161
v_cvt_pk_bf16_f32 v81, v162, v163
v_cvt_pk_bf16_f32 v82, v164, v165
v_cvt_pk_bf16_f32 v83, v166, v167
v_mfma_f32_32x32x16_bf16 v[144:159], v[76:79], v[204:207], v[144:159]
ds_read_b64_tr_b16 v[196:197], v235 offset:43008
ds_read_b64_tr_b16 v[198:199], v235 offset:43520
v_cvt_pk_bf16_f32 v84, v168, v169
v_cvt_pk_bf16_f32 v85, v170, v171
v_cvt_pk_bf16_f32 v86, v172, v173
v_cvt_pk_bf16_f32 v87, v174, v175
s_waitcnt lgkmcnt(8)
v_smfmac_f32_32x32x32_bf16 v[176:191], v[216:219], v[88:95], v220
s_waitcnt lgkmcnt(4)
ds_read_b128 v[160:163], v221
ds_read_b128 v[164:167], v221
v_mov_b32 v168, v250
v_mov_b32 v169, v250
v_mov_b32 v170, v250
v_mfma_f32_32x32x16_bf16 v[128:143], v[80:83], v[208:211], v[128:143]
ds_read_b64_tr_b16 v[200:201], v234 offset:57344
ds_read_b64_tr_b16 v[202:203], v234 offset:57856
v_mov_b32 v171, v250
v_mov_b32 v172, v250
v_mov_b32 v173, v250
v_mfma_f32_32x32x16_bf16 v[144:159], v[80:83], v[212:215], v[144:159]
ds_read_b64_tr_b16 v[204:205], v235 offset:57344
ds_read_b64_tr_b16 v[206:207], v235 offset:57856
v_mov_b32 v174, v250
v_mov_b32 v175, v250
s_waitcnt lgkmcnt(6)
v_mfma_f32_32x32x16_bf16 v[128:143], v[84:87], v[192:195], v[128:143]
ds_read_b32 v252, v245 offset:0
ds_read_b64_tr_b16 v[208:209], v234 offset:59392
)ASM" R"ASM(
ds_read_b64_tr_b16 v[210:211], v234 offset:59904
v_cvt_pk_bf16_f32 v88, v176, v177
v_cvt_pk_bf16_f32 v89, v178, v179
v_cvt_pk_bf16_f32 v90, v180, v181
v_cvt_pk_bf16_f32 v91, v182, v183
v_mfma_f32_32x32x16_bf16 v[144:159], v[84:87], v[196:199], v[144:159]
ds_read_b64_tr_b16 v[212:213], v235 offset:59392
ds_read_b64_tr_b16 v[214:215], v235 offset:59904
v_cvt_pk_bf16_f32 v92, v184, v185
v_cvt_pk_bf16_f32 v93, v186, v187
v_cvt_pk_bf16_f32 v94, v188, v189
v_cvt_pk_bf16_f32 v95, v190, v191
s_waitcnt lgkmcnt(9)
v_smfmac_f32_32x32x32_bf16 v[160:175], v[216:219], v[96:103], v220
s_waitcnt lgkmcnt(5)
ds_read_b128 v[176:179], v221
ds_read_b128 v[180:183], v221
v_mov_b32 v184, v250
v_mov_b32 v185, v250
v_mov_b32 v186, v250
v_mfma_f32_32x32x16_bf16 v[128:143], v[88:91], v[200:203], v[128:143]
s_waitcnt lgkmcnt(6)
v_cmp_gt_u32 vcc, 4, v252
s_nop 0
s_nop 0
s_cbranch_vccnz .Lk_ls_13
.Lk_ld_14:
ds_read_b64_tr_b16 v[192:193], v234 offset:12288
ds_read_b64_tr_b16 v[194:195], v234 offset:12800
v_mov_b32 v187, v250
v_mov_b32 v188, v250
v_mov_b32 v189, v250
v_mfma_f32_32x32x16_bf16 v[144:159], v[88:91], v[204:207], v[144:159]
ds_read_b64_tr_b16 v[196:197], v235 offset:12288
ds_read_b64_tr_b16 v[198:199], v235 offset:12800
v_mov_b32 v190, v250
v_mov_b32 v191, v250
s_waitcnt lgkmcnt(6)
v_mfma_f32_32x32x16_bf16 v[128:143], v[92:95], v[208:211], v[128:143]
ds_read_b64_tr_b16 v[200:201], v234 offset:14336
ds_read_b64_tr_b16 v[202:203], v234 offset:14848
v_cvt_pk_bf16_f32 v96, v160, v161
v_cvt_pk_bf16_f32 v97, v162, v163
v_cvt_pk_bf16_f32 v98, v164, v165
v_cvt_pk_bf16_f32 v99, v166, v167
v_mfma_f32_32x32x16_bf16 v[144:159], v[92:95], v[212:215], v[144:159]
ds_read_b64_tr_b16 v[204:205], v235 offset:14336
ds_read_b64_tr_b16 v[206:207], v235 offset:14848
v_cvt_pk_bf16_f32 v100, v168, v169
v_cvt_pk_bf16_f32 v101, v170, v171
v_cvt_pk_bf16_f32 v102, v172, v173
v_cvt_pk_bf16_f32 v103, v174, v175
s_waitcnt lgkmcnt(8)
v_smfmac_f32_32x32x32_bf16 v[176:191], v[216:219], v[104:111], v220
s_waitcnt lgkmcnt(4)
ds_read_b128 v[160:163], v221
ds_read_b128 v[164:167], v221
v_mov_b32 v168, v250
v_mov_b32 v169, v250
v_mov_b32 v170, v250
v_mfma_f32_32x32x16_bf16 v[128:143], v[96:99], v[192:195], v[128:143]
ds_read_b64_tr_b16 v[208:209], v234 offset:28672
ds_read_b64_tr_b16 v[210:211], v234 offset:29184
v_mov_b32 v171, v250
v_mov_b32 v172, v250
v_mov_b32 v173, v250
v_mfma_f32_32x32x16_bf16 v[144:159], v[96:99], v[196:199], v[144:159]
ds_read_b64_tr_b16 v[212:213], v235 offset:28672
ds_read_b64_tr_b16 v[214:215], v235 offset:29184
v_mov_b32 v174, v250
v_mov_b32 v175, v250
s_waitcnt lgkmcnt(6)
v_mfma_f32_32x32x16_bf16 v[128:143], v[100:103], v[200:203], v[128:143]
ds_read_b64_tr_b16 v[192:193], v234 offset:30720
ds_read_b64_tr_b16 v[194:195], v234 offset:31232
v_cvt_pk_bf16_f32 v104, v176, v177
v_cvt_pk_bf16_f32 v105, v178, v179
v_cvt_pk_bf16_f32 v106, v180, v181
v_cvt_pk_bf16_f32 v107, v182, v183
v_mfma_f32_32x32x16_bf16 v[144:159], v[100:103], v[204:207], v[144:159]
ds_read_b64_tr_b16 v[196:197], v235 offset:30720
ds_read_b64_tr_b16 v[198:199], v235 offset:31232
v_cvt_pk_bf16_f32 v108, v184, v185
v_cvt_pk_bf16_f32 v109, v186, v187
v_cvt_pk_bf16_f32 v110, v188, v189
v_cvt_pk_bf16_f32 v111, v190, v191
s_waitcnt lgkmcnt(8)
v_smfmac_f32_32x32x32_bf16 v[160:175], v[216:219], v[112:119], v220
s_waitcnt lgkmcnt(4)
ds_read_b128 v[176:179], v221
ds_read_b128 v[180:183], v221
v_mov_b32 v184, v250
v_mov_b32 v185, v250
v_mov_b32 v186, v250
v_mfma_f32_32x32x16_bf16 v[128:143], v[104:107], v[208:211], v[128:143]
ds_read_b64_tr_b16 v[200:201], v234 offset:45056
ds_read_b64_tr_b16 v[202:203], v234 offset:45568
v_mov_b32 v187, v250
v_mov_b32 v188, v250
v_mov_b32 v189, v250
v_mfma_f32_32x32x16_bf16 v[144:159], v[104:107], v[212:215], v[144:159]
ds_read_b64_tr_b16 v[204:205], v235 offset:45056
ds_read_b64_tr_b16 v[206:207], v235 offset:45568
v_mov_b32 v190, v250
v_mov_b32 v191, v250
s_waitcnt lgkmcnt(6)
v_mfma_f32_32x32x16_bf16 v[128:143], v[108:111], v[192:195], v[128:143]
ds_read_b64_tr_b16 v[208:209], v234 offset:47104
ds_read_b64_tr_b16 v[210:211], v234 offset:47616
v_cvt_pk_bf16_f32 v112, v160, v161
v_cvt_pk_bf16_f32 v113, v162, v163
v_cvt_pk_bf16_f32 v114, v164, v165
v_cvt_pk_bf16_f32 v115, v166, v167
v_mfma_f32_32x32x16_bf16 v[144:159], v[108:111], v[196:199], v[144:159]
ds_read_b64_tr_b16 v[212:213], v235 offset:47104
ds_read_b64_tr_b16 v[214:215], v235 offset:47616
v_cvt_pk_bf16_f32 v116, v168, v169
v_cvt_pk_bf16_f32 v117, v170, v171
v_cvt_pk_bf16_f32 v118, v172, v173
v_cvt_pk_bf16_f32 v119, v174, v175
s_waitcnt lgkmcnt(8)
v_smfmac_f32_32x32x32_bf16 v[176:191], v[216:219], v[120:127], v220
s_waitcnt lgkmcnt(4)
v_mfma_f32_32x32x16_bf16 v[128:143], v[112:115], v[200:203], v[128:143]
ds_read_b64_tr_b16 v[192:193], v234 offset:61440
ds_read_b64_tr_b16 v[194:195], v234 offset:61952
v_mfma_f32_32x32x16_bf16 v[144:159], v[112:115], v[204:207], v[144:159]
ds_read_b64_tr_b16 v[196:197], v235 offset:61440
ds_read_b64_tr_b16 v[198:199], v235 offset:61952
s_waitcnt lgkmcnt(4)
v_mfma_f32_32x32x16_bf16 v[128:143], v[116:119], v[208:211], v[128:143]
ds_read_b64_tr_b16 v[200:201], v234 offset:63488
ds_read_b64_tr_b16 v[202:203], v234 offset:64000
s_nop 0
v_cvt_pk_bf16_f32 v120, v176, v177
v_cvt_pk_bf16_f32 v121, v178, v179
v_cvt_pk_bf16_f32 v122, v180, v181
v_cvt_pk_bf16_f32 v123, v182, v183
v_mfma_f32_32x32x16_bf16 v[144:159], v[116:119], v[212:215], v[144:159]
ds_read_b64_tr_b16 v[204:205], v235 offset:63488
ds_read_b64_tr_b16 v[206:207], v235 offset:64000
v_cvt_pk_bf16_f32 v124, v184, v185
v_cvt_pk_bf16_f32 v125, v186, v187
v_cvt_pk_bf16_f32 v126, v188, v189
v_cvt_pk_bf16_f32 v127, v190, v191
s_waitcnt lgkmcnt(4)
v_mfma_f32_32x32x16_bf16 v[128:143], v[120:123], v[192:195], v[128:143]
ds_read_b128 v[160:163], v249 offset:0
ds_read_b128 v[164:167], v249 offset:32
ds_read_b128 v[168:171], v249 offset:64
ds_read_b128 v[172:175], v249 offset:96
v_mfma_f32_32x32x16_bf16 v[144:159], v[120:123], v[196:199], v[144:159]
s_waitcnt lgkmcnt(4)
v_mfma_f32_32x32x16_bf16 v[128:143], v[124:127], v[200:203], v[128:143]
v_mfma_f32_32x32x16_bf16 v[144:159], v[124:127], v[204:207], v[144:159]
s_waitcnt lgkmcnt(0)
s_add_u32 s68, s99, 0
s_nop 8
v_pk_mul_f32 v[128:129], v[128:129], v[160:161]
v_pk_mul_f32 v[130:131], v[130:131], v[162:163]
v_pk_mul_f32 v[132:133], v[132:133], v[164:165]
v_pk_mul_f32 v[134:135], v[134:135], v[166:167]
v_cvt_pk_bf16_f32 v128, v128, v129
v_cvt_pk_bf16_f32 v129, v130, v131
v_cvt_pk_bf16_f32 v130, v132, v133
v_cvt_pk_bf16_f32 v131, v134, v135
buffer_store_dwordx4 v[128:131], v243, s[48:51], s68 offen offset:1024 sc1
s_add_u32 s68, s99, 0
v_pk_mul_f32 v[136:137], v[136:137], v[168:169]
v_pk_mul_f32 v[138:139], v[138:139], v[170:171]
v_pk_mul_f32 v[140:141], v[140:141], v[172:173]
v_pk_mul_f32 v[142:143], v[142:143], v[174:175]
v_cvt_pk_bf16_f32 v136, v136, v137
v_cvt_pk_bf16_f32 v137, v138, v139
v_cvt_pk_bf16_f32 v138, v140, v141
v_cvt_pk_bf16_f32 v139, v142, v143
buffer_store_dwordx4 v[136:139], v243, s[48:51], s68 offen offset:1536 sc1
s_add_u32 s68, s99, 262144
v_pk_mul_f32 v[144:145], v[144:145], v[160:161]
v_pk_mul_f32 v[146:147], v[146:147], v[162:163]
v_pk_mul_f32 v[148:149], v[148:149], v[164:165]
v_pk_mul_f32 v[150:151], v[150:151], v[166:167]
v_cvt_pk_bf16_f32 v144, v144, v145
v_cvt_pk_bf16_f32 v145, v146, v147
v_cvt_pk_bf16_f32 v146, v148, v149
v_cvt_pk_bf16_f32 v147, v150, v151
buffer_store_dwordx4 v[144:147], v243, s[48:51], s68 offen offset:1024 sc1
s_add_u32 s68, s99, 262144
v_pk_mul_f32 v[152:153], v[152:153], v[168:169]
v_pk_mul_f32 v[154:155], v[154:155], v[170:171]
v_pk_mul_f32 v[156:157], v[156:157], v[172:173]
v_pk_mul_f32 v[158:159], v[158:159], v[174:175]
v_cvt_pk_bf16_f32 v152, v152, v153
v_cvt_pk_bf16_f32 v153, v154, v155
v_cvt_pk_bf16_f32 v154, v156, v157
v_cvt_pk_bf16_f32 v155, v158, v159
buffer_store_dwordx4 v[152:155], v243, s[48:51], s68 offen offset:1536 sc1
.Lk_qproj:
s_barrier
v_mov_b32 v227, 163328
)ASM" R"ASM(
ds_read_b64 v[248:249], v227
ds_read_b64_tr_b16 v[192:193], v238 offset:0
ds_read_b64_tr_b16 v[194:195], v238 offset:256
ds_read_b64_tr_b16 v[196:197], v238 offset:1024
ds_read_b64_tr_b16 v[198:199], v238 offset:1280
ds_read_b64_tr_b16 v[200:201], v238 offset:16384
ds_read_b64_tr_b16 v[202:203], v238 offset:16640
ds_read_b64_tr_b16 v[204:205], v238 offset:17408
ds_read_b64_tr_b16 v[206:207], v238 offset:17664
s_waitcnt lgkmcnt(4)
v_mfma_f32_32x32x16_bf16 v[160:175], v[192:195], v[0:3], 0
ds_read_b64_tr_b16 v[208:209], v238 offset:32768
ds_read_b64_tr_b16 v[210:211], v238 offset:33024
v_mfma_f32_32x32x16_bf16 v[160:175], v[196:199], v[4:7], v[160:175]
ds_read_b64_tr_b16 v[212:213], v238 offset:33792
ds_read_b64_tr_b16 v[214:215], v238 offset:34048
s_waitcnt lgkmcnt(4)
v_mfma_f32_32x32x16_bf16 v[160:175], v[200:203], v[8:11], v[160:175]
ds_read_b64_tr_b16 v[216:217], v238 offset:49152
ds_read_b64_tr_b16 v[218:219], v238 offset:49408
v_mfma_f32_32x32x16_bf16 v[160:175], v[204:207], v[12:15], v[160:175]
ds_read_b64_tr_b16 v[220:221], v238 offset:50176
ds_read_b64_tr_b16 v[222:223], v238 offset:50432
s_waitcnt lgkmcnt(4)
v_mfma_f32_32x32x16_bf16 v[160:175], v[208:211], v[16:19], v[160:175]
ds_read_b64_tr_b16 v[192:193], v238 offset:2048
ds_read_b64_tr_b16 v[194:195], v238 offset:2304
v_mfma_f32_32x32x16_bf16 v[160:175], v[212:215], v[20:23], v[160:175]
ds_read_b64_tr_b16 v[196:197], v238 offset:3072
ds_read_b64_tr_b16 v[198:199], v238 offset:3328
s_waitcnt lgkmcnt(4)
v_mfma_f32_32x32x16_bf16 v[160:175], v[216:219], v[24:27], v[160:175]
ds_read_b64_tr_b16 v[200:201], v238 offset:18432
ds_read_b64_tr_b16 v[202:203], v238 offset:18688
v_mfma_f32_32x32x16_bf16 v[160:175], v[220:223], v[28:31], v[160:175]
ds_read_b64_tr_b16 v[204:205], v238 offset:19456
ds_read_b64_tr_b16 v[206:207], v238 offset:19712
s_waitcnt lgkmcnt(4)
v_mfma_f32_32x32x16_bf16 v[160:175], v[192:195], v[32:35], v[160:175]
ds_read_b64_tr_b16 v[208:209], v238 offset:34816
ds_read_b64_tr_b16 v[210:211], v238 offset:35072
v_mfma_f32_32x32x16_bf16 v[160:175], v[196:199], v[36:39], v[160:175]
ds_read_b64_tr_b16 v[212:213], v238 offset:35840
ds_read_b64_tr_b16 v[214:215], v238 offset:36096
s_waitcnt lgkmcnt(4)
v_mfma_f32_32x32x16_bf16 v[160:175], v[200:203], v[40:43], v[160:175]
ds_read_b64_tr_b16 v[216:217], v238 offset:51200
ds_read_b64_tr_b16 v[218:219], v238 offset:51456
s_waitcnt vmcnt(0)
s_mov_b64 exec, 1
buffer_store_dwordx2 v[248:249], off, s[48:51], s15 offset:0 sc1
s_mov_b64 exec, -1
ds_add_u32 v245, v246 offset:32512
v_mfma_f32_32x32x16_bf16 v[160:175], v[204:207], v[44:47], v[160:175]
ds_read_b64_tr_b16 v[220:221], v238 offset:52224
ds_read_b64_tr_b16 v[222:223], v238 offset:52480
s_waitcnt lgkmcnt(5)
v_mfma_f32_32x32x16_bf16 v[160:175], v[208:211], v[48:51], v[160:175]
ds_read_b64_tr_b16 v[192:193], v238 offset:4096
ds_read_b64_tr_b16 v[194:195], v238 offset:4352
v_mfma_f32_32x32x16_bf16 v[160:175], v[212:215], v[52:55], v[160:175]
ds_read_b64_tr_b16 v[196:197], v238 offset:5120
ds_read_b64_tr_b16 v[198:199], v238 offset:5376
s_waitcnt lgkmcnt(4)
v_mfma_f32_32x32x16_bf16 v[160:175], v[216:219], v[56:59], v[160:175]
ds_read_b64_tr_b16 v[200:201], v238 offset:20480
ds_read_b64_tr_b16 v[202:203], v238 offset:20736
v_mfma_f32_32x32x16_bf16 v[160:175], v[220:223], v[60:63], v[160:175]
ds_read_b64_tr_b16 v[204:205], v238 offset:21504
ds_read_b64_tr_b16 v[206:207], v238 offset:21760
s_waitcnt lgkmcnt(4)
v_mfma_f32_32x32x16_bf16 v[160:175], v[192:195], v[64:67], v[160:175]
ds_read_b64_tr_b16 v[208:209], v238 offset:36864
ds_read_b64_tr_b16 v[210:211], v238 offset:37120
v_mfma_f32_32x32x16_bf16 v[160:175], v[196:199], v[68:71], v[160:175]
ds_read_b64_tr_b16 v[212:213], v238 offset:37888
ds_read_b64_tr_b16 v[214:215], v238 offset:38144
s_waitcnt lgkmcnt(4)
v_mfma_f32_32x32x16_bf16 v[160:175], v[200:203], v[72:75], v[160:175]
ds_read_b64_tr_b16 v[216:217], v238 offset:53248
ds_read_b64_tr_b16 v[218:219], v238 offset:53504
v_mfma_f32_32x32x16_bf16 v[160:175], v[204:207], v[76:79], v[160:175]
ds_read_b64_tr_b16 v[220:221], v238 offset:54272
ds_read_b64_tr_b16 v[222:223], v238 offset:54528
s_waitcnt lgkmcnt(4)
v_mfma_f32_32x32x16_bf16 v[160:175], v[208:211], v[80:83], v[160:175]
ds_read_b64_tr_b16 v[192:193], v238 offset:6144
ds_read_b64_tr_b16 v[194:195], v238 offset:6400
v_mfma_f32_32x32x16_bf16 v[160:175], v[212:215], v[84:87], v[160:175]
ds_read_b64_tr_b16 v[196:197], v238 offset:7168
ds_read_b64_tr_b16 v[198:199], v238 offset:7424
s_waitcnt lgkmcnt(4)
v_mfma_f32_32x32x16_bf16 v[160:175], v[216:219], v[88:91], v[160:175]
ds_read_b64_tr_b16 v[200:201], v238 offset:22528
ds_read_b64_tr_b16 v[202:203], v238 offset:22784
v_mfma_f32_32x32x16_bf16 v[160:175], v[220:223], v[92:95], v[160:175]
ds_read_b64_tr_b16 v[204:205], v238 offset:23552
ds_read_b64_tr_b16 v[206:207], v238 offset:23808
s_waitcnt lgkmcnt(4)
v_mfma_f32_32x32x16_bf16 v[160:175], v[192:195], v[96:99], v[160:175]
ds_read_b64_tr_b16 v[208:209], v238 offset:38912
ds_read_b64_tr_b16 v[210:211], v238 offset:39168
v_mfma_f32_32x32x16_bf16 v[160:175], v[196:199], v[100:103], v[160:175]
ds_read_b64_tr_b16 v[212:213], v238 offset:39936
ds_read_b64_tr_b16 v[214:215], v238 offset:40192
s_waitcnt lgkmcnt(4)
v_mfma_f32_32x32x16_bf16 v[160:175], v[200:203], v[104:107], v[160:175]
ds_read_b64_tr_b16 v[216:217], v238 offset:55296
ds_read_b64_tr_b16 v[218:219], v238 offset:55552
v_mfma_f32_32x32x16_bf16 v[160:175], v[204:207], v[108:111], v[160:175]
ds_read_b64_tr_b16 v[220:221], v238 offset:56320
ds_read_b64_tr_b16 v[222:223], v238 offset:56576
s_waitcnt lgkmcnt(4)
v_mfma_f32_32x32x16_bf16 v[160:175], v[208:211], v[112:115], v[160:175]
v_mfma_f32_32x32x16_bf16 v[160:175], v[212:215], v[116:119], v[160:175]
s_waitcnt lgkmcnt(0)
v_mfma_f32_32x32x16_bf16 v[160:175], v[216:219], v[120:123], v[160:175]
v_mfma_f32_32x32x16_bf16 v[160:175], v[220:223], v[124:127], v[160:175]
v_mul_f32 v250, 0x3eb8aa3b, v228
v_mov_b32 v251, v250
s_lshl_b32 s62, s53, 10
s_add_u32 s62, s62, 131072
v_add_u32 v227, s62, v244
v_subrev_u32 v239, 0x10000, v238
ds_read_b32 v252, v245 offset:29184
s_nop 4
v_pk_mul_f32 v[160:161], v[160:161], v[250:251]
v_pk_mul_f32 v[162:163], v[162:163], v[250:251]
v_pk_mul_f32 v[164:165], v[164:165], v[250:251]
v_pk_mul_f32 v[166:167], v[166:167], v[250:251]
v_cvt_pk_bf16_f32 v160, v160, v161
v_cvt_pk_bf16_f32 v161, v162, v163
v_cvt_pk_bf16_f32 v162, v164, v165
v_cvt_pk_bf16_f32 v163, v166, v167
ds_write_b128 v227, v[160:163] offset:0
v_pk_mul_f32 v[168:169], v[168:169], v[250:251]
v_pk_mul_f32 v[170:171], v[170:171], v[250:251]
v_pk_mul_f32 v[172:173], v[172:173], v[250:251]
v_pk_mul_f32 v[174:175], v[174:175], v[250:251]
v_cvt_pk_bf16_f32 v168, v168, v169
v_cvt_pk_bf16_f32 v169, v170, v171
v_cvt_pk_bf16_f32 v170, v172, v173
v_cvt_pk_bf16_f32 v171, v174, v175
ds_write_b128 v227, v[168:171] offset:4096
ds_add_u32 v245, v246 offset:28928
s_waitcnt lgkmcnt(3)
v_cmp_gt_u32 vcc, 4, v252
s_nop 0
s_nop 0
s_cbranch_vccnz .Lk_ls_15
.Lk_ld_16:
ds_read_b64_tr_b16 v[192:193], v236 offset:8192
ds_read_b64_tr_b16 v[194:195], v236 offset:8704
ds_read_b64_tr_b16 v[196:197], v237 offset:8192
ds_read_b64_tr_b16 v[198:199], v237 offset:8704
ds_read_b64_tr_b16 v[200:201], v236 offset:12288
ds_read_b64_tr_b16 v[202:203], v236 offset:12800
ds_read_b64_tr_b16 v[204:205], v237 offset:12288
ds_read_b64_tr_b16 v[206:207], v237 offset:12800
s_bitcmp1_b32 s55, 1
s_cbranch_scc1 .Lk_light
ds_add_u32 v245, v246 offset:28672
s_waitcnt lgkmcnt(7)
v_mfma_f32_32x32x16_bf16 v[128:143], v[192:195], v[0:3], 0
ds_read_b64_tr_b16 v[208:209], v236 offset:10240
ds_read_b64_tr_b16 v[210:211], v236 offset:10752
s_waitcnt lgkmcnt(7)
v_mfma_f32_32x32x16_bf16 v[144:159], v[196:199], v[0:3], 0
ds_read_b64_tr_b16 v[212:213], v237 offset:10240
ds_read_b64_tr_b16 v[214:215], v237 offset:10752
s_waitcnt lgkmcnt(7)
v_mfma_f32_32x32x16_bf16 v[160:175], v[200:203], v[0:3], 0
ds_read_b64_tr_b16 v[216:217], v236 offset:14336
ds_read_b64_tr_b16 v[218:219], v236 offset:14848
s_waitcnt lgkmcnt(7)
v_mfma_f32_32x32x16_bf16 v[176:191], v[204:207], v[0:3], 0
ds_read_b64_tr_b16 v[220:221], v237 offset:14336
ds_read_b64_tr_b16 v[222:223], v237 offset:14848
s_waitcnt lgkmcnt(6)
v_mfma_f32_32x32x16_bf16 v[128:143], v[208:211], v[4:7], v[128:143]
ds_read_b64_tr_b16 v[192:193], v236 offset:24576
ds_read_b64_tr_b16 v[194:195], v236 offset:25088
s_waitcnt lgkmcnt(6)
v_mfma_f32_32x32x16_bf16 v[144:159], v[212:215], v[4:7], v[144:159]
ds_read_b64_tr_b16 v[196:197], v237 offset:24576
ds_read_b64_tr_b16 v[198:199], v237 offset:25088
s_waitcnt lgkmcnt(6)
v_mfma_f32_32x32x16_bf16 v[160:175], v[216:219], v[4:7], v[160:175]
ds_read_b64_tr_b16 v[200:201], v236 offset:28672
ds_read_b64_tr_b16 v[202:203], v236 offset:29184
s_waitcnt lgkmcnt(6)
v_mfma_f32_32x32x16_bf16 v[176:191], v[220:223], v[4:7], v[176:191]
ds_read_b64_tr_b16 v[204:205], v237 offset:28672
ds_read_b64_tr_b16 v[206:207], v237 offset:29184
ds_read_b32 v227, v245 offset:28672
s_waitcnt lgkmcnt(7)
v_mfma_f32_32x32x16_bf16 v[128:143], v[192:195], v[8:11], v[128:143]
ds_read_b64_tr_b16 v[208:209], v236 offset:26624
ds_read_b64_tr_b16 v[210:211], v236 offset:27136
s_waitcnt lgkmcnt(7)
)ASM" R"ASM(
v_mfma_f32_32x32x16_bf16 v[144:159], v[196:199], v[8:11], v[144:159]
s_waitcnt lgkmcnt(2)
s_setprio 3
v_cmp_gt_u32 vcc, 4, v227
s_setprio 0
s_nop 0
s_cbranch_vccnz .Lk_ls_17
.Lk_ld_18:
ds_read_b64_tr_b16 v[212:213], v237 offset:26624
ds_read_b64_tr_b16 v[214:215], v237 offset:27136
v_mfma_f32_32x32x16_bf16 v[160:175], v[200:203], v[8:11], v[160:175]
ds_read_b64_tr_b16 v[216:217], v236 offset:30720
ds_read_b64_tr_b16 v[218:219], v236 offset:31232
v_mfma_f32_32x32x16_bf16 v[176:191], v[204:207], v[8:11], v[176:191]
ds_read_b64_tr_b16 v[220:221], v237 offset:30720
ds_read_b64_tr_b16 v[222:223], v237 offset:31232
s_waitcnt lgkmcnt(6)
v_mfma_f32_32x32x16_bf16 v[128:143], v[208:211], v[12:15], v[128:143]
ds_read_b64_tr_b16 v[192:193], v236 offset:40960
ds_read_b64_tr_b16 v[194:195], v236 offset:41472
s_waitcnt lgkmcnt(6)
v_mfma_f32_32x32x16_bf16 v[144:159], v[212:215], v[12:15], v[144:159]
ds_read_b64_tr_b16 v[196:197], v237 offset:40960
ds_read_b64_tr_b16 v[198:199], v237 offset:41472
s_waitcnt lgkmcnt(6)
v_mfma_f32_32x32x16_bf16 v[160:175], v[216:219], v[12:15], v[160:175]
ds_read_b64_tr_b16 v[200:201], v236 offset:45056
ds_read_b64_tr_b16 v[202:203], v236 offset:45568
s_waitcnt lgkmcnt(6)
v_mfma_f32_32x32x16_bf16 v[176:191], v[220:223], v[12:15], v[176:191]
ds_read_b64_tr_b16 v[204:205], v237 offset:45056
ds_read_b64_tr_b16 v[206:207], v237 offset:45568
s_waitcnt lgkmcnt(6)
v_mfma_f32_32x32x16_bf16 v[128:143], v[192:195], v[16:19], v[128:143]
ds_read_b64_tr_b16 v[208:209], v236 offset:43008
ds_read_b64_tr_b16 v[210:211], v236 offset:43520
s_waitcnt lgkmcnt(6)
v_mfma_f32_32x32x16_bf16 v[144:159], v[196:199], v[16:19], v[144:159]
ds_read_b64_tr_b16 v[212:213], v237 offset:43008
ds_read_b64_tr_b16 v[214:215], v237 offset:43520
s_waitcnt lgkmcnt(6)
v_mfma_f32_32x32x16_bf16 v[160:175], v[200:203], v[16:19], v[160:175]
ds_read_b64_tr_b16 v[216:217], v236 offset:47104
ds_read_b64_tr_b16 v[218:219], v236 offset:47616
s_waitcnt lgkmcnt(6)
v_mfma_f32_32x32x16_bf16 v[176:191], v[204:207], v[16:19], v[176:191]
ds_read_b64_tr_b16 v[220:221], v237 offset:47104
ds_read_b64_tr_b16 v[222:223], v237 offset:47616
s_waitcnt lgkmcnt(6)
v_mfma_f32_32x32x16_bf16 v[128:143], v[208:211], v[20:23], v[128:143]
ds_read_b64_tr_b16 v[192:193], v236 offset:57344
ds_read_b64_tr_b16 v[194:195], v236 offset:57856
s_waitcnt lgkmcnt(6)
v_mfma_f32_32x32x16_bf16 v[144:159], v[212:215], v[20:23], v[144:159]
ds_read_b64_tr_b16 v[196:197], v237 offset:57344
ds_read_b64_tr_b16 v[198:199], v237 offset:57856
s_waitcnt lgkmcnt(6)
v_mfma_f32_32x32x16_bf16 v[160:175], v[216:219], v[20:23], v[160:175]
ds_read_b64_tr_b16 v[200:201], v236 offset:61440
ds_read_b64_tr_b16 v[202:203], v236 offset:61952
s_waitcnt lgkmcnt(6)
v_mfma_f32_32x32x16_bf16 v[176:191], v[220:223], v[20:23], v[176:191]
ds_read_b64_tr_b16 v[204:205], v237 offset:61440
ds_read_b64_tr_b16 v[206:207], v237 offset:61952
s_waitcnt lgkmcnt(6)
v_mfma_f32_32x32x16_bf16 v[128:143], v[192:195], v[24:27], v[128:143]
ds_read_b64_tr_b16 v[208:209], v236 offset:59392
ds_read_b64_tr_b16 v[210:211], v236 offset:59904
s_waitcnt lgkmcnt(6)
v_mfma_f32_32x32x16_bf16 v[144:159], v[196:199], v[24:27], v[144:159]
ds_read_b64_tr_b16 v[212:213], v237 offset:59392
ds_read_b64_tr_b16 v[214:215], v237 offset:59904
s_waitcnt lgkmcnt(6)
v_mfma_f32_32x32x16_bf16 v[160:175], v[200:203], v[24:27], v[160:175]
ds_read_b32 v227, v245 offset:29440
ds_read_b64_tr_b16 v[216:217], v236 offset:63488
ds_read_b64_tr_b16 v[218:219], v236 offset:64000
s_waitcnt lgkmcnt(7)
v_mfma_f32_32x32x16_bf16 v[176:191], v[204:207], v[24:27], v[176:191]
ds_read_b64_tr_b16 v[220:221], v237 offset:63488
ds_read_b64_tr_b16 v[222:223], v237 offset:64000
s_waitcnt lgkmcnt(7)
v_mfma_f32_32x32x16_bf16 v[128:143], v[208:211], v[28:31], v[128:143]
s_waitcnt lgkmcnt(4)
s_setprio 3
v_cmp_gt_u32 vcc, 4, v227
s_setprio 0
s_nop 0
s_cbranch_vccnz .Lk_ls_19
.Lk_ld_20:
ds_read_b64_tr_b16 v[192:193], v234 offset:0
ds_read_b64_tr_b16 v[194:195], v234 offset:512
v_mfma_f32_32x32x16_bf16 v[144:159], v[212:215], v[28:31], v[144:159]
ds_read_b64_tr_b16 v[196:197], v235 offset:0
ds_read_b64_tr_b16 v[198:199], v235 offset:512
s_waitcnt lgkmcnt(6)
v_mfma_f32_32x32x16_bf16 v[160:175], v[216:219], v[28:31], v[160:175]
ds_read_b64_tr_b16 v[200:201], v234 offset:4096
ds_read_b64_tr_b16 v[202:203], v234 offset:4608
s_waitcnt lgkmcnt(6)
v_mfma_f32_32x32x16_bf16 v[176:191], v[220:223], v[28:31], v[176:191]
ds_read_b64_tr_b16 v[204:205], v235 offset:4096
ds_read_b64_tr_b16 v[206:207], v235 offset:4608
s_waitcnt lgkmcnt(6)
v_mfma_f32_32x32x16_bf16 v[128:143], v[192:195], v[32:35], v[128:143]
ds_read_b64_tr_b16 v[208:209], v234 offset:2048
ds_read_b64_tr_b16 v[210:211], v234 offset:2560
s_waitcnt lgkmcnt(6)
v_mfma_f32_32x32x16_bf16 v[144:159], v[196:199], v[32:35], v[144:159]
ds_read_b64_tr_b16 v[212:213], v235 offset:2048
ds_read_b64_tr_b16 v[214:215], v235 offset:2560
s_add_u32 m0, s96, 8192
s_add_u32 s62, s94, 0
buffer_load_dwordx4 v240, s[28:31], s62 offen  lds
s_add_u32 m0, s96, 9216
s_add_u32 s62, s94, 28672
buffer_load_dwordx4 v240, s[28:31], s62 offen  lds
s_waitcnt lgkmcnt(6)
v_mfma_f32_32x32x16_bf16 v[160:175], v[200:203], v[32:35], v[160:175]
ds_read_b64_tr_b16 v[216:217], v234 offset:6144
ds_read_b64_tr_b16 v[218:219], v234 offset:6656
s_waitcnt lgkmcnt(6)
v_mfma_f32_32x32x16_bf16 v[176:191], v[204:207], v[32:35], v[176:191]
ds_read_b64_tr_b16 v[220:221], v235 offset:6144
ds_read_b64_tr_b16 v[222:223], v235 offset:6656
s_add_u32 m0, s96, 10240
s_add_u32 s62, s94, 57344
buffer_load_dwordx4 v240, s[28:31], s62 offen  lds
s_add_u32 m0, s96, 11264
s_add_u32 s62, s94, 86016
buffer_load_dwordx4 v240, s[28:31], s62 offen  lds
s_waitcnt lgkmcnt(6)
v_mfma_f32_32x32x16_bf16 v[128:143], v[208:211], v[36:39], v[128:143]
ds_read_b64_tr_b16 v[192:193], v234 offset:16384
ds_read_b64_tr_b16 v[194:195], v234 offset:16896
s_waitcnt lgkmcnt(6)
v_mfma_f32_32x32x16_bf16 v[144:159], v[212:215], v[36:39], v[144:159]
ds_read_b64_tr_b16 v[196:197], v235 offset:16384
ds_read_b64_tr_b16 v[198:199], v235 offset:16896
s_add_u32 m0, s96, 12288
s_add_u32 s62, s94, 0
buffer_load_dwordx4 v240, s[32:35], s62 offen  lds
s_add_u32 m0, s96, 13312
s_add_u32 s62, s94, 28672
buffer_load_dwordx4 v240, s[32:35], s62 offen  lds
s_waitcnt lgkmcnt(6)
v_mfma_f32_32x32x16_bf16 v[160:175], v[216:219], v[36:39], v[160:175]
ds_read_b64_tr_b16 v[200:201], v234 offset:20480
ds_read_b64_tr_b16 v[202:203], v234 offset:20992
s_waitcnt lgkmcnt(6)
v_mfma_f32_32x32x16_bf16 v[176:191], v[220:223], v[36:39], v[176:191]
ds_read_b64_tr_b16 v[204:205], v235 offset:20480
ds_read_b64_tr_b16 v[206:207], v235 offset:20992
s_add_u32 m0, s96, 14336
s_add_u32 s62, s94, 57344
buffer_load_dwordx4 v240, s[32:35], s62 offen  lds
s_add_u32 m0, s96, 15360
s_add_u32 s62, s94, 86016
buffer_load_dwordx4 v240, s[32:35], s62 offen  lds
s_waitcnt lgkmcnt(6)
v_mfma_f32_32x32x16_bf16 v[128:143], v[192:195], v[40:43], v[128:143]
ds_read_b64_tr_b16 v[208:209], v234 offset:18432
ds_read_b64_tr_b16 v[210:211], v234 offset:18944
s_waitcnt lgkmcnt(6)
v_mfma_f32_32x32x16_bf16 v[144:159], v[196:199], v[40:43], v[144:159]
ds_read_b64_tr_b16 v[212:213], v235 offset:18432
ds_read_b64_tr_b16 v[214:215], v235 offset:18944
s_add_u32 m0, s96, 65536
s_add_u32 s62, s95, 0
buffer_load_dwordx4 v240, s[28:31], s62 offen  lds
s_add_u32 m0, s96, 66560
s_add_u32 s62, s95, 28672
buffer_load_dwordx4 v240, s[28:31], s62 offen  lds
s_waitcnt lgkmcnt(6)
v_mfma_f32_32x32x16_bf16 v[160:175], v[200:203], v[40:43], v[160:175]
ds_read_b64_tr_b16 v[216:217], v234 offset:22528
ds_read_b64_tr_b16 v[218:219], v234 offset:23040
s_waitcnt lgkmcnt(6)
v_mfma_f32_32x32x16_bf16 v[176:191], v[204:207], v[40:43], v[176:191]
ds_read_b64_tr_b16 v[220:221], v235 offset:22528
ds_read_b64_tr_b16 v[222:223], v235 offset:23040
s_add_u32 m0, s96, 67584
s_add_u32 s62, s95, 57344
buffer_load_dwordx4 v240, s[28:31], s62 offen  lds
s_add_u32 m0, s96, 68608
s_add_u32 s62, s95, 86016
buffer_load_dwordx4 v240, s[28:31], s62 offen  lds
s_waitcnt lgkmcnt(6)
v_mfma_f32_32x32x16_bf16 v[128:143], v[208:211], v[44:47], v[128:143]
ds_read_b64_tr_b16 v[192:193], v234 offset:32768
ds_read_b64_tr_b16 v[194:195], v234 offset:33280
s_waitcnt lgkmcnt(6)
v_mfma_f32_32x32x16_bf16 v[144:159], v[212:215], v[44:47], v[144:159]
ds_read_b64_tr_b16 v[196:197], v235 offset:32768
ds_read_b64_tr_b16 v[198:199], v235 offset:33280
s_add_u32 m0, s96, 69632
s_add_u32 s62, s95, 0
buffer_load_dwordx4 v240, s[32:35], s62 offen  lds
s_add_u32 m0, s96, 70656
s_add_u32 s62, s95, 28672
)ASM" R"ASM(
buffer_load_dwordx4 v240, s[32:35], s62 offen  lds
s_waitcnt lgkmcnt(6)
v_mfma_f32_32x32x16_bf16 v[160:175], v[216:219], v[44:47], v[160:175]
ds_read_b64_tr_b16 v[200:201], v234 offset:36864
ds_read_b64_tr_b16 v[202:203], v234 offset:37376
s_waitcnt lgkmcnt(6)
v_mfma_f32_32x32x16_bf16 v[176:191], v[220:223], v[44:47], v[176:191]
ds_read_b64_tr_b16 v[204:205], v235 offset:36864
ds_read_b64_tr_b16 v[206:207], v235 offset:37376
s_add_u32 m0, s96, 71680
s_add_u32 s62, s95, 57344
buffer_load_dwordx4 v240, s[32:35], s62 offen  lds
s_add_u32 m0, s96, 72704
s_add_u32 s62, s95, 86016
buffer_load_dwordx4 v240, s[32:35], s62 offen  lds
s_waitcnt lgkmcnt(6)
v_mfma_f32_32x32x16_bf16 v[128:143], v[192:195], v[48:51], v[128:143]
ds_read_b64_tr_b16 v[208:209], v234 offset:34816
ds_read_b64_tr_b16 v[210:211], v234 offset:35328
s_waitcnt lgkmcnt(6)
v_mfma_f32_32x32x16_bf16 v[144:159], v[196:199], v[48:51], v[144:159]
ds_read_b64_tr_b16 v[212:213], v235 offset:34816
ds_read_b64_tr_b16 v[214:215], v235 offset:35328
s_waitcnt lgkmcnt(6)
v_mfma_f32_32x32x16_bf16 v[160:175], v[200:203], v[48:51], v[160:175]
ds_read_b64_tr_b16 v[216:217], v234 offset:38912
ds_read_b64_tr_b16 v[218:219], v234 offset:39424
s_waitcnt lgkmcnt(6)
v_mfma_f32_32x32x16_bf16 v[176:191], v[204:207], v[48:51], v[176:191]
ds_read_b64_tr_b16 v[220:221], v235 offset:38912
ds_read_b64_tr_b16 v[222:223], v235 offset:39424
s_waitcnt lgkmcnt(6)
v_mfma_f32_32x32x16_bf16 v[128:143], v[208:211], v[52:55], v[128:143]
ds_read_b64_tr_b16 v[192:193], v234 offset:49152
ds_read_b64_tr_b16 v[194:195], v234 offset:49664
s_waitcnt lgkmcnt(6)
v_mfma_f32_32x32x16_bf16 v[144:159], v[212:215], v[52:55], v[144:159]
ds_read_b64_tr_b16 v[196:197], v235 offset:49152
ds_read_b64_tr_b16 v[198:199], v235 offset:49664
s_waitcnt vmcnt(8)
ds_add_u32 v245, v246 offset:28672
s_waitcnt lgkmcnt(7)
v_mfma_f32_32x32x16_bf16 v[160:175], v[216:219], v[52:55], v[160:175]
ds_read_b64_tr_b16 v[200:201], v234 offset:53248
ds_read_b64_tr_b16 v[202:203], v234 offset:53760
s_waitcnt lgkmcnt(7)
v_mfma_f32_32x32x16_bf16 v[176:191], v[220:223], v[52:55], v[176:191]
ds_read_b64_tr_b16 v[204:205], v235 offset:53248
ds_read_b64_tr_b16 v[206:207], v235 offset:53760
s_waitcnt lgkmcnt(7)
v_mfma_f32_32x32x16_bf16 v[128:143], v[192:195], v[56:59], v[128:143]
ds_read_b64_tr_b16 v[208:209], v234 offset:51200
ds_read_b64_tr_b16 v[210:211], v234 offset:51712
s_waitcnt lgkmcnt(7)
v_mfma_f32_32x32x16_bf16 v[144:159], v[196:199], v[56:59], v[144:159]
ds_read_b64_tr_b16 v[212:213], v235 offset:51200
ds_read_b64_tr_b16 v[214:215], v235 offset:51712
ds_read_b32 v227, v245 offset:28672
s_waitcnt lgkmcnt(7)
v_mfma_f32_32x32x16_bf16 v[160:175], v[200:203], v[56:59], v[160:175]
ds_read_b64_tr_b16 v[216:217], v234 offset:55296
ds_read_b64_tr_b16 v[218:219], v234 offset:55808
s_waitcnt lgkmcnt(7)
v_mfma_f32_32x32x16_bf16 v[176:191], v[204:207], v[56:59], v[176:191]
s_waitcnt lgkmcnt(2)
s_setprio 3
v_cmp_gt_u32 vcc, 8, v227
s_setprio 0
s_nop 0
s_cbranch_vccnz .Lk_ls_21
.Lk_ld_22:
ds_read_b64_tr_b16 v[220:221], v235 offset:55296
ds_read_b64_tr_b16 v[222:223], v235 offset:55808
v_mfma_f32_32x32x16_bf16 v[128:143], v[208:211], v[60:63], v[128:143]
ds_read_b64_tr_b16 v[192:193], v234 offset:8192
ds_read_b64_tr_b16 v[194:195], v234 offset:8704
v_mfma_f32_32x32x16_bf16 v[144:159], v[212:215], v[60:63], v[144:159]
ds_read_b64_tr_b16 v[196:197], v235 offset:8192
ds_read_b64_tr_b16 v[198:199], v235 offset:8704
s_waitcnt lgkmcnt(6)
v_mfma_f32_32x32x16_bf16 v[160:175], v[216:219], v[60:63], v[160:175]
ds_read_b64_tr_b16 v[200:201], v234 offset:12288
ds_read_b64_tr_b16 v[202:203], v234 offset:12800
s_waitcnt lgkmcnt(6)
v_mfma_f32_32x32x16_bf16 v[176:191], v[220:223], v[60:63], v[176:191]
ds_read_b64_tr_b16 v[204:205], v235 offset:12288
ds_read_b64_tr_b16 v[206:207], v235 offset:12800
s_waitcnt vmcnt(0)
ds_add_u32 v245, v246 offset:28672
s_waitcnt lgkmcnt(7)
v_mfma_f32_32x32x16_bf16 v[128:143], v[192:195], v[64:67], v[128:143]
ds_read_b64_tr_b16 v[208:209], v234 offset:10240
ds_read_b64_tr_b16 v[210:211], v234 offset:10752
s_waitcnt lgkmcnt(7)
v_mfma_f32_32x32x16_bf16 v[144:159], v[196:199], v[64:67], v[144:159]
ds_read_b64_tr_b16 v[212:213], v235 offset:10240
ds_read_b64_tr_b16 v[214:215], v235 offset:10752
s_add_u32 m0, s96, 73728
s_add_u32 s62, s92, 128
buffer_load_dwordx4 v240, s[28:31], s62 offen  lds
s_waitcnt lgkmcnt(7)
v_mfma_f32_32x32x16_bf16 v[160:175], v[200:203], v[64:67], v[160:175]
ds_read_b64_tr_b16 v[216:217], v234 offset:14336
ds_read_b64_tr_b16 v[218:219], v234 offset:14848
s_waitcnt lgkmcnt(7)
v_mfma_f32_32x32x16_bf16 v[176:191], v[204:207], v[64:67], v[176:191]
ds_read_b64_tr_b16 v[220:221], v235 offset:14336
ds_read_b64_tr_b16 v[222:223], v235 offset:14848
s_add_u32 m0, s96, 74752
s_add_u32 s62, s92, 28800
buffer_load_dwordx4 v240, s[28:31], s62 offen  lds
s_waitcnt lgkmcnt(6)
v_mfma_f32_32x32x16_bf16 v[128:143], v[208:211], v[68:71], v[128:143]
ds_read_b64_tr_b16 v[192:193], v234 offset:24576
ds_read_b64_tr_b16 v[194:195], v234 offset:25088
s_waitcnt lgkmcnt(6)
v_mfma_f32_32x32x16_bf16 v[144:159], v[212:215], v[68:71], v[144:159]
ds_read_b64_tr_b16 v[196:197], v235 offset:24576
ds_read_b64_tr_b16 v[198:199], v235 offset:25088
s_add_u32 m0, s96, 75776
s_add_u32 s62, s92, 57472
buffer_load_dwordx4 v240, s[28:31], s62 offen  lds
s_waitcnt lgkmcnt(6)
v_mfma_f32_32x32x16_bf16 v[160:175], v[216:219], v[68:71], v[160:175]
ds_read_b64_tr_b16 v[200:201], v234 offset:28672
ds_read_b64_tr_b16 v[202:203], v234 offset:29184
s_waitcnt lgkmcnt(6)
v_mfma_f32_32x32x16_bf16 v[176:191], v[220:223], v[68:71], v[176:191]
ds_read_b64_tr_b16 v[204:205], v235 offset:28672
ds_read_b64_tr_b16 v[206:207], v235 offset:29184
ds_read_b32 v227, v245 offset:28672
s_add_u32 m0, s96, 76800
s_add_u32 s62, s92, 86144
buffer_load_dwordx4 v240, s[28:31], s62 offen  lds
s_waitcnt lgkmcnt(7)
v_mfma_f32_32x32x16_bf16 v[128:143], v[192:195], v[72:75], v[128:143]
ds_read_b64_tr_b16 v[208:209], v234 offset:26624
ds_read_b64_tr_b16 v[210:211], v234 offset:27136
s_waitcnt lgkmcnt(7)
v_mfma_f32_32x32x16_bf16 v[144:159], v[196:199], v[72:75], v[144:159]
s_waitcnt lgkmcnt(2)
s_setprio 3
v_cmp_gt_u32 vcc, 12, v227
s_setprio 0
s_nop 0
s_cbranch_vccnz .Lk_ls_23
.Lk_ld_24:
ds_read_b64_tr_b16 v[212:213], v235 offset:26624
ds_read_b64_tr_b16 v[214:215], v235 offset:27136
s_add_u32 m0, s96, 77824
s_add_u32 s62, s92, 128
buffer_load_dwordx4 v240, s[32:35], s62 offen  lds
v_mfma_f32_32x32x16_bf16 v[160:175], v[200:203], v[72:75], v[160:175]
ds_read_b64_tr_b16 v[216:217], v234 offset:30720
ds_read_b64_tr_b16 v[218:219], v234 offset:31232
v_mfma_f32_32x32x16_bf16 v[176:191], v[204:207], v[72:75], v[176:191]
ds_read_b64_tr_b16 v[220:221], v235 offset:30720
ds_read_b64_tr_b16 v[222:223], v235 offset:31232
s_add_u32 m0, s96, 78848
s_add_u32 s62, s92, 28800
buffer_load_dwordx4 v240, s[32:35], s62 offen  lds
s_waitcnt lgkmcnt(6)
v_mfma_f32_32x32x16_bf16 v[128:143], v[208:211], v[76:79], v[128:143]
ds_read_b64_tr_b16 v[192:193], v234 offset:40960
ds_read_b64_tr_b16 v[194:195], v234 offset:41472
s_waitcnt lgkmcnt(6)
v_mfma_f32_32x32x16_bf16 v[144:159], v[212:215], v[76:79], v[144:159]
ds_read_b64_tr_b16 v[196:197], v235 offset:40960
ds_read_b64_tr_b16 v[198:199], v235 offset:41472
s_add_u32 m0, s96, 79872
s_add_u32 s62, s92, 57472
buffer_load_dwordx4 v240, s[32:35], s62 offen  lds
s_waitcnt lgkmcnt(6)
v_mfma_f32_32x32x16_bf16 v[160:175], v[216:219], v[76:79], v[160:175]
ds_read_b64_tr_b16 v[200:201], v234 offset:45056
ds_read_b64_tr_b16 v[202:203], v234 offset:45568
s_waitcnt lgkmcnt(6)
v_mfma_f32_32x32x16_bf16 v[176:191], v[220:223], v[76:79], v[176:191]
ds_read_b64_tr_b16 v[204:205], v235 offset:45056
ds_read_b64_tr_b16 v[206:207], v235 offset:45568
s_add_u32 m0, s96, 80896
s_add_u32 s62, s92, 86144
buffer_load_dwordx4 v240, s[32:35], s62 offen  lds
s_waitcnt lgkmcnt(6)
v_mfma_f32_32x32x16_bf16 v[128:143], v[192:195], v[80:83], v[128:143]
ds_read_b64_tr_b16 v[208:209], v234 offset:43008
ds_read_b64_tr_b16 v[210:211], v234 offset:43520
s_waitcnt lgkmcnt(6)
v_mfma_f32_32x32x16_bf16 v[144:159], v[196:199], v[80:83], v[144:159]
ds_read_b64_tr_b16 v[212:213], v235 offset:43008
ds_read_b64_tr_b16 v[214:215], v235 offset:43520
s_add_u32 m0, s96, 0
s_add_u32 s62, s93, 128
buffer_load_dwordx4 v240, s[28:31], s62 offen  lds
s_waitcnt lgkmcnt(6)
v_mfma_f32_32x32x16_bf16 v[160:175], v[200:203], v[80:83], v[160:175]
ds_read_b64_tr_b16 v[216:217], v234 offset:47104
ds_read_b64_tr_b16 v[218:219], v234 offset:47616
s_waitcnt lgkmcnt(6)
v_mfma_f32_32x32x16_bf16 v[176:191], v[204:207], v[80:83], v[176:191]
)ASM" R"ASM(
ds_read_b64_tr_b16 v[220:221], v235 offset:47104
ds_read_b64_tr_b16 v[222:223], v235 offset:47616
s_add_u32 m0, s96, 1024
s_add_u32 s62, s93, 28800
buffer_load_dwordx4 v240, s[28:31], s62 offen  lds
s_waitcnt lgkmcnt(6)
v_mfma_f32_32x32x16_bf16 v[128:143], v[208:211], v[84:87], v[128:143]
ds_read_b64_tr_b16 v[192:193], v234 offset:57344
ds_read_b64_tr_b16 v[194:195], v234 offset:57856
s_waitcnt lgkmcnt(6)
v_mfma_f32_32x32x16_bf16 v[144:159], v[212:215], v[84:87], v[144:159]
ds_read_b64_tr_b16 v[196:197], v235 offset:57344
ds_read_b64_tr_b16 v[198:199], v235 offset:57856
s_add_u32 m0, s96, 2048
s_add_u32 s62, s93, 57472
buffer_load_dwordx4 v240, s[28:31], s62 offen  lds
s_waitcnt lgkmcnt(6)
v_mfma_f32_32x32x16_bf16 v[160:175], v[216:219], v[84:87], v[160:175]
ds_read_b64_tr_b16 v[200:201], v234 offset:61440
ds_read_b64_tr_b16 v[202:203], v234 offset:61952
s_waitcnt lgkmcnt(6)
v_mfma_f32_32x32x16_bf16 v[176:191], v[220:223], v[84:87], v[176:191]
ds_read_b64_tr_b16 v[204:205], v235 offset:61440
ds_read_b64_tr_b16 v[206:207], v235 offset:61952
s_add_u32 m0, s96, 3072
s_add_u32 s62, s93, 86144
buffer_load_dwordx4 v240, s[28:31], s62 offen  lds
s_waitcnt lgkmcnt(6)
v_mfma_f32_32x32x16_bf16 v[128:143], v[192:195], v[88:91], v[128:143]
ds_read_b64_tr_b16 v[208:209], v234 offset:59392
ds_read_b64_tr_b16 v[210:211], v234 offset:59904
s_waitcnt lgkmcnt(6)
v_mfma_f32_32x32x16_bf16 v[144:159], v[196:199], v[88:91], v[144:159]
ds_read_b64_tr_b16 v[212:213], v235 offset:59392
ds_read_b64_tr_b16 v[214:215], v235 offset:59904
s_add_u32 m0, s96, 4096
s_add_u32 s62, s93, 128
buffer_load_dwordx4 v240, s[32:35], s62 offen  lds
s_waitcnt lgkmcnt(6)
v_mfma_f32_32x32x16_bf16 v[160:175], v[200:203], v[88:91], v[160:175]
ds_read_b64_tr_b16 v[216:217], v234 offset:63488
ds_read_b64_tr_b16 v[218:219], v234 offset:64000
s_waitcnt lgkmcnt(6)
v_mfma_f32_32x32x16_bf16 v[176:191], v[204:207], v[88:91], v[176:191]
ds_read_b64_tr_b16 v[220:221], v235 offset:63488
ds_read_b64_tr_b16 v[222:223], v235 offset:64000
s_add_u32 m0, s96, 5120
s_add_u32 s62, s93, 28800
buffer_load_dwordx4 v240, s[32:35], s62 offen  lds
s_waitcnt lgkmcnt(6)
v_mfma_f32_32x32x16_bf16 v[128:143], v[208:211], v[92:95], v[128:143]
ds_read_b64_tr_b16 v[192:193], v236 offset:0
ds_read_b64_tr_b16 v[194:195], v236 offset:512
s_waitcnt lgkmcnt(6)
v_mfma_f32_32x32x16_bf16 v[144:159], v[212:215], v[92:95], v[144:159]
ds_read_b64_tr_b16 v[196:197], v237 offset:0
ds_read_b64_tr_b16 v[198:199], v237 offset:512
s_add_u32 m0, s96, 6144
s_add_u32 s62, s93, 57472
buffer_load_dwordx4 v240, s[32:35], s62 offen  lds
s_waitcnt lgkmcnt(6)
v_mfma_f32_32x32x16_bf16 v[160:175], v[216:219], v[92:95], v[160:175]
ds_read_b64_tr_b16 v[200:201], v236 offset:4096
ds_read_b64_tr_b16 v[202:203], v236 offset:4608
s_waitcnt lgkmcnt(6)
v_mfma_f32_32x32x16_bf16 v[176:191], v[220:223], v[92:95], v[176:191]
ds_read_b64_tr_b16 v[204:205], v237 offset:4096
ds_read_b64_tr_b16 v[206:207], v237 offset:4608
s_add_u32 m0, s96, 7168
s_add_u32 s62, s93, 86144
buffer_load_dwordx4 v240, s[32:35], s62 offen  lds
s_waitcnt vmcnt(8)
ds_add_u32 v245, v246 offset:28672
s_waitcnt lgkmcnt(7)
v_mfma_f32_32x32x16_bf16 v[128:143], v[192:195], v[96:99], v[128:143]
ds_read_b64_tr_b16 v[208:209], v236 offset:2048
ds_read_b64_tr_b16 v[210:211], v236 offset:2560
s_waitcnt lgkmcnt(7)
v_mfma_f32_32x32x16_bf16 v[144:159], v[196:199], v[96:99], v[144:159]
ds_read_b64_tr_b16 v[212:213], v237 offset:2048
ds_read_b64_tr_b16 v[214:215], v237 offset:2560
s_waitcnt lgkmcnt(7)
v_mfma_f32_32x32x16_bf16 v[160:175], v[200:203], v[96:99], v[160:175]
ds_read_b64_tr_b16 v[216:217], v236 offset:6144
ds_read_b64_tr_b16 v[218:219], v236 offset:6656
s_waitcnt lgkmcnt(7)
v_mfma_f32_32x32x16_bf16 v[176:191], v[204:207], v[96:99], v[176:191]
ds_read_b64_tr_b16 v[220:221], v237 offset:6144
ds_read_b64_tr_b16 v[222:223], v237 offset:6656
s_waitcnt lgkmcnt(6)
v_mfma_f32_32x32x16_bf16 v[128:143], v[208:211], v[100:103], v[128:143]
ds_read_b64_tr_b16 v[192:193], v236 offset:16384
ds_read_b64_tr_b16 v[194:195], v236 offset:16896
s_waitcnt lgkmcnt(6)
v_mfma_f32_32x32x16_bf16 v[144:159], v[212:215], v[100:103], v[144:159]
ds_read_b64_tr_b16 v[196:197], v237 offset:16384
ds_read_b64_tr_b16 v[198:199], v237 offset:16896
s_waitcnt lgkmcnt(6)
v_mfma_f32_32x32x16_bf16 v[160:175], v[216:219], v[100:103], v[160:175]
ds_read_b64_tr_b16 v[200:201], v236 offset:20480
ds_read_b64_tr_b16 v[202:203], v236 offset:20992
s_waitcnt lgkmcnt(6)
v_mfma_f32_32x32x16_bf16 v[176:191], v[220:223], v[100:103], v[176:191]
ds_read_b64_tr_b16 v[204:205], v237 offset:20480
ds_read_b64_tr_b16 v[206:207], v237 offset:20992
ds_read_b32 v227, v245 offset:28672
s_waitcnt lgkmcnt(7)
v_mfma_f32_32x32x16_bf16 v[128:143], v[192:195], v[104:107], v[128:143]
ds_read_b64_tr_b16 v[208:209], v236 offset:18432
ds_read_b64_tr_b16 v[210:211], v236 offset:18944
s_waitcnt lgkmcnt(7)
v_mfma_f32_32x32x16_bf16 v[144:159], v[196:199], v[104:107], v[144:159]
s_waitcnt lgkmcnt(2)
s_setprio 3
v_cmp_gt_u32 vcc, 16, v227
s_setprio 0
s_nop 0
s_cbranch_vccnz .Lk_ls_25
.Lk_ld_26:
ds_read_b64_tr_b16 v[212:213], v237 offset:18432
ds_read_b64_tr_b16 v[214:215], v237 offset:18944
s_add_u32 m0, s96, 8192
s_add_u32 s62, s94, 128
buffer_load_dwordx4 v240, s[28:31], s62 offen  lds
v_mfma_f32_32x32x16_bf16 v[160:175], v[200:203], v[104:107], v[160:175]
ds_read_b64_tr_b16 v[216:217], v236 offset:22528
ds_read_b64_tr_b16 v[218:219], v236 offset:23040
v_mfma_f32_32x32x16_bf16 v[176:191], v[204:207], v[104:107], v[176:191]
ds_read_b64_tr_b16 v[220:221], v237 offset:22528
ds_read_b64_tr_b16 v[222:223], v237 offset:23040
s_add_u32 m0, s96, 9216
s_add_u32 s62, s94, 28800
buffer_load_dwordx4 v240, s[28:31], s62 offen  lds
s_waitcnt lgkmcnt(6)
v_mfma_f32_32x32x16_bf16 v[128:143], v[208:211], v[108:111], v[128:143]
ds_read_b64_tr_b16 v[192:193], v236 offset:32768
ds_read_b64_tr_b16 v[194:195], v236 offset:33280
s_waitcnt lgkmcnt(6)
v_mfma_f32_32x32x16_bf16 v[144:159], v[212:215], v[108:111], v[144:159]
ds_read_b64_tr_b16 v[196:197], v237 offset:32768
ds_read_b64_tr_b16 v[198:199], v237 offset:33280
s_add_u32 m0, s96, 10240
s_add_u32 s62, s94, 57472
buffer_load_dwordx4 v240, s[28:31], s62 offen  lds
s_waitcnt lgkmcnt(6)
v_mfma_f32_32x32x16_bf16 v[160:175], v[216:219], v[108:111], v[160:175]
ds_read_b64_tr_b16 v[200:201], v236 offset:36864
ds_read_b64_tr_b16 v[202:203], v236 offset:37376
s_waitcnt lgkmcnt(6)
v_mfma_f32_32x32x16_bf16 v[176:191], v[220:223], v[108:111], v[176:191]
ds_read_b64_tr_b16 v[204:205], v237 offset:36864
ds_read_b64_tr_b16 v[206:207], v237 offset:37376
s_add_u32 m0, s96, 11264
s_add_u32 s62, s94, 86144
buffer_load_dwordx4 v240, s[28:31], s62 offen  lds
s_waitcnt lgkmcnt(6)
v_mfma_f32_32x32x16_bf16 v[128:143], v[192:195], v[112:115], v[128:143]
ds_read_b64_tr_b16 v[208:209], v236 offset:34816
ds_read_b64_tr_b16 v[210:211], v236 offset:35328
s_waitcnt lgkmcnt(6)
v_mfma_f32_32x32x16_bf16 v[144:159], v[196:199], v[112:115], v[144:159]
ds_read_b64_tr_b16 v[212:213], v237 offset:34816
ds_read_b64_tr_b16 v[214:215], v237 offset:35328
s_add_u32 m0, s96, 12288
s_add_u32 s62, s94, 128
buffer_load_dwordx4 v240, s[32:35], s62 offen  lds
s_waitcnt lgkmcnt(6)
v_mfma_f32_32x32x16_bf16 v[160:175], v[200:203], v[112:115], v[160:175]
ds_read_b64_tr_b16 v[216:217], v236 offset:38912
ds_read_b64_tr_b16 v[218:219], v236 offset:39424
s_waitcnt lgkmcnt(6)
v_mfma_f32_32x32x16_bf16 v[176:191], v[204:207], v[112:115], v[176:191]
ds_read_b64_tr_b16 v[220:221], v237 offset:38912
ds_read_b64_tr_b16 v[222:223], v237 offset:39424
s_add_u32 m0, s96, 13312
s_add_u32 s62, s94, 28800
buffer_load_dwordx4 v240, s[32:35], s62 offen  lds
s_waitcnt lgkmcnt(6)
v_mfma_f32_32x32x16_bf16 v[128:143], v[208:211], v[116:119], v[128:143]
ds_read_b64_tr_b16 v[192:193], v236 offset:49152
ds_read_b64_tr_b16 v[194:195], v236 offset:49664
s_waitcnt lgkmcnt(6)
v_mfma_f32_32x32x16_bf16 v[144:159], v[212:215], v[116:119], v[144:159]
ds_read_b64_tr_b16 v[196:197], v237 offset:49152
ds_read_b64_tr_b16 v[198:199], v237 offset:49664
s_add_u32 m0, s96, 14336
s_add_u32 s62, s94, 57472
buffer_load_dwordx4 v240, s[32:35], s62 offen  lds
s_waitcnt lgkmcnt(6)
v_mfma_f32_32x32x16_bf16 v[160:175], v[216:219], v[116:119], v[160:175]
ds_read_b64_tr_b16 v[200:201], v236 offset:53248
ds_read_b64_tr_b16 v[202:203], v236 offset:53760
s_waitcnt lgkmcnt(6)
v_mfma_f32_32x32x16_bf16 v[176:191], v[220:223], v[116:119], v[176:191]
ds_read_b64_tr_b16 v[204:205], v237 offset:53248
ds_read_b64_tr_b16 v[206:207], v237 offset:53760
s_add_u32 m0, s96, 15360
s_add_u32 s62, s94, 86144
buffer_load_dwordx4 v240, s[32:35], s62 offen  lds
s_waitcnt lgkmcnt(6)
)ASM" R"ASM(
v_mfma_f32_32x32x16_bf16 v[128:143], v[192:195], v[120:123], v[128:143]
ds_read_b64_tr_b16 v[208:209], v236 offset:51200
ds_read_b64_tr_b16 v[210:211], v236 offset:51712
s_waitcnt lgkmcnt(6)
v_mfma_f32_32x32x16_bf16 v[144:159], v[196:199], v[120:123], v[144:159]
ds_read_b64_tr_b16 v[212:213], v237 offset:51200
ds_read_b64_tr_b16 v[214:215], v237 offset:51712
s_mov_b64 exec, s[60:61]
buffer_load_dword v247, v247, s[24:27], s18 offen
s_mov_b64 exec, -1
s_waitcnt lgkmcnt(6)
v_mfma_f32_32x32x16_bf16 v[160:175], v[200:203], v[120:123], v[160:175]
ds_read_b64_tr_b16 v[216:217], v236 offset:55296
ds_read_b64_tr_b16 v[218:219], v236 offset:55808
s_waitcnt lgkmcnt(6)
v_mfma_f32_32x32x16_bf16 v[176:191], v[204:207], v[120:123], v[176:191]
ds_read_b64_tr_b16 v[220:221], v237 offset:55296
ds_read_b64_tr_b16 v[222:223], v237 offset:55808
s_waitcnt lgkmcnt(6)
v_mfma_f32_32x32x16_bf16 v[128:143], v[208:211], v[124:127], v[128:143]
s_waitcnt lgkmcnt(4)
v_mfma_f32_32x32x16_bf16 v[144:159], v[212:215], v[124:127], v[144:159]
s_waitcnt lgkmcnt(2)
v_mfma_f32_32x32x16_bf16 v[160:175], v[216:219], v[124:127], v[160:175]
s_waitcnt lgkmcnt(0)
v_mfma_f32_32x32x16_bf16 v[176:191], v[220:223], v[124:127], v[176:191]
ds_read_b64_tr_b16 v[192:193], v236 offset:8192
ds_read_b64_tr_b16 v[194:195], v236 offset:8704
ds_read_b64_tr_b16 v[196:197], v237 offset:8192
ds_read_b64_tr_b16 v[198:199], v237 offset:8704
ds_read_b64_tr_b16 v[200:201], v236 offset:12288
ds_read_b64_tr_b16 v[202:203], v236 offset:12800
ds_read_b64_tr_b16 v[204:205], v237 offset:12288
ds_read_b64_tr_b16 v[206:207], v237 offset:12800
s_setprio 3
v_pk_mul_f32 v[128:129], v[128:129], v[232:233]
v_pk_mul_f32 v[130:131], v[130:131], v[232:233]
v_pk_mul_f32 v[132:133], v[132:133], v[232:233]
v_pk_mul_f32 v[134:135], v[134:135], v[232:233]
v_exp_f32 v128, v128
v_exp_f32 v129, v129
v_exp_f32 v130, v130
v_exp_f32 v131, v131
v_exp_f32 v132, v132
v_exp_f32 v133, v133
v_exp_f32 v134, v134
v_exp_f32 v135, v135
v_pk_fma_f32 v[128:129], v[128:129], v[230:231], v[230:231]
v_pk_fma_f32 v[130:131], v[130:131], v[230:231], v[230:231]
v_pk_fma_f32 v[132:133], v[132:133], v[230:231], v[230:231]
v_pk_fma_f32 v[134:135], v[134:135], v[230:231], v[230:231]
v_rcp_f32 v128, v128
v_rcp_f32 v129, v129
v_rcp_f32 v130, v130
v_rcp_f32 v131, v131
v_rcp_f32 v132, v132
v_rcp_f32 v133, v133
v_rcp_f32 v134, v134
v_rcp_f32 v135, v135
v_pk_mul_f32 v[160:161], v[160:161], v[128:129]
v_pk_mul_f32 v[162:163], v[162:163], v[130:131]
v_pk_mul_f32 v[164:165], v[164:165], v[132:133]
v_pk_mul_f32 v[166:167], v[166:167], v[134:135]
v_cvt_pk_bf16_f32 v128, v160, v161
v_cvt_pk_bf16_f32 v129, v162, v163
v_cvt_pk_bf16_f32 v130, v164, v165
v_cvt_pk_bf16_f32 v131, v166, v167
buffer_store_dwordx4 v[128:131], v244, s[48:51], s7 offen offset:0 sc1
v_pk_mul_f32 v[136:137], v[136:137], v[232:233]
v_pk_mul_f32 v[138:139], v[138:139], v[232:233]
v_pk_mul_f32 v[140:141], v[140:141], v[232:233]
v_pk_mul_f32 v[142:143], v[142:143], v[232:233]
v_exp_f32 v136, v136
v_exp_f32 v137, v137
v_exp_f32 v138, v138
v_exp_f32 v139, v139
v_exp_f32 v140, v140
v_exp_f32 v141, v141
v_exp_f32 v142, v142
v_exp_f32 v143, v143
v_pk_fma_f32 v[136:137], v[136:137], v[230:231], v[230:231]
v_pk_fma_f32 v[138:139], v[138:139], v[230:231], v[230:231]
v_pk_fma_f32 v[140:141], v[140:141], v[230:231], v[230:231]
v_pk_fma_f32 v[142:143], v[142:143], v[230:231], v[230:231]
v_rcp_f32 v136, v136
v_rcp_f32 v137, v137
v_rcp_f32 v138, v138
v_rcp_f32 v139, v139
v_rcp_f32 v140, v140
v_rcp_f32 v141, v141
v_rcp_f32 v142, v142
v_rcp_f32 v143, v143
v_pk_mul_f32 v[168:169], v[168:169], v[136:137]
v_pk_mul_f32 v[170:171], v[170:171], v[138:139]
v_pk_mul_f32 v[172:173], v[172:173], v[140:141]
v_pk_mul_f32 v[174:175], v[174:175], v[142:143]
v_cvt_pk_bf16_f32 v136, v168, v169
v_cvt_pk_bf16_f32 v137, v170, v171
v_cvt_pk_bf16_f32 v138, v172, v173
v_cvt_pk_bf16_f32 v139, v174, v175
buffer_store_dwordx4 v[136:139], v244, s[48:51], s7 offen offset:1024 sc1
v_pk_mul_f32 v[144:145], v[144:145], v[232:233]
v_pk_mul_f32 v[146:147], v[146:147], v[232:233]
v_pk_mul_f32 v[148:149], v[148:149], v[232:233]
v_pk_mul_f32 v[150:151], v[150:151], v[232:233]
v_exp_f32 v144, v144
v_exp_f32 v145, v145
v_exp_f32 v146, v146
v_exp_f32 v147, v147
v_exp_f32 v148, v148
v_exp_f32 v149, v149
v_exp_f32 v150, v150
v_exp_f32 v151, v151
v_pk_fma_f32 v[144:145], v[144:145], v[230:231], v[230:231]
v_pk_fma_f32 v[146:147], v[146:147], v[230:231], v[230:231]
v_pk_fma_f32 v[148:149], v[148:149], v[230:231], v[230:231]
v_pk_fma_f32 v[150:151], v[150:151], v[230:231], v[230:231]
v_rcp_f32 v144, v144
v_rcp_f32 v145, v145
v_rcp_f32 v146, v146
v_rcp_f32 v147, v147
v_rcp_f32 v148, v148
v_rcp_f32 v149, v149
v_rcp_f32 v150, v150
v_rcp_f32 v151, v151
v_pk_mul_f32 v[176:177], v[176:177], v[144:145]
v_pk_mul_f32 v[178:179], v[178:179], v[146:147]
v_pk_mul_f32 v[180:181], v[180:181], v[148:149]
v_pk_mul_f32 v[182:183], v[182:183], v[150:151]
v_cvt_pk_bf16_f32 v144, v176, v177
v_cvt_pk_bf16_f32 v145, v178, v179
v_cvt_pk_bf16_f32 v146, v180, v181
v_cvt_pk_bf16_f32 v147, v182, v183
buffer_store_dwordx4 v[144:147], v244, s[48:51], s7 offen offset:2048 sc1
v_pk_mul_f32 v[152:153], v[152:153], v[232:233]
v_pk_mul_f32 v[154:155], v[154:155], v[232:233]
v_pk_mul_f32 v[156:157], v[156:157], v[232:233]
v_pk_mul_f32 v[158:159], v[158:159], v[232:233]
v_exp_f32 v152, v152
v_exp_f32 v153, v153
v_exp_f32 v154, v154
v_exp_f32 v155, v155
v_exp_f32 v156, v156
v_exp_f32 v157, v157
v_exp_f32 v158, v158
v_exp_f32 v159, v159
v_pk_fma_f32 v[152:153], v[152:153], v[230:231], v[230:231]
v_pk_fma_f32 v[154:155], v[154:155], v[230:231], v[230:231]
v_pk_fma_f32 v[156:157], v[156:157], v[230:231], v[230:231]
v_pk_fma_f32 v[158:159], v[158:159], v[230:231], v[230:231]
v_rcp_f32 v152, v152
v_rcp_f32 v153, v153
v_rcp_f32 v154, v154
v_rcp_f32 v155, v155
v_rcp_f32 v156, v156
v_rcp_f32 v157, v157
v_rcp_f32 v158, v158
v_rcp_f32 v159, v159
v_pk_mul_f32 v[184:185], v[184:185], v[152:153]
v_pk_mul_f32 v[186:187], v[186:187], v[154:155]
v_pk_mul_f32 v[188:189], v[188:189], v[156:157]
v_pk_mul_f32 v[190:191], v[190:191], v[158:159]
v_cvt_pk_bf16_f32 v152, v184, v185
v_cvt_pk_bf16_f32 v153, v186, v187
v_cvt_pk_bf16_f32 v154, v188, v189
v_cvt_pk_bf16_f32 v155, v190, v191
buffer_store_dwordx4 v[152:155], v244, s[48:51], s7 offen offset:3072 sc1
v_and_b32 v243, 15, v224
v_lshrrev_b32 v176, 4, v224
v_lshlrev_b32 v243, 4, v243
v_lshl_add_u32 v243, v176, 9, v243
s_setprio 0
s_waitcnt vmcnt(13)
ds_add_u32 v245, v246 offset:28672
s_waitcnt lgkmcnt(7)
v_mfma_f32_32x32x16_bf16 v[128:143], v[192:195], v[0:3], 0
ds_read_b64_tr_b16 v[208:209], v236 offset:10240
ds_read_b64_tr_b16 v[210:211], v236 offset:10752
s_waitcnt lgkmcnt(7)
v_mfma_f32_32x32x16_bf16 v[144:159], v[196:199], v[0:3], 0
ds_read_b64_tr_b16 v[212:213], v237 offset:10240
ds_read_b64_tr_b16 v[214:215], v237 offset:10752
s_waitcnt lgkmcnt(7)
v_mfma_f32_32x32x16_bf16 v[160:175], v[200:203], v[0:3], 0
ds_read_b64_tr_b16 v[216:217], v236 offset:14336
ds_read_b64_tr_b16 v[218:219], v236 offset:14848
s_waitcnt lgkmcnt(7)
v_mfma_f32_32x32x16_bf16 v[176:191], v[204:207], v[0:3], 0
ds_read_b64_tr_b16 v[220:221], v237 offset:14336
ds_read_b64_tr_b16 v[222:223], v237 offset:14848
s_waitcnt lgkmcnt(6)
v_mfma_f32_32x32x16_bf16 v[128:143], v[208:211], v[4:7], v[128:143]
ds_read_b64_tr_b16 v[192:193], v236 offset:24576
ds_read_b64_tr_b16 v[194:195], v236 offset:25088
s_waitcnt lgkmcnt(6)
v_mfma_f32_32x32x16_bf16 v[144:159], v[212:215], v[4:7], v[144:159]
ds_read_b64_tr_b16 v[196:197], v237 offset:24576
ds_read_b64_tr_b16 v[198:199], v237 offset:25088
s_waitcnt lgkmcnt(6)
v_mfma_f32_32x32x16_bf16 v[160:175], v[216:219], v[4:7], v[160:175]
)ASM" R"ASM(
ds_read_b64_tr_b16 v[200:201], v236 offset:28672
ds_read_b64_tr_b16 v[202:203], v236 offset:29184
s_waitcnt lgkmcnt(6)
v_mfma_f32_32x32x16_bf16 v[176:191], v[220:223], v[4:7], v[176:191]
ds_read_b64_tr_b16 v[204:205], v237 offset:28672
ds_read_b64_tr_b16 v[206:207], v237 offset:29184
ds_read_b32 v227, v245 offset:28672
s_waitcnt vmcnt(0)
s_mov_b64 exec, 1
buffer_store_dwordx2 v[248:249], off, s[48:51], s16 offset:0 sc1
s_mov_b64 exec, -1
s_waitcnt lgkmcnt(7)
v_mfma_f32_32x32x16_bf16 v[128:143], v[192:195], v[8:11], v[128:143]
ds_read_b64_tr_b16 v[208:209], v236 offset:26624
ds_read_b64_tr_b16 v[210:211], v236 offset:27136
s_waitcnt lgkmcnt(7)
v_mfma_f32_32x32x16_bf16 v[144:159], v[196:199], v[8:11], v[144:159]
s_waitcnt lgkmcnt(2)
s_setprio 3
v_cmp_gt_u32 vcc, 20, v227
s_setprio 0
s_nop 0
s_cbranch_vccnz .Lk_ls_27
.Lk_ld_28:
ds_read_b64_tr_b16 v[212:213], v237 offset:26624
ds_read_b64_tr_b16 v[214:215], v237 offset:27136
s_add_u32 m0, s96, 65536
s_add_u32 s62, s95, 128
buffer_load_dwordx4 v240, s[28:31], s62 offen  lds
v_mfma_f32_32x32x16_bf16 v[160:175], v[200:203], v[8:11], v[160:175]
ds_read_b64_tr_b16 v[216:217], v236 offset:30720
ds_read_b64_tr_b16 v[218:219], v236 offset:31232
v_mfma_f32_32x32x16_bf16 v[176:191], v[204:207], v[8:11], v[176:191]
ds_read_b64_tr_b16 v[220:221], v237 offset:30720
ds_read_b64_tr_b16 v[222:223], v237 offset:31232
s_add_u32 m0, s96, 66560
s_add_u32 s62, s95, 28800
buffer_load_dwordx4 v240, s[28:31], s62 offen  lds
s_waitcnt lgkmcnt(6)
v_mfma_f32_32x32x16_bf16 v[128:143], v[208:211], v[12:15], v[128:143]
ds_read_b64_tr_b16 v[192:193], v236 offset:40960
ds_read_b64_tr_b16 v[194:195], v236 offset:41472
s_waitcnt lgkmcnt(6)
v_mfma_f32_32x32x16_bf16 v[144:159], v[212:215], v[12:15], v[144:159]
ds_read_b64_tr_b16 v[196:197], v237 offset:40960
ds_read_b64_tr_b16 v[198:199], v237 offset:41472
s_add_u32 m0, s96, 67584
s_add_u32 s62, s95, 57472
buffer_load_dwordx4 v240, s[28:31], s62 offen  lds
s_waitcnt lgkmcnt(6)
v_mfma_f32_32x32x16_bf16 v[160:175], v[216:219], v[12:15], v[160:175]
ds_read_b64_tr_b16 v[200:201], v236 offset:45056
ds_read_b64_tr_b16 v[202:203], v236 offset:45568
s_waitcnt lgkmcnt(6)
v_mfma_f32_32x32x16_bf16 v[176:191], v[220:223], v[12:15], v[176:191]
ds_read_b64_tr_b16 v[204:205], v237 offset:45056
ds_read_b64_tr_b16 v[206:207], v237 offset:45568
s_add_u32 m0, s96, 68608
s_add_u32 s62, s95, 86144
buffer_load_dwordx4 v240, s[28:31], s62 offen  lds
s_waitcnt lgkmcnt(6)
v_mfma_f32_32x32x16_bf16 v[128:143], v[192:195], v[16:19], v[128:143]
ds_read_b64_tr_b16 v[208:209], v236 offset:43008
ds_read_b64_tr_b16 v[210:211], v236 offset:43520
s_waitcnt lgkmcnt(6)
v_mfma_f32_32x32x16_bf16 v[144:159], v[196:199], v[16:19], v[144:159]
ds_read_b64_tr_b16 v[212:213], v237 offset:43008
ds_read_b64_tr_b16 v[214:215], v237 offset:43520
s_add_u32 m0, s96, 69632
s_add_u32 s62, s95, 128
buffer_load_dwordx4 v240, s[32:35], s62 offen  lds
s_waitcnt lgkmcnt(6)
v_mfma_f32_32x32x16_bf16 v[160:175], v[200:203], v[16:19], v[160:175]
ds_read_b64_tr_b16 v[216:217], v236 offset:47104
ds_read_b64_tr_b16 v[218:219], v236 offset:47616
s_waitcnt lgkmcnt(6)
v_mfma_f32_32x32x16_bf16 v[176:191], v[204:207], v[16:19], v[176:191]
ds_read_b64_tr_b16 v[220:221], v237 offset:47104
ds_read_b64_tr_b16 v[222:223], v237 offset:47616
s_add_u32 m0, s96, 70656
s_add_u32 s62, s95, 28800
buffer_load_dwordx4 v240, s[32:35], s62 offen  lds
s_waitcnt lgkmcnt(6)
v_mfma_f32_32x32x16_bf16 v[128:143], v[208:211], v[20:23], v[128:143]
ds_read_b64_tr_b16 v[192:193], v236 offset:57344
ds_read_b64_tr_b16 v[194:195], v236 offset:57856
s_waitcnt lgkmcnt(6)
v_mfma_f32_32x32x16_bf16 v[144:159], v[212:215], v[20:23], v[144:159]
ds_read_b64_tr_b16 v[196:197], v237 offset:57344
ds_read_b64_tr_b16 v[198:199], v237 offset:57856
s_add_u32 m0, s96, 71680
s_add_u32 s62, s95, 57472
buffer_load_dwordx4 v240, s[32:35], s62 offen  lds
s_waitcnt lgkmcnt(6)
v_mfma_f32_32x32x16_bf16 v[160:175], v[216:219], v[20:23], v[160:175]
ds_read_b64_tr_b16 v[200:201], v236 offset:61440
ds_read_b64_tr_b16 v[202:203], v236 offset:61952
s_waitcnt lgkmcnt(6)
v_mfma_f32_32x32x16_bf16 v[176:191], v[220:223], v[20:23], v[176:191]
ds_read_b64_tr_b16 v[204:205], v237 offset:61440
ds_read_b64_tr_b16 v[206:207], v237 offset:61952
s_add_u32 m0, s96, 72704
s_add_u32 s62, s95, 86144
buffer_load_dwordx4 v240, s[32:35], s62 offen  lds
s_waitcnt lgkmcnt(6)
v_mfma_f32_32x32x16_bf16 v[128:143], v[192:195], v[24:27], v[128:143]
ds_read_b64_tr_b16 v[208:209], v236 offset:59392
ds_read_b64_tr_b16 v[210:211], v236 offset:59904
s_waitcnt lgkmcnt(6)
v_mfma_f32_32x32x16_bf16 v[144:159], v[196:199], v[24:27], v[144:159]
ds_read_b64_tr_b16 v[212:213], v237 offset:59392
ds_read_b64_tr_b16 v[214:215], v237 offset:59904
s_waitcnt lgkmcnt(6)
v_mfma_f32_32x32x16_bf16 v[160:175], v[200:203], v[24:27], v[160:175]
ds_read_b64_tr_b16 v[216:217], v236 offset:63488
ds_read_b64_tr_b16 v[218:219], v236 offset:64000
s_waitcnt lgkmcnt(6)
v_mfma_f32_32x32x16_bf16 v[176:191], v[204:207], v[24:27], v[176:191]
ds_read_b64_tr_b16 v[220:221], v237 offset:63488
ds_read_b64_tr_b16 v[222:223], v237 offset:64000
s_waitcnt lgkmcnt(6)
v_mfma_f32_32x32x16_bf16 v[128:143], v[208:211], v[28:31], v[128:143]
ds_read_b64_tr_b16 v[192:193], v234 offset:0
ds_read_b64_tr_b16 v[194:195], v234 offset:512
s_waitcnt lgkmcnt(6)
v_mfma_f32_32x32x16_bf16 v[144:159], v[212:215], v[28:31], v[144:159]
ds_read_b64_tr_b16 v[196:197], v235 offset:0
ds_read_b64_tr_b16 v[198:199], v235 offset:512
s_waitcnt lgkmcnt(6)
v_mfma_f32_32x32x16_bf16 v[160:175], v[216:219], v[28:31], v[160:175]
ds_read_b64_tr_b16 v[200:201], v234 offset:4096
ds_read_b64_tr_b16 v[202:203], v234 offset:4608
s_waitcnt lgkmcnt(6)
v_mfma_f32_32x32x16_bf16 v[176:191], v[220:223], v[28:31], v[176:191]
ds_read_b64_tr_b16 v[204:205], v235 offset:4096
ds_read_b64_tr_b16 v[206:207], v235 offset:4608
ds_add_u32 v245, v246 offset:28672
s_waitcnt lgkmcnt(7)
v_mfma_f32_32x32x16_bf16 v[128:143], v[192:195], v[32:35], v[128:143]
ds_read_b64_tr_b16 v[208:209], v234 offset:2048
ds_read_b64_tr_b16 v[210:211], v234 offset:2560
s_waitcnt lgkmcnt(7)
v_mfma_f32_32x32x16_bf16 v[144:159], v[196:199], v[32:35], v[144:159]
ds_read_b64_tr_b16 v[212:213], v235 offset:2048
ds_read_b64_tr_b16 v[214:215], v235 offset:2560
s_waitcnt lgkmcnt(7)
v_mfma_f32_32x32x16_bf16 v[160:175], v[200:203], v[32:35], v[160:175]
ds_read_b64_tr_b16 v[216:217], v234 offset:6144
ds_read_b64_tr_b16 v[218:219], v234 offset:6656
s_waitcnt lgkmcnt(7)
v_mfma_f32_32x32x16_bf16 v[176:191], v[204:207], v[32:35], v[176:191]
ds_read_b64_tr_b16 v[220:221], v235 offset:6144
ds_read_b64_tr_b16 v[222:223], v235 offset:6656
s_waitcnt lgkmcnt(6)
v_mfma_f32_32x32x16_bf16 v[128:143], v[208:211], v[36:39], v[128:143]
ds_read_b64_tr_b16 v[192:193], v234 offset:16384
ds_read_b64_tr_b16 v[194:195], v234 offset:16896
s_waitcnt lgkmcnt(6)
v_mfma_f32_32x32x16_bf16 v[144:159], v[212:215], v[36:39], v[144:159]
ds_read_b64_tr_b16 v[196:197], v235 offset:16384
ds_read_b64_tr_b16 v[198:199], v235 offset:16896
s_waitcnt lgkmcnt(6)
v_mfma_f32_32x32x16_bf16 v[160:175], v[216:219], v[36:39], v[160:175]
ds_read_b64_tr_b16 v[200:201], v234 offset:20480
ds_read_b64_tr_b16 v[202:203], v234 offset:20992
s_waitcnt lgkmcnt(6)
v_mfma_f32_32x32x16_bf16 v[176:191], v[220:223], v[36:39], v[176:191]
ds_read_b64_tr_b16 v[204:205], v235 offset:20480
ds_read_b64_tr_b16 v[206:207], v235 offset:20992
ds_read_b32 v227, v245 offset:28672
s_waitcnt lgkmcnt(7)
v_mfma_f32_32x32x16_bf16 v[128:143], v[192:195], v[40:43], v[128:143]
ds_read_b64_tr_b16 v[208:209], v234 offset:18432
ds_read_b64_tr_b16 v[210:211], v234 offset:18944
s_waitcnt lgkmcnt(7)
v_mfma_f32_32x32x16_bf16 v[144:159], v[196:199], v[40:43], v[144:159]
s_waitcnt lgkmcnt(2)
s_setprio 3
v_cmp_gt_u32 vcc, 24, v227
s_setprio 0
s_nop 0
s_cbranch_vccnz .Lk_ls_29
.Lk_ld_30:
ds_read_b64_tr_b16 v[212:213], v235 offset:18432
ds_read_b64_tr_b16 v[214:215], v235 offset:18944
s_add_u32 m0, s96, 73728
s_add_u32 s62, s11, 0
buffer_load_dwordx4 v242, s[36:39], s62 offen  lds
v_mfma_f32_32x32x16_bf16 v[160:175], v[200:203], v[40:43], v[160:175]
ds_read_b64_tr_b16 v[216:217], v234 offset:22528
ds_read_b64_tr_b16 v[218:219], v234 offset:23040
v_mfma_f32_32x32x16_bf16 v[176:191], v[204:207], v[40:43], v[176:191]
ds_read_b64_tr_b16 v[220:221], v235 offset:22528
ds_read_b64_tr_b16 v[222:223], v235 offset:23040
s_add_u32 m0, s96, 74752
s_add_u32 s62, s11, 8192
buffer_load_dwordx4 v242, s[36:39], s62 offen  lds
s_waitcnt lgkmcnt(6)
v_mfma_f32_32x32x16_bf16 v[128:143], v[208:211], v[44:47], v[128:143]
ds_read_b64_tr_b16 v[192:193], v234 offset:32768
)ASM" R"ASM(
ds_read_b64_tr_b16 v[194:195], v234 offset:33280
s_waitcnt lgkmcnt(6)
v_mfma_f32_32x32x16_bf16 v[144:159], v[212:215], v[44:47], v[144:159]
ds_read_b64_tr_b16 v[196:197], v235 offset:32768
ds_read_b64_tr_b16 v[198:199], v235 offset:33280
s_add_u32 m0, s96, 75776
s_add_u32 s62, s11, 16384
buffer_load_dwordx4 v242, s[36:39], s62 offen  lds
s_waitcnt lgkmcnt(6)
v_mfma_f32_32x32x16_bf16 v[160:175], v[216:219], v[44:47], v[160:175]
ds_read_b64_tr_b16 v[200:201], v234 offset:36864
ds_read_b64_tr_b16 v[202:203], v234 offset:37376
s_waitcnt lgkmcnt(6)
v_mfma_f32_32x32x16_bf16 v[176:191], v[220:223], v[44:47], v[176:191]
ds_read_b64_tr_b16 v[204:205], v235 offset:36864
ds_read_b64_tr_b16 v[206:207], v235 offset:37376
s_add_u32 m0, s96, 76800
s_add_u32 s62, s11, 24576
buffer_load_dwordx4 v242, s[36:39], s62 offen  lds
s_waitcnt lgkmcnt(6)
v_mfma_f32_32x32x16_bf16 v[128:143], v[192:195], v[48:51], v[128:143]
ds_read_b64_tr_b16 v[208:209], v234 offset:34816
ds_read_b64_tr_b16 v[210:211], v234 offset:35328
s_waitcnt lgkmcnt(6)
v_mfma_f32_32x32x16_bf16 v[144:159], v[196:199], v[48:51], v[144:159]
ds_read_b64_tr_b16 v[212:213], v235 offset:34816
ds_read_b64_tr_b16 v[214:215], v235 offset:35328
s_add_u32 m0, s96, 77824
s_add_u32 s62, s11, 32768
buffer_load_dwordx4 v242, s[36:39], s62 offen  lds
s_waitcnt lgkmcnt(6)
v_mfma_f32_32x32x16_bf16 v[160:175], v[200:203], v[48:51], v[160:175]
ds_read_b64_tr_b16 v[216:217], v234 offset:38912
ds_read_b64_tr_b16 v[218:219], v234 offset:39424
s_waitcnt lgkmcnt(6)
v_mfma_f32_32x32x16_bf16 v[176:191], v[204:207], v[48:51], v[176:191]
ds_read_b64_tr_b16 v[220:221], v235 offset:38912
ds_read_b64_tr_b16 v[222:223], v235 offset:39424
s_add_u32 m0, s96, 78848
s_add_u32 s62, s11, 40960
buffer_load_dwordx4 v242, s[36:39], s62 offen  lds
s_waitcnt lgkmcnt(6)
v_mfma_f32_32x32x16_bf16 v[128:143], v[208:211], v[52:55], v[128:143]
ds_read_b64_tr_b16 v[192:193], v234 offset:49152
ds_read_b64_tr_b16 v[194:195], v234 offset:49664
s_waitcnt lgkmcnt(6)
v_mfma_f32_32x32x16_bf16 v[144:159], v[212:215], v[52:55], v[144:159]
ds_read_b64_tr_b16 v[196:197], v235 offset:49152
ds_read_b64_tr_b16 v[198:199], v235 offset:49664
s_add_u32 m0, s96, 79872
s_add_u32 s62, s11, 49152
buffer_load_dwordx4 v242, s[36:39], s62 offen  lds
s_waitcnt lgkmcnt(6)
v_mfma_f32_32x32x16_bf16 v[160:175], v[216:219], v[52:55], v[160:175]
ds_read_b64_tr_b16 v[200:201], v234 offset:53248
ds_read_b64_tr_b16 v[202:203], v234 offset:53760
s_waitcnt lgkmcnt(6)
v_mfma_f32_32x32x16_bf16 v[176:191], v[220:223], v[52:55], v[176:191]
ds_read_b64_tr_b16 v[204:205], v235 offset:53248
ds_read_b64_tr_b16 v[206:207], v235 offset:53760
s_add_u32 m0, s96, 80896
s_add_u32 s62, s11, 57344
buffer_load_dwordx4 v242, s[36:39], s62 offen  lds
s_waitcnt lgkmcnt(6)
v_mfma_f32_32x32x16_bf16 v[128:143], v[192:195], v[56:59], v[128:143]
ds_read_b64_tr_b16 v[208:209], v234 offset:51200
ds_read_b64_tr_b16 v[210:211], v234 offset:51712
s_waitcnt lgkmcnt(6)
v_mfma_f32_32x32x16_bf16 v[144:159], v[196:199], v[56:59], v[144:159]
ds_read_b64_tr_b16 v[212:213], v235 offset:51200
ds_read_b64_tr_b16 v[214:215], v235 offset:51712
s_waitcnt lgkmcnt(6)
v_mfma_f32_32x32x16_bf16 v[160:175], v[200:203], v[56:59], v[160:175]
ds_read_b64_tr_b16 v[216:217], v234 offset:55296
ds_read_b64_tr_b16 v[218:219], v234 offset:55808
s_waitcnt lgkmcnt(6)
v_mfma_f32_32x32x16_bf16 v[176:191], v[204:207], v[56:59], v[176:191]
ds_read_b64_tr_b16 v[220:221], v235 offset:55296
ds_read_b64_tr_b16 v[222:223], v235 offset:55808
buffer_load_dwordx2 v[250:251], v225, s[48:51], 0 offen offset:0 sc1
s_waitcnt lgkmcnt(6)
v_mfma_f32_32x32x16_bf16 v[128:143], v[208:211], v[60:63], v[128:143]
ds_read_b64_tr_b16 v[192:193], v234 offset:8192
ds_read_b64_tr_b16 v[194:195], v234 offset:8704
s_waitcnt lgkmcnt(6)
v_mfma_f32_32x32x16_bf16 v[144:159], v[212:215], v[60:63], v[144:159]
ds_read_b64_tr_b16 v[196:197], v235 offset:8192
ds_read_b64_tr_b16 v[198:199], v235 offset:8704
s_waitcnt lgkmcnt(6)
v_mfma_f32_32x32x16_bf16 v[160:175], v[216:219], v[60:63], v[160:175]
ds_read_b64_tr_b16 v[200:201], v234 offset:12288
ds_read_b64_tr_b16 v[202:203], v234 offset:12800
s_waitcnt lgkmcnt(6)
v_mfma_f32_32x32x16_bf16 v[176:191], v[220:223], v[60:63], v[176:191]
ds_read_b64_tr_b16 v[204:205], v235 offset:12288
ds_read_b64_tr_b16 v[206:207], v235 offset:12800
s_waitcnt vmcnt(9)
ds_add_u32 v245, v246 offset:28672
s_waitcnt vmcnt(0)
s_setprio 3
v_cmp_ne_u64 vcc, v[248:249], v[250:251]
s_setprio 0
s_nop 0
s_cbranch_vccnz .Lk_ps_31
.Lk_pd_32:
s_waitcnt lgkmcnt(7)
v_mfma_f32_32x32x16_bf16 v[128:143], v[192:195], v[64:67], v[128:143]
ds_read_b64_tr_b16 v[208:209], v234 offset:10240
ds_read_b64_tr_b16 v[210:211], v234 offset:10752
s_waitcnt lgkmcnt(7)
v_mfma_f32_32x32x16_bf16 v[144:159], v[196:199], v[64:67], v[144:159]
ds_read_b64_tr_b16 v[212:213], v235 offset:10240
ds_read_b64_tr_b16 v[214:215], v235 offset:10752
s_add_u32 s62, s13, 0
buffer_load_dwordx4 v[0:3], v243, s[48:51], s62 offen offset:0 sc1
buffer_load_dwordx4 v[4:7], v243, s[48:51], s62 offen offset:256 sc1
s_waitcnt lgkmcnt(7)
v_mfma_f32_32x32x16_bf16 v[160:175], v[200:203], v[64:67], v[160:175]
ds_read_b64_tr_b16 v[216:217], v234 offset:14336
ds_read_b64_tr_b16 v[218:219], v234 offset:14848
s_waitcnt lgkmcnt(7)
v_mfma_f32_32x32x16_bf16 v[176:191], v[204:207], v[64:67], v[176:191]
ds_read_b64_tr_b16 v[220:221], v235 offset:14336
ds_read_b64_tr_b16 v[222:223], v235 offset:14848
s_add_u32 s62, s13, 147456
buffer_load_dwordx4 v[8:11], v243, s[48:51], s62 offen offset:0 sc1
buffer_load_dwordx4 v[12:15], v243, s[48:51], s62 offen offset:256 sc1
s_waitcnt lgkmcnt(6)
v_mfma_f32_32x32x16_bf16 v[128:143], v[208:211], v[68:71], v[128:143]
ds_read_b64_tr_b16 v[192:193], v234 offset:24576
ds_read_b64_tr_b16 v[194:195], v234 offset:25088
s_waitcnt lgkmcnt(6)
v_mfma_f32_32x32x16_bf16 v[144:159], v[212:215], v[68:71], v[144:159]
ds_read_b64_tr_b16 v[196:197], v235 offset:24576
ds_read_b64_tr_b16 v[198:199], v235 offset:25088
s_add_u32 s62, s13, 0
buffer_load_dwordx4 v[16:19], v243, s[48:51], s62 offen offset:2048 sc1
buffer_load_dwordx4 v[20:23], v243, s[48:51], s62 offen offset:2304 sc1
s_waitcnt lgkmcnt(6)
v_mfma_f32_32x32x16_bf16 v[160:175], v[216:219], v[68:71], v[160:175]
ds_read_b64_tr_b16 v[200:201], v234 offset:28672
ds_read_b64_tr_b16 v[202:203], v234 offset:29184
s_waitcnt lgkmcnt(6)
v_mfma_f32_32x32x16_bf16 v[176:191], v[220:223], v[68:71], v[176:191]
ds_read_b64_tr_b16 v[204:205], v235 offset:28672
ds_read_b64_tr_b16 v[206:207], v235 offset:29184
ds_read_b32 v227, v245 offset:28672
s_add_u32 s62, s13, 147456
buffer_load_dwordx4 v[24:27], v243, s[48:51], s62 offen offset:2048 sc1
buffer_load_dwordx4 v[28:31], v243, s[48:51], s62 offen offset:2304 sc1
s_waitcnt lgkmcnt(7)
v_mfma_f32_32x32x16_bf16 v[128:143], v[192:195], v[72:75], v[128:143]
ds_read_b64_tr_b16 v[208:209], v234 offset:26624
ds_read_b64_tr_b16 v[210:211], v234 offset:27136
s_waitcnt lgkmcnt(7)
v_mfma_f32_32x32x16_bf16 v[144:159], v[196:199], v[72:75], v[144:159]
s_waitcnt lgkmcnt(2)
s_setprio 3
v_cmp_gt_u32 vcc, 28, v227
s_setprio 0
s_nop 0
s_cbranch_vccnz .Lk_ls_33
.Lk_ld_34:
ds_read_b64_tr_b16 v[212:213], v235 offset:26624
ds_read_b64_tr_b16 v[214:215], v235 offset:27136
s_add_u32 s62, s13, 8192
buffer_load_dwordx4 v[32:35], v243, s[48:51], s62 offen offset:0 sc1
buffer_load_dwordx4 v[36:39], v243, s[48:51], s62 offen offset:256 sc1
v_mfma_f32_32x32x16_bf16 v[160:175], v[200:203], v[72:75], v[160:175]
ds_read_b64_tr_b16 v[216:217], v234 offset:30720
ds_read_b64_tr_b16 v[218:219], v234 offset:31232
v_mfma_f32_32x32x16_bf16 v[176:191], v[204:207], v[72:75], v[176:191]
ds_read_b64_tr_b16 v[220:221], v235 offset:30720
ds_read_b64_tr_b16 v[222:223], v235 offset:31232
s_add_u32 s62, s13, 155648
buffer_load_dwordx4 v[40:43], v243, s[48:51], s62 offen offset:0 sc1
buffer_load_dwordx4 v[44:47], v243, s[48:51], s62 offen offset:256 sc1
s_waitcnt lgkmcnt(6)
v_mfma_f32_32x32x16_bf16 v[128:143], v[208:211], v[76:79], v[128:143]
ds_read_b64_tr_b16 v[192:193], v234 offset:40960
ds_read_b64_tr_b16 v[194:195], v234 offset:41472
s_waitcnt lgkmcnt(6)
v_mfma_f32_32x32x16_bf16 v[144:159], v[212:215], v[76:79], v[144:159]
ds_read_b64_tr_b16 v[196:197], v235 offset:40960
ds_read_b64_tr_b16 v[198:199], v235 offset:41472
s_add_u32 s62, s13, 8192
buffer_load_dwordx4 v[48:51], v243, s[48:51], s62 offen offset:2048 sc1
buffer_load_dwordx4 v[52:55], v243, s[48:51], s62 offen offset:2304 sc1
s_waitcnt lgkmcnt(6)
v_mfma_f32_32x32x16_bf16 v[160:175], v[216:219], v[76:79], v[160:175]
ds_read_b64_tr_b16 v[200:201], v234 offset:45056
ds_read_b64_tr_b16 v[202:203], v234 offset:45568
s_waitcnt lgkmcnt(6)
v_mfma_f32_32x32x16_bf16 v[176:191], v[220:223], v[76:79], v[176:191]
ds_read_b64_tr_b16 v[204:205], v235 offset:45056
ds_read_b64_tr_b16 v[206:207], v235 offset:45568
s_add_u32 s62, s13, 155648
buffer_load_dwordx4 v[56:59], v243, s[48:51], s62 offen offset:2048 sc1
buffer_load_dwordx4 v[60:63], v243, s[48:51], s62 offen offset:2304 sc1
s_waitcnt lgkmcnt(6)
)ASM" R"ASM(
v_mfma_f32_32x32x16_bf16 v[128:143], v[192:195], v[80:83], v[128:143]
ds_read_b64_tr_b16 v[208:209], v234 offset:43008
ds_read_b64_tr_b16 v[210:211], v234 offset:43520
s_waitcnt lgkmcnt(6)
v_mfma_f32_32x32x16_bf16 v[144:159], v[196:199], v[80:83], v[144:159]
ds_read_b64_tr_b16 v[212:213], v235 offset:43008
ds_read_b64_tr_b16 v[214:215], v235 offset:43520
s_add_u32 m0, s96, 0
s_add_u32 s62, s11, 131072
buffer_load_dwordx4 v242, s[36:39], s62 offen  lds
s_waitcnt lgkmcnt(6)
v_mfma_f32_32x32x16_bf16 v[160:175], v[200:203], v[80:83], v[160:175]
ds_read_b64_tr_b16 v[216:217], v234 offset:47104
ds_read_b64_tr_b16 v[218:219], v234 offset:47616
s_waitcnt lgkmcnt(6)
v_mfma_f32_32x32x16_bf16 v[176:191], v[204:207], v[80:83], v[176:191]
ds_read_b64_tr_b16 v[220:221], v235 offset:47104
ds_read_b64_tr_b16 v[222:223], v235 offset:47616
s_add_u32 m0, s96, 1024
s_add_u32 s62, s11, 139264
buffer_load_dwordx4 v242, s[36:39], s62 offen  lds
s_waitcnt lgkmcnt(6)
v_mfma_f32_32x32x16_bf16 v[128:143], v[208:211], v[84:87], v[128:143]
ds_read_b64_tr_b16 v[192:193], v234 offset:57344
ds_read_b64_tr_b16 v[194:195], v234 offset:57856
s_waitcnt lgkmcnt(6)
v_mfma_f32_32x32x16_bf16 v[144:159], v[212:215], v[84:87], v[144:159]
ds_read_b64_tr_b16 v[196:197], v235 offset:57344
ds_read_b64_tr_b16 v[198:199], v235 offset:57856
s_add_u32 m0, s96, 2048
s_add_u32 s62, s11, 147456
buffer_load_dwordx4 v242, s[36:39], s62 offen  lds
s_waitcnt lgkmcnt(6)
v_mfma_f32_32x32x16_bf16 v[160:175], v[216:219], v[84:87], v[160:175]
ds_read_b64_tr_b16 v[200:201], v234 offset:61440
ds_read_b64_tr_b16 v[202:203], v234 offset:61952
s_waitcnt lgkmcnt(6)
v_mfma_f32_32x32x16_bf16 v[176:191], v[220:223], v[84:87], v[176:191]
ds_read_b64_tr_b16 v[204:205], v235 offset:61440
ds_read_b64_tr_b16 v[206:207], v235 offset:61952
s_add_u32 m0, s96, 3072
s_add_u32 s62, s11, 155648
buffer_load_dwordx4 v242, s[36:39], s62 offen  lds
s_waitcnt lgkmcnt(6)
v_mfma_f32_32x32x16_bf16 v[128:143], v[192:195], v[88:91], v[128:143]
ds_read_b64_tr_b16 v[208:209], v234 offset:59392
ds_read_b64_tr_b16 v[210:211], v234 offset:59904
s_waitcnt lgkmcnt(6)
v_mfma_f32_32x32x16_bf16 v[144:159], v[196:199], v[88:91], v[144:159]
ds_read_b64_tr_b16 v[212:213], v235 offset:59392
ds_read_b64_tr_b16 v[214:215], v235 offset:59904
s_add_u32 m0, s96, 4096
s_add_u32 s62, s11, 163840
buffer_load_dwordx4 v242, s[36:39], s62 offen  lds
s_waitcnt lgkmcnt(6)
v_mfma_f32_32x32x16_bf16 v[160:175], v[200:203], v[88:91], v[160:175]
ds_read_b64_tr_b16 v[216:217], v234 offset:63488
ds_read_b64_tr_b16 v[218:219], v234 offset:64000
s_waitcnt lgkmcnt(6)
v_mfma_f32_32x32x16_bf16 v[176:191], v[204:207], v[88:91], v[176:191]
ds_read_b64_tr_b16 v[220:221], v235 offset:63488
ds_read_b64_tr_b16 v[222:223], v235 offset:64000
s_add_u32 m0, s96, 5120
s_add_u32 s62, s11, 172032
buffer_load_dwordx4 v242, s[36:39], s62 offen  lds
s_waitcnt lgkmcnt(6)
v_mfma_f32_32x32x16_bf16 v[128:143], v[208:211], v[92:95], v[128:143]
ds_read_b64_tr_b16 v[192:193], v236 offset:0
ds_read_b64_tr_b16 v[194:195], v236 offset:512
s_waitcnt lgkmcnt(6)
v_mfma_f32_32x32x16_bf16 v[144:159], v[212:215], v[92:95], v[144:159]
ds_read_b64_tr_b16 v[196:197], v237 offset:0
ds_read_b64_tr_b16 v[198:199], v237 offset:512
s_add_u32 m0, s96, 6144
s_add_u32 s62, s11, 180224
buffer_load_dwordx4 v242, s[36:39], s62 offen  lds
s_waitcnt lgkmcnt(6)
v_mfma_f32_32x32x16_bf16 v[160:175], v[216:219], v[92:95], v[160:175]
ds_read_b64_tr_b16 v[200:201], v236 offset:4096
ds_read_b64_tr_b16 v[202:203], v236 offset:4608
s_waitcnt lgkmcnt(6)
v_mfma_f32_32x32x16_bf16 v[176:191], v[220:223], v[92:95], v[176:191]
ds_read_b64_tr_b16 v[204:205], v237 offset:4096
ds_read_b64_tr_b16 v[206:207], v237 offset:4608
s_add_u32 m0, s96, 7168
s_add_u32 s62, s11, 188416
buffer_load_dwordx4 v242, s[36:39], s62 offen  lds
ds_add_u32 v245, v246 offset:28672
s_waitcnt lgkmcnt(7)
v_mfma_f32_32x32x16_bf16 v[128:143], v[192:195], v[96:99], v[128:143]
ds_read_b64_tr_b16 v[208:209], v236 offset:2048
ds_read_b64_tr_b16 v[210:211], v236 offset:2560
s_waitcnt lgkmcnt(7)
v_mfma_f32_32x32x16_bf16 v[144:159], v[196:199], v[96:99], v[144:159]
ds_read_b64_tr_b16 v[212:213], v237 offset:2048
ds_read_b64_tr_b16 v[214:215], v237 offset:2560
s_add_u32 s62, s13, 16384
buffer_load_dwordx4 v[64:67], v243, s[48:51], s62 offen offset:0 sc1
buffer_load_dwordx4 v[68:71], v243, s[48:51], s62 offen offset:256 sc1
s_waitcnt lgkmcnt(7)
v_mfma_f32_32x32x16_bf16 v[160:175], v[200:203], v[96:99], v[160:175]
ds_read_b64_tr_b16 v[216:217], v236 offset:6144
ds_read_b64_tr_b16 v[218:219], v236 offset:6656
s_waitcnt lgkmcnt(7)
v_mfma_f32_32x32x16_bf16 v[176:191], v[204:207], v[96:99], v[176:191]
ds_read_b64_tr_b16 v[220:221], v237 offset:6144
ds_read_b64_tr_b16 v[222:223], v237 offset:6656
s_add_u32 s62, s13, 163840
buffer_load_dwordx4 v[72:75], v243, s[48:51], s62 offen offset:0 sc1
buffer_load_dwordx4 v[76:79], v243, s[48:51], s62 offen offset:256 sc1
s_waitcnt lgkmcnt(6)
v_mfma_f32_32x32x16_bf16 v[128:143], v[208:211], v[100:103], v[128:143]
ds_read_b64_tr_b16 v[192:193], v236 offset:16384
ds_read_b64_tr_b16 v[194:195], v236 offset:16896
s_waitcnt lgkmcnt(6)
v_mfma_f32_32x32x16_bf16 v[144:159], v[212:215], v[100:103], v[144:159]
ds_read_b64_tr_b16 v[196:197], v237 offset:16384
ds_read_b64_tr_b16 v[198:199], v237 offset:16896
s_add_u32 s62, s13, 16384
buffer_load_dwordx4 v[80:83], v243, s[48:51], s62 offen offset:2048 sc1
buffer_load_dwordx4 v[84:87], v243, s[48:51], s62 offen offset:2304 sc1
s_waitcnt lgkmcnt(6)
v_mfma_f32_32x32x16_bf16 v[160:175], v[216:219], v[100:103], v[160:175]
ds_read_b64_tr_b16 v[200:201], v236 offset:20480
ds_read_b64_tr_b16 v[202:203], v236 offset:20992
s_waitcnt lgkmcnt(6)
v_mfma_f32_32x32x16_bf16 v[176:191], v[220:223], v[100:103], v[176:191]
ds_read_b64_tr_b16 v[204:205], v237 offset:20480
ds_read_b64_tr_b16 v[206:207], v237 offset:20992
ds_read_b32 v227, v245 offset:28672
s_add_u32 s62, s13, 163840
buffer_load_dwordx4 v[88:91], v243, s[48:51], s62 offen offset:2048 sc1
buffer_load_dwordx4 v[92:95], v243, s[48:51], s62 offen offset:2304 sc1
s_waitcnt lgkmcnt(7)
v_mfma_f32_32x32x16_bf16 v[128:143], v[192:195], v[104:107], v[128:143]
ds_read_b64_tr_b16 v[208:209], v236 offset:18432
ds_read_b64_tr_b16 v[210:211], v236 offset:18944
s_waitcnt lgkmcnt(7)
v_mfma_f32_32x32x16_bf16 v[144:159], v[196:199], v[104:107], v[144:159]
s_waitcnt lgkmcnt(2)
s_setprio 3
v_cmp_gt_u32 vcc, 32, v227
s_setprio 0
s_nop 0
s_cbranch_vccnz .Lk_ls_35
.Lk_ld_36:
ds_read_b64_tr_b16 v[212:213], v237 offset:18432
ds_read_b64_tr_b16 v[214:215], v237 offset:18944
s_add_u32 m0, s96, 8192
s_add_u32 s62, s11, 262144
buffer_load_dwordx4 v242, s[36:39], s62 offen  lds
v_mfma_f32_32x32x16_bf16 v[160:175], v[200:203], v[104:107], v[160:175]
ds_read_b64_tr_b16 v[216:217], v236 offset:22528
ds_read_b64_tr_b16 v[218:219], v236 offset:23040
v_mfma_f32_32x32x16_bf16 v[176:191], v[204:207], v[104:107], v[176:191]
ds_read_b64_tr_b16 v[220:221], v237 offset:22528
ds_read_b64_tr_b16 v[222:223], v237 offset:23040
s_add_u32 m0, s96, 9216
s_add_u32 s62, s11, 270336
buffer_load_dwordx4 v242, s[36:39], s62 offen  lds
s_waitcnt lgkmcnt(6)
v_mfma_f32_32x32x16_bf16 v[128:143], v[208:211], v[108:111], v[128:143]
ds_read_b64_tr_b16 v[192:193], v236 offset:32768
ds_read_b64_tr_b16 v[194:195], v236 offset:33280
s_waitcnt lgkmcnt(6)
v_mfma_f32_32x32x16_bf16 v[144:159], v[212:215], v[108:111], v[144:159]
ds_read_b64_tr_b16 v[196:197], v237 offset:32768
ds_read_b64_tr_b16 v[198:199], v237 offset:33280
s_add_u32 m0, s96, 10240
s_add_u32 s62, s11, 278528
buffer_load_dwordx4 v242, s[36:39], s62 offen  lds
s_waitcnt lgkmcnt(6)
v_mfma_f32_32x32x16_bf16 v[160:175], v[216:219], v[108:111], v[160:175]
ds_read_b64_tr_b16 v[200:201], v236 offset:36864
ds_read_b64_tr_b16 v[202:203], v236 offset:37376
s_waitcnt lgkmcnt(6)
v_mfma_f32_32x32x16_bf16 v[176:191], v[220:223], v[108:111], v[176:191]
ds_read_b64_tr_b16 v[204:205], v237 offset:36864
ds_read_b64_tr_b16 v[206:207], v237 offset:37376
s_add_u32 m0, s96, 11264
s_add_u32 s62, s11, 286720
buffer_load_dwordx4 v242, s[36:39], s62 offen  lds
s_waitcnt lgkmcnt(6)
v_mfma_f32_32x32x16_bf16 v[128:143], v[192:195], v[112:115], v[128:143]
ds_read_b64_tr_b16 v[208:209], v236 offset:34816
ds_read_b64_tr_b16 v[210:211], v236 offset:35328
s_waitcnt lgkmcnt(6)
v_mfma_f32_32x32x16_bf16 v[144:159], v[196:199], v[112:115], v[144:159]
ds_read_b64_tr_b16 v[212:213], v237 offset:34816
ds_read_b64_tr_b16 v[214:215], v237 offset:35328
s_add_u32 m0, s96, 12288
s_add_u32 s62, s11, 294912
buffer_load_dwordx4 v242, s[36:39], s62 offen  lds
s_waitcnt lgkmcnt(6)
v_mfma_f32_32x32x16_bf16 v[160:175], v[200:203], v[112:115], v[160:175]
ds_read_b64_tr_b16 v[216:217], v236 offset:38912
ds_read_b64_tr_b16 v[218:219], v236 offset:39424
s_waitcnt lgkmcnt(6)
v_mfma_f32_32x32x16_bf16 v[176:191], v[204:207], v[112:115], v[176:191]
ds_read_b64_tr_b16 v[220:221], v237 offset:38912
)ASM" R"ASM(
ds_read_b64_tr_b16 v[222:223], v237 offset:39424
s_add_u32 m0, s96, 13312
s_add_u32 s62, s11, 303104
buffer_load_dwordx4 v242, s[36:39], s62 offen  lds
s_waitcnt lgkmcnt(6)
v_mfma_f32_32x32x16_bf16 v[128:143], v[208:211], v[116:119], v[128:143]
ds_read_b64_tr_b16 v[192:193], v236 offset:49152
ds_read_b64_tr_b16 v[194:195], v236 offset:49664
s_waitcnt lgkmcnt(6)
v_mfma_f32_32x32x16_bf16 v[144:159], v[212:215], v[116:119], v[144:159]
ds_read_b64_tr_b16 v[196:197], v237 offset:49152
ds_read_b64_tr_b16 v[198:199], v237 offset:49664
s_add_u32 m0, s96, 14336
s_add_u32 s62, s11, 311296
buffer_load_dwordx4 v242, s[36:39], s62 offen  lds
s_waitcnt lgkmcnt(6)
v_mfma_f32_32x32x16_bf16 v[160:175], v[216:219], v[116:119], v[160:175]
ds_read_b64_tr_b16 v[200:201], v236 offset:53248
ds_read_b64_tr_b16 v[202:203], v236 offset:53760
s_waitcnt lgkmcnt(6)
v_mfma_f32_32x32x16_bf16 v[176:191], v[220:223], v[116:119], v[176:191]
ds_read_b64_tr_b16 v[204:205], v237 offset:53248
ds_read_b64_tr_b16 v[206:207], v237 offset:53760
s_add_u32 m0, s96, 15360
s_add_u32 s62, s11, 319488
buffer_load_dwordx4 v242, s[36:39], s62 offen  lds
s_waitcnt lgkmcnt(6)
v_mfma_f32_32x32x16_bf16 v[128:143], v[192:195], v[120:123], v[128:143]
ds_read_b64_tr_b16 v[208:209], v236 offset:51200
ds_read_b64_tr_b16 v[210:211], v236 offset:51712
s_waitcnt lgkmcnt(6)
v_mfma_f32_32x32x16_bf16 v[144:159], v[196:199], v[120:123], v[144:159]
ds_read_b64_tr_b16 v[212:213], v237 offset:51200
ds_read_b64_tr_b16 v[214:215], v237 offset:51712
s_waitcnt lgkmcnt(6)
v_mfma_f32_32x32x16_bf16 v[160:175], v[200:203], v[120:123], v[160:175]
ds_read_b64_tr_b16 v[216:217], v236 offset:55296
ds_read_b64_tr_b16 v[218:219], v236 offset:55808
s_waitcnt lgkmcnt(6)
v_mfma_f32_32x32x16_bf16 v[176:191], v[204:207], v[120:123], v[176:191]
ds_read_b64_tr_b16 v[220:221], v237 offset:55296
ds_read_b64_tr_b16 v[222:223], v237 offset:55808
s_waitcnt lgkmcnt(6)
v_mfma_f32_32x32x16_bf16 v[128:143], v[208:211], v[124:127], v[128:143]
s_waitcnt lgkmcnt(4)
v_mfma_f32_32x32x16_bf16 v[144:159], v[212:215], v[124:127], v[144:159]
s_waitcnt lgkmcnt(2)
v_mfma_f32_32x32x16_bf16 v[160:175], v[216:219], v[124:127], v[160:175]
s_waitcnt lgkmcnt(0)
v_mfma_f32_32x32x16_bf16 v[176:191], v[220:223], v[124:127], v[176:191]
ds_add_u32 v245, v246 offset:28672
s_add_u32 s62, s13, 24576
buffer_load_dwordx4 v[96:99], v243, s[48:51], s62 offen offset:0 sc1
buffer_load_dwordx4 v[100:103], v243, s[48:51], s62 offen offset:256 sc1
s_add_u32 s62, s13, 172032
buffer_load_dwordx4 v[104:107], v243, s[48:51], s62 offen offset:0 sc1
buffer_load_dwordx4 v[108:111], v243, s[48:51], s62 offen offset:256 sc1
s_add_u32 s62, s13, 24576
buffer_load_dwordx4 v[112:115], v243, s[48:51], s62 offen offset:2048 sc1
buffer_load_dwordx4 v[116:119], v243, s[48:51], s62 offen offset:2304 sc1
s_add_u32 s62, s13, 172032
buffer_load_dwordx4 v[120:123], v243, s[48:51], s62 offen offset:2048 sc1
buffer_load_dwordx4 v[124:127], v243, s[48:51], s62 offen offset:2304 sc1
s_setprio 3
v_pk_mul_f32 v[128:129], v[128:129], v[232:233]
v_pk_mul_f32 v[130:131], v[130:131], v[232:233]
v_pk_mul_f32 v[132:133], v[132:133], v[232:233]
v_pk_mul_f32 v[134:135], v[134:135], v[232:233]
v_exp_f32 v128, v128
v_exp_f32 v129, v129
v_exp_f32 v130, v130
v_exp_f32 v131, v131
v_exp_f32 v132, v132
v_exp_f32 v133, v133
v_exp_f32 v134, v134
v_exp_f32 v135, v135
v_pk_fma_f32 v[128:129], v[128:129], v[230:231], v[230:231]
v_pk_fma_f32 v[130:131], v[130:131], v[230:231], v[230:231]
v_pk_fma_f32 v[132:133], v[132:133], v[230:231], v[230:231]
v_pk_fma_f32 v[134:135], v[134:135], v[230:231], v[230:231]
v_rcp_f32 v128, v128
v_rcp_f32 v129, v129
v_rcp_f32 v130, v130
v_rcp_f32 v131, v131
v_rcp_f32 v132, v132
v_rcp_f32 v133, v133
v_rcp_f32 v134, v134
v_rcp_f32 v135, v135
v_pk_mul_f32 v[160:161], v[160:161], v[128:129]
v_pk_mul_f32 v[162:163], v[162:163], v[130:131]
v_pk_mul_f32 v[164:165], v[164:165], v[132:133]
v_pk_mul_f32 v[166:167], v[166:167], v[134:135]
v_cvt_pk_bf16_f32 v128, v160, v161
v_cvt_pk_bf16_f32 v129, v162, v163
v_cvt_pk_bf16_f32 v130, v164, v165
v_cvt_pk_bf16_f32 v131, v166, v167
buffer_store_dwordx4 v[128:131], v244, s[48:51], s19 offen offset:0 sc1
v_pk_mul_f32 v[136:137], v[136:137], v[232:233]
v_pk_mul_f32 v[138:139], v[138:139], v[232:233]
v_pk_mul_f32 v[140:141], v[140:141], v[232:233]
v_pk_mul_f32 v[142:143], v[142:143], v[232:233]
v_exp_f32 v136, v136
v_exp_f32 v137, v137
v_exp_f32 v138, v138
v_exp_f32 v139, v139
v_exp_f32 v140, v140
v_exp_f32 v141, v141
v_exp_f32 v142, v142
v_exp_f32 v143, v143
v_pk_fma_f32 v[136:137], v[136:137], v[230:231], v[230:231]
v_pk_fma_f32 v[138:139], v[138:139], v[230:231], v[230:231]
v_pk_fma_f32 v[140:141], v[140:141], v[230:231], v[230:231]
v_pk_fma_f32 v[142:143], v[142:143], v[230:231], v[230:231]
v_rcp_f32 v136, v136
v_rcp_f32 v137, v137
v_rcp_f32 v138, v138
v_rcp_f32 v139, v139
v_rcp_f32 v140, v140
v_rcp_f32 v141, v141
v_rcp_f32 v142, v142
v_rcp_f32 v143, v143
v_pk_mul_f32 v[168:169], v[168:169], v[136:137]
v_pk_mul_f32 v[170:171], v[170:171], v[138:139]
v_pk_mul_f32 v[172:173], v[172:173], v[140:141]
v_pk_mul_f32 v[174:175], v[174:175], v[142:143]
v_cvt_pk_bf16_f32 v136, v168, v169
v_cvt_pk_bf16_f32 v137, v170, v171
v_cvt_pk_bf16_f32 v138, v172, v173
v_cvt_pk_bf16_f32 v139, v174, v175
buffer_store_dwordx4 v[136:139], v244, s[48:51], s19 offen offset:1024 sc1
v_pk_mul_f32 v[144:145], v[144:145], v[232:233]
v_pk_mul_f32 v[146:147], v[146:147], v[232:233]
v_pk_mul_f32 v[148:149], v[148:149], v[232:233]
v_pk_mul_f32 v[150:151], v[150:151], v[232:233]
v_exp_f32 v144, v144
v_exp_f32 v145, v145
v_exp_f32 v146, v146
v_exp_f32 v147, v147
v_exp_f32 v148, v148
v_exp_f32 v149, v149
v_exp_f32 v150, v150
v_exp_f32 v151, v151
v_pk_fma_f32 v[144:145], v[144:145], v[230:231], v[230:231]
v_pk_fma_f32 v[146:147], v[146:147], v[230:231], v[230:231]
v_pk_fma_f32 v[148:149], v[148:149], v[230:231], v[230:231]
v_pk_fma_f32 v[150:151], v[150:151], v[230:231], v[230:231]
v_rcp_f32 v144, v144
v_rcp_f32 v145, v145
v_rcp_f32 v146, v146
v_rcp_f32 v147, v147
v_rcp_f32 v148, v148
v_rcp_f32 v149, v149
v_rcp_f32 v150, v150
v_rcp_f32 v151, v151
v_pk_mul_f32 v[176:177], v[176:177], v[144:145]
v_pk_mul_f32 v[178:179], v[178:179], v[146:147]
v_pk_mul_f32 v[180:181], v[180:181], v[148:149]
v_pk_mul_f32 v[182:183], v[182:183], v[150:151]
v_cvt_pk_bf16_f32 v144, v176, v177
v_cvt_pk_bf16_f32 v145, v178, v179
v_cvt_pk_bf16_f32 v146, v180, v181
v_cvt_pk_bf16_f32 v147, v182, v183
buffer_store_dwordx4 v[144:147], v244, s[48:51], s19 offen offset:2048 sc1
v_pk_mul_f32 v[152:153], v[152:153], v[232:233]
v_pk_mul_f32 v[154:155], v[154:155], v[232:233]
v_pk_mul_f32 v[156:157], v[156:157], v[232:233]
v_pk_mul_f32 v[158:159], v[158:159], v[232:233]
v_exp_f32 v152, v152
v_exp_f32 v153, v153
v_exp_f32 v154, v154
v_exp_f32 v155, v155
v_exp_f32 v156, v156
v_exp_f32 v157, v157
v_exp_f32 v158, v158
v_exp_f32 v159, v159
v_pk_fma_f32 v[152:153], v[152:153], v[230:231], v[230:231]
v_pk_fma_f32 v[154:155], v[154:155], v[230:231], v[230:231]
v_pk_fma_f32 v[156:157], v[156:157], v[230:231], v[230:231]
v_pk_fma_f32 v[158:159], v[158:159], v[230:231], v[230:231]
v_rcp_f32 v152, v152
v_rcp_f32 v153, v153
v_rcp_f32 v154, v154
v_rcp_f32 v155, v155
v_rcp_f32 v156, v156
v_rcp_f32 v157, v157
v_rcp_f32 v158, v158
v_rcp_f32 v159, v159
v_pk_mul_f32 v[184:185], v[184:185], v[152:153]
v_pk_mul_f32 v[186:187], v[186:187], v[154:155]
v_pk_mul_f32 v[188:189], v[188:189], v[156:157]
v_pk_mul_f32 v[190:191], v[190:191], v[158:159]
v_cvt_pk_bf16_f32 v152, v184, v185
v_cvt_pk_bf16_f32 v153, v186, v187
v_cvt_pk_bf16_f32 v154, v188, v189
v_cvt_pk_bf16_f32 v155, v190, v191
buffer_store_dwordx4 v[152:155], v244, s[48:51], s19 offen offset:3072 sc1
v_bfe_u32 v176, v224, 2, 3
v_lshrrev_b32 v177, 5, v224
v_lshl_add_u32 v176, v177, 4, v176
v_bfe_u32 v177, v224, 3, 2
)ASM" R"ASM(
v_lshl_add_u32 v177, v176, 2, v177
v_and_b32 v178, 3, v224
v_lshlrev_b32 v178, 3, v178
v_lshl_add_u32 v234, v177, 5, v178
v_add_u32 v234, s96, v234
v_xor_b32 v235, 32, v234
v_xor_b32 v236, 64, v234
v_xor_b32 v237, 96, v234
v_add_u32 v238, 0x10000, v234
v_add_u32 v239, 0x10000, v235
v_add_u32 v232, 0x10000, v236
v_add_u32 v233, 0x10000, v237
s_setprio 0
ds_read_b32 v227, v245 offset:28672
ds_read_b64_tr_b16 v[192:193], v238 offset:8192
ds_read_b64_tr_b16 v[194:195], v238 offset:9216
ds_read_b64_tr_b16 v[196:197], v239 offset:8192
ds_read_b64_tr_b16 v[198:199], v239 offset:9216
ds_read_b64_tr_b16 v[200:201], v232 offset:8192
ds_read_b64_tr_b16 v[202:203], v232 offset:9216
ds_read_b64_tr_b16 v[204:205], v233 offset:8192
ds_read_b64_tr_b16 v[206:207], v233 offset:9216
s_waitcnt lgkmcnt(8)
s_setprio 3
v_cmp_gt_u32 vcc, 36, v227
s_setprio 0
s_nop 0
s_cbranch_vccnz .Lk_ls_37
.Lk_ld_38:
s_waitcnt vmcnt(48)
s_waitcnt lgkmcnt(6)
v_mfma_f32_16x16x32_bf16 v[128:131], v[192:195], v[0:3], 0
v_mfma_f32_16x16x32_bf16 v[132:135], v[192:195], v[4:7], 0
ds_read_b64_tr_b16 v[208:209], v238 offset:12288
v_mfma_f32_16x16x32_bf16 v[136:139], v[192:195], v[8:11], 0
ds_read_b64_tr_b16 v[210:211], v238 offset:13312
v_mfma_f32_16x16x32_bf16 v[140:143], v[192:195], v[12:15], 0
s_add_u32 m0, s96, 65536
s_add_u32 s62, s11, 393216
buffer_load_dwordx4 v242, s[36:39], s62 offen  lds
s_waitcnt lgkmcnt(6)
v_mfma_f32_16x16x32_bf16 v[144:147], v[196:199], v[0:3], 0
v_mfma_f32_16x16x32_bf16 v[148:151], v[196:199], v[4:7], 0
ds_read_b64_tr_b16 v[212:213], v239 offset:12288
v_mfma_f32_16x16x32_bf16 v[152:155], v[196:199], v[8:11], 0
ds_read_b64_tr_b16 v[214:215], v239 offset:13312
v_mfma_f32_16x16x32_bf16 v[156:159], v[196:199], v[12:15], 0
s_add_u32 m0, s96, 66560
s_add_u32 s62, s11, 401408
buffer_load_dwordx4 v242, s[36:39], s62 offen  lds
s_waitcnt lgkmcnt(6)
v_mfma_f32_16x16x32_bf16 v[160:163], v[200:203], v[0:3], 0
v_mfma_f32_16x16x32_bf16 v[164:167], v[200:203], v[4:7], 0
ds_read_b64_tr_b16 v[216:217], v232 offset:12288
v_mfma_f32_16x16x32_bf16 v[168:171], v[200:203], v[8:11], 0
ds_read_b64_tr_b16 v[218:219], v232 offset:13312
v_mfma_f32_16x16x32_bf16 v[172:175], v[200:203], v[12:15], 0
s_add_u32 m0, s96, 67584
s_add_u32 s62, s11, 409600
buffer_load_dwordx4 v242, s[36:39], s62 offen  lds
s_waitcnt lgkmcnt(6)
v_mfma_f32_16x16x32_bf16 v[176:179], v[204:207], v[0:3], 0
v_mfma_f32_16x16x32_bf16 v[180:183], v[204:207], v[4:7], 0
ds_read_b64_tr_b16 v[220:221], v233 offset:12288
v_mfma_f32_16x16x32_bf16 v[184:187], v[204:207], v[8:11], 0
ds_read_b64_tr_b16 v[222:223], v233 offset:13312
v_mfma_f32_16x16x32_bf16 v[188:191], v[204:207], v[12:15], 0
s_add_u32 m0, s96, 68608
s_add_u32 s62, s11, 417792
buffer_load_dwordx4 v242, s[36:39], s62 offen  lds
s_waitcnt vmcnt(48)
s_waitcnt lgkmcnt(6)
v_mfma_f32_16x16x32_bf16 v[128:131], v[208:211], v[16:19], v[128:131]
v_mfma_f32_16x16x32_bf16 v[132:135], v[208:211], v[20:23], v[132:135]
v_mfma_f32_16x16x32_bf16 v[136:139], v[208:211], v[24:27], v[136:139]
v_mfma_f32_16x16x32_bf16 v[140:143], v[208:211], v[28:31], v[140:143]
s_waitcnt vmcnt(32)
ds_read_b64_tr_b16 v[192:193], v234 offset:0
ds_read_b64_tr_b16 v[194:195], v234 offset:1024
s_add_u32 m0, s96, 69632
s_add_u32 s62, s11, 425984
buffer_load_dwordx4 v242, s[36:39], s62 offen  lds
s_waitcnt lgkmcnt(6)
v_mfma_f32_16x16x32_bf16 v[144:147], v[212:215], v[16:19], v[144:147]
v_mfma_f32_16x16x32_bf16 v[148:151], v[212:215], v[20:23], v[148:151]
ds_read_b64_tr_b16 v[196:197], v235 offset:0
v_mfma_f32_16x16x32_bf16 v[152:155], v[212:215], v[24:27], v[152:155]
ds_read_b64_tr_b16 v[198:199], v235 offset:1024
v_mfma_f32_16x16x32_bf16 v[156:159], v[212:215], v[28:31], v[156:159]
s_add_u32 m0, s96, 70656
s_add_u32 s62, s11, 434176
buffer_load_dwordx4 v242, s[36:39], s62 offen  lds
s_waitcnt vmcnt(6)
s_mov_b64 exec, 1
buffer_store_dwordx2 v[248:249], off, s[48:51], s16 offset:8 sc1
s_mov_b64 exec, -1
s_waitcnt lgkmcnt(6)
v_mfma_f32_16x16x32_bf16 v[160:163], v[216:219], v[16:19], v[160:163]
v_mfma_f32_16x16x32_bf16 v[164:167], v[216:219], v[20:23], v[164:167]
ds_read_b64_tr_b16 v[200:201], v236 offset:0
v_mfma_f32_16x16x32_bf16 v[168:171], v[216:219], v[24:27], v[168:171]
ds_read_b64_tr_b16 v[202:203], v236 offset:1024
v_mfma_f32_16x16x32_bf16 v[172:175], v[216:219], v[28:31], v[172:175]
s_add_u32 m0, s96, 71680
s_add_u32 s62, s11, 442368
buffer_load_dwordx4 v242, s[36:39], s62 offen  lds
s_waitcnt lgkmcnt(6)
v_mfma_f32_16x16x32_bf16 v[176:179], v[220:223], v[16:19], v[176:179]
v_mfma_f32_16x16x32_bf16 v[180:183], v[220:223], v[20:23], v[180:183]
ds_read_b64_tr_b16 v[204:205], v237 offset:0
v_mfma_f32_16x16x32_bf16 v[184:187], v[220:223], v[24:27], v[184:187]
ds_read_b64_tr_b16 v[206:207], v237 offset:1024
v_mfma_f32_16x16x32_bf16 v[188:191], v[220:223], v[28:31], v[188:191]
s_add_u32 m0, s96, 72704
s_add_u32 s62, s11, 450560
buffer_load_dwordx4 v242, s[36:39], s62 offen  lds
s_waitcnt lgkmcnt(6)
v_mfma_f32_16x16x32_bf16 v[128:131], v[192:195], v[32:35], v[128:131]
v_mfma_f32_16x16x32_bf16 v[132:135], v[192:195], v[36:39], v[132:135]
ds_read_b64_tr_b16 v[208:209], v234 offset:4096
v_mfma_f32_16x16x32_bf16 v[136:139], v[192:195], v[40:43], v[136:139]
ds_read_b64_tr_b16 v[210:211], v234 offset:5120
v_mfma_f32_16x16x32_bf16 v[140:143], v[192:195], v[44:47], v[140:143]
s_add_u32 m0, s96, 73728
s_add_u32 s62, s11, 65536
buffer_load_dwordx4 v242, s[36:39], s62 offen  lds
s_add_u32 m0, s96, 74752
s_add_u32 s62, s11, 73728
buffer_load_dwordx4 v242, s[36:39], s62 offen  lds
s_waitcnt lgkmcnt(6)
v_mfma_f32_16x16x32_bf16 v[144:147], v[196:199], v[32:35], v[144:147]
v_mfma_f32_16x16x32_bf16 v[148:151], v[196:199], v[36:39], v[148:151]
ds_read_b64_tr_b16 v[212:213], v235 offset:4096
v_mfma_f32_16x16x32_bf16 v[152:155], v[196:199], v[40:43], v[152:155]
ds_read_b64_tr_b16 v[214:215], v235 offset:5120
v_mfma_f32_16x16x32_bf16 v[156:159], v[196:199], v[44:47], v[156:159]
s_add_u32 m0, s96, 75776
s_add_u32 s62, s11, 81920
buffer_load_dwordx4 v242, s[36:39], s62 offen  lds
s_add_u32 m0, s96, 76800
s_add_u32 s62, s11, 90112
buffer_load_dwordx4 v242, s[36:39], s62 offen  lds
s_waitcnt lgkmcnt(6)
v_mfma_f32_16x16x32_bf16 v[160:163], v[200:203], v[32:35], v[160:163]
v_mfma_f32_16x16x32_bf16 v[164:167], v[200:203], v[36:39], v[164:167]
ds_read_b64_tr_b16 v[216:217], v236 offset:4096
v_mfma_f32_16x16x32_bf16 v[168:171], v[200:203], v[40:43], v[168:171]
ds_read_b64_tr_b16 v[218:219], v236 offset:5120
v_mfma_f32_16x16x32_bf16 v[172:175], v[200:203], v[44:47], v[172:175]
s_add_u32 m0, s96, 77824
s_add_u32 s62, s11, 98304
buffer_load_dwordx4 v242, s[36:39], s62 offen  lds
s_add_u32 m0, s96, 78848
s_add_u32 s62, s11, 106496
buffer_load_dwordx4 v242, s[36:39], s62 offen  lds
s_waitcnt lgkmcnt(6)
v_mfma_f32_16x16x32_bf16 v[176:179], v[204:207], v[32:35], v[176:179]
v_mfma_f32_16x16x32_bf16 v[180:183], v[204:207], v[36:39], v[180:183]
ds_read_b64_tr_b16 v[220:221], v237 offset:4096
v_mfma_f32_16x16x32_bf16 v[184:187], v[204:207], v[40:43], v[184:187]
ds_read_b64_tr_b16 v[222:223], v237 offset:5120
v_mfma_f32_16x16x32_bf16 v[188:191], v[204:207], v[44:47], v[188:191]
s_add_u32 m0, s96, 79872
s_add_u32 s62, s11, 114688
buffer_load_dwordx4 v242, s[36:39], s62 offen  lds
s_add_u32 m0, s96, 80896
s_add_u32 s62, s11, 122880
buffer_load_dwordx4 v242, s[36:39], s62 offen  lds
s_waitcnt lgkmcnt(6)
v_mfma_f32_16x16x32_bf16 v[128:131], v[208:211], v[48:51], v[128:131]
v_mfma_f32_16x16x32_bf16 v[132:135], v[208:211], v[52:55], v[132:135]
ds_read_b64_tr_b16 v[192:193], v234 offset:8192
v_mfma_f32_16x16x32_bf16 v[136:139], v[208:211], v[56:59], v[136:139]
ds_read_b64_tr_b16 v[194:195], v234 offset:9216
v_mfma_f32_16x16x32_bf16 v[140:143], v[208:211], v[60:63], v[140:143]
s_add_u32 m0, s96, 0
s_add_u32 s62, s11, 196608
buffer_load_dwordx4 v242, s[36:39], s62 offen  lds
s_add_u32 m0, s96, 1024
s_add_u32 s62, s11, 204800
buffer_load_dwordx4 v242, s[36:39], s62 offen  lds
s_waitcnt lgkmcnt(6)
v_mfma_f32_16x16x32_bf16 v[144:147], v[212:215], v[48:51], v[144:147]
v_mfma_f32_16x16x32_bf16 v[148:151], v[212:215], v[52:55], v[148:151]
ds_read_b64_tr_b16 v[196:197], v235 offset:8192
v_mfma_f32_16x16x32_bf16 v[152:155], v[212:215], v[56:59], v[152:155]
ds_read_b64_tr_b16 v[198:199], v235 offset:9216
v_mfma_f32_16x16x32_bf16 v[156:159], v[212:215], v[60:63], v[156:159]
s_add_u32 m0, s96, 2048
s_add_u32 s62, s11, 212992
buffer_load_dwordx4 v242, s[36:39], s62 offen  lds
s_add_u32 m0, s96, 3072
s_add_u32 s62, s11, 221184
buffer_load_dwordx4 v242, s[36:39], s62 offen  lds
buffer_load_dwordx2 v[250:251], v225, s[48:51], 0 offen offset:8 sc1
s_waitcnt lgkmcnt(6)
v_mfma_f32_16x16x32_bf16 v[160:163], v[216:219], v[48:51], v[160:163]
v_mfma_f32_16x16x32_bf16 v[164:167], v[216:219], v[52:55], v[164:167]
ds_read_b64_tr_b16 v[200:201], v236 offset:8192
v_mfma_f32_16x16x32_bf16 v[168:171], v[216:219], v[56:59], v[168:171]
)ASM" R"ASM(
ds_read_b64_tr_b16 v[202:203], v236 offset:9216
v_mfma_f32_16x16x32_bf16 v[172:175], v[216:219], v[60:63], v[172:175]
s_waitcnt lgkmcnt(6)
v_mfma_f32_16x16x32_bf16 v[176:179], v[220:223], v[48:51], v[176:179]
v_mfma_f32_16x16x32_bf16 v[180:183], v[220:223], v[52:55], v[180:183]
ds_read_b64_tr_b16 v[204:205], v237 offset:8192
v_mfma_f32_16x16x32_bf16 v[184:187], v[220:223], v[56:59], v[184:187]
ds_read_b64_tr_b16 v[206:207], v237 offset:9216
v_mfma_f32_16x16x32_bf16 v[188:191], v[220:223], v[60:63], v[188:191]
s_add_u32 m0, s96, 4096
s_add_u32 s62, s11, 229376
buffer_load_dwordx4 v242, s[36:39], s62 offen  lds
s_add_u32 m0, s96, 5120
s_add_u32 s62, s11, 237568
buffer_load_dwordx4 v242, s[36:39], s62 offen  lds
s_waitcnt lgkmcnt(6)
v_mfma_f32_16x16x32_bf16 v[128:131], v[192:195], v[64:67], v[128:131]
v_mfma_f32_16x16x32_bf16 v[132:135], v[192:195], v[68:71], v[132:135]
ds_read_b64_tr_b16 v[208:209], v234 offset:12288
v_mfma_f32_16x16x32_bf16 v[136:139], v[192:195], v[72:75], v[136:139]
ds_read_b64_tr_b16 v[210:211], v234 offset:13312
v_mfma_f32_16x16x32_bf16 v[140:143], v[192:195], v[76:79], v[140:143]
s_add_u32 m0, s96, 6144
s_add_u32 s62, s11, 245760
buffer_load_dwordx4 v242, s[36:39], s62 offen  lds
s_add_u32 m0, s96, 7168
s_add_u32 s62, s11, 253952
buffer_load_dwordx4 v242, s[36:39], s62 offen  lds
s_waitcnt lgkmcnt(6)
v_mfma_f32_16x16x32_bf16 v[144:147], v[196:199], v[64:67], v[144:147]
v_mfma_f32_16x16x32_bf16 v[148:151], v[196:199], v[68:71], v[148:151]
ds_read_b64_tr_b16 v[212:213], v235 offset:12288
v_mfma_f32_16x16x32_bf16 v[152:155], v[196:199], v[72:75], v[152:155]
ds_read_b64_tr_b16 v[214:215], v235 offset:13312
v_mfma_f32_16x16x32_bf16 v[156:159], v[196:199], v[76:79], v[156:159]
s_waitcnt vmcnt(4)
s_setprio 3
v_cmp_ne_u64 vcc, v[248:249], v[250:251]
s_setprio 0
s_nop 0
s_cbranch_vccnz .Lk_ps_39
.Lk_pd_40:
s_waitcnt lgkmcnt(6)
v_mfma_f32_16x16x32_bf16 v[160:163], v[200:203], v[64:67], v[160:163]
v_mfma_f32_16x16x32_bf16 v[164:167], v[200:203], v[68:71], v[164:167]
ds_read_b64_tr_b16 v[216:217], v236 offset:12288
v_mfma_f32_16x16x32_bf16 v[168:171], v[200:203], v[72:75], v[168:171]
ds_read_b64_tr_b16 v[218:219], v236 offset:13312
v_mfma_f32_16x16x32_bf16 v[172:175], v[200:203], v[76:79], v[172:175]
s_add_u32 s62, s13, 4096
buffer_load_dwordx4 v[0:3], v243, s[48:51], s62 offen offset:0 sc1
buffer_load_dwordx4 v[4:7], v243, s[48:51], s62 offen offset:256 sc1
s_waitcnt lgkmcnt(6)
v_mfma_f32_16x16x32_bf16 v[176:179], v[204:207], v[64:67], v[176:179]
v_mfma_f32_16x16x32_bf16 v[180:183], v[204:207], v[68:71], v[180:183]
ds_read_b64_tr_b16 v[220:221], v237 offset:12288
v_mfma_f32_16x16x32_bf16 v[184:187], v[204:207], v[72:75], v[184:187]
ds_read_b64_tr_b16 v[222:223], v237 offset:13312
v_mfma_f32_16x16x32_bf16 v[188:191], v[204:207], v[76:79], v[188:191]
s_add_u32 m0, s96, 8192
s_add_u32 s62, s11, 327680
buffer_load_dwordx4 v242, s[36:39], s62 offen  lds
s_add_u32 m0, s96, 9216
s_add_u32 s62, s11, 335872
buffer_load_dwordx4 v242, s[36:39], s62 offen  lds
s_add_u32 s62, s13, 151552
buffer_load_dwordx4 v[8:11], v243, s[48:51], s62 offen offset:0 sc1
buffer_load_dwordx4 v[12:15], v243, s[48:51], s62 offen offset:256 sc1
s_waitcnt lgkmcnt(6)
v_mfma_f32_16x16x32_bf16 v[128:131], v[208:211], v[80:83], v[128:131]
v_mfma_f32_16x16x32_bf16 v[132:135], v[208:211], v[84:87], v[132:135]
ds_read_b64_tr_b16 v[192:193], v238 offset:0
v_mfma_f32_16x16x32_bf16 v[136:139], v[208:211], v[88:91], v[136:139]
ds_read_b64_tr_b16 v[194:195], v238 offset:1024
v_mfma_f32_16x16x32_bf16 v[140:143], v[208:211], v[92:95], v[140:143]
s_add_u32 m0, s96, 10240
s_add_u32 s62, s11, 344064
buffer_load_dwordx4 v242, s[36:39], s62 offen  lds
s_add_u32 m0, s96, 11264
s_add_u32 s62, s11, 352256
buffer_load_dwordx4 v242, s[36:39], s62 offen  lds
s_add_u32 s62, s13, 4096
buffer_load_dwordx4 v[16:19], v243, s[48:51], s62 offen offset:2048 sc1
buffer_load_dwordx4 v[20:23], v243, s[48:51], s62 offen offset:2304 sc1
s_waitcnt lgkmcnt(6)
v_mfma_f32_16x16x32_bf16 v[144:147], v[212:215], v[80:83], v[144:147]
v_mfma_f32_16x16x32_bf16 v[148:151], v[212:215], v[84:87], v[148:151]
ds_read_b64_tr_b16 v[196:197], v239 offset:0
v_mfma_f32_16x16x32_bf16 v[152:155], v[212:215], v[88:91], v[152:155]
ds_read_b64_tr_b16 v[198:199], v239 offset:1024
v_mfma_f32_16x16x32_bf16 v[156:159], v[212:215], v[92:95], v[156:159]
s_add_u32 s62, s13, 151552
buffer_load_dwordx4 v[24:27], v243, s[48:51], s62 offen offset:2048 sc1
buffer_load_dwordx4 v[28:31], v243, s[48:51], s62 offen offset:2304 sc1
s_waitcnt lgkmcnt(6)
v_mfma_f32_16x16x32_bf16 v[160:163], v[216:219], v[80:83], v[160:163]
v_mfma_f32_16x16x32_bf16 v[164:167], v[216:219], v[84:87], v[164:167]
ds_read_b64_tr_b16 v[200:201], v232 offset:0
v_mfma_f32_16x16x32_bf16 v[168:171], v[216:219], v[88:91], v[168:171]
ds_read_b64_tr_b16 v[202:203], v232 offset:1024
v_mfma_f32_16x16x32_bf16 v[172:175], v[216:219], v[92:95], v[172:175]
s_add_u32 s62, s13, 12288
buffer_load_dwordx4 v[32:35], v243, s[48:51], s62 offen offset:0 sc1
buffer_load_dwordx4 v[36:39], v243, s[48:51], s62 offen offset:256 sc1
s_waitcnt lgkmcnt(6)
v_mfma_f32_16x16x32_bf16 v[176:179], v[220:223], v[80:83], v[176:179]
v_mfma_f32_16x16x32_bf16 v[180:183], v[220:223], v[84:87], v[180:183]
ds_read_b64_tr_b16 v[204:205], v233 offset:0
v_mfma_f32_16x16x32_bf16 v[184:187], v[220:223], v[88:91], v[184:187]
ds_read_b64_tr_b16 v[206:207], v233 offset:1024
v_mfma_f32_16x16x32_bf16 v[188:191], v[220:223], v[92:95], v[188:191]
s_add_u32 m0, s96, 12288
s_add_u32 s62, s11, 360448
buffer_load_dwordx4 v242, s[36:39], s62 offen  lds
s_add_u32 m0, s96, 13312
s_add_u32 s62, s11, 368640
buffer_load_dwordx4 v242, s[36:39], s62 offen  lds
s_add_u32 s62, s13, 159744
buffer_load_dwordx4 v[40:43], v243, s[48:51], s62 offen offset:0 sc1
buffer_load_dwordx4 v[44:47], v243, s[48:51], s62 offen offset:256 sc1
buffer_load_dwordx4 v[228:231], v253, s[20:23], s59 offen
s_waitcnt lgkmcnt(6)
v_mfma_f32_16x16x32_bf16 v[128:131], v[192:195], v[96:99], v[128:131]
v_mfma_f32_16x16x32_bf16 v[132:135], v[192:195], v[100:103], v[132:135]
ds_read_b64_tr_b16 v[208:209], v238 offset:4096
v_mfma_f32_16x16x32_bf16 v[136:139], v[192:195], v[104:107], v[136:139]
ds_read_b64_tr_b16 v[210:211], v238 offset:5120
v_mfma_f32_16x16x32_bf16 v[140:143], v[192:195], v[108:111], v[140:143]
s_add_u32 m0, s96, 14336
s_add_u32 s62, s11, 376832
buffer_load_dwordx4 v242, s[36:39], s62 offen  lds
s_add_u32 m0, s96, 15360
s_add_u32 s62, s11, 385024
buffer_load_dwordx4 v242, s[36:39], s62 offen  lds
s_add_u32 s62, s13, 12288
buffer_load_dwordx4 v[48:51], v243, s[48:51], s62 offen offset:2048 sc1
buffer_load_dwordx4 v[52:55], v243, s[48:51], s62 offen offset:2304 sc1
s_waitcnt lgkmcnt(6)
v_mfma_f32_16x16x32_bf16 v[144:147], v[196:199], v[96:99], v[144:147]
v_mfma_f32_16x16x32_bf16 v[148:151], v[196:199], v[100:103], v[148:151]
ds_read_b64_tr_b16 v[212:213], v239 offset:4096
v_mfma_f32_16x16x32_bf16 v[152:155], v[196:199], v[104:107], v[152:155]
ds_read_b64_tr_b16 v[214:215], v239 offset:5120
v_mfma_f32_16x16x32_bf16 v[156:159], v[196:199], v[108:111], v[156:159]
s_add_u32 s62, s13, 159744
buffer_load_dwordx4 v[56:59], v243, s[48:51], s62 offen offset:2048 sc1
buffer_load_dwordx4 v[60:63], v243, s[48:51], s62 offen offset:2304 sc1
s_waitcnt lgkmcnt(6)
v_mfma_f32_16x16x32_bf16 v[160:163], v[200:203], v[96:99], v[160:163]
v_mfma_f32_16x16x32_bf16 v[164:167], v[200:203], v[100:103], v[164:167]
ds_read_b64_tr_b16 v[216:217], v232 offset:4096
v_mfma_f32_16x16x32_bf16 v[168:171], v[200:203], v[104:107], v[168:171]
ds_read_b64_tr_b16 v[218:219], v232 offset:5120
v_mfma_f32_16x16x32_bf16 v[172:175], v[200:203], v[108:111], v[172:175]
s_add_u32 s62, s13, 20480
buffer_load_dwordx4 v[64:67], v243, s[48:51], s62 offen offset:0 sc1
buffer_load_dwordx4 v[68:71], v243, s[48:51], s62 offen offset:256 sc1
s_waitcnt lgkmcnt(6)
v_mfma_f32_16x16x32_bf16 v[176:179], v[204:207], v[96:99], v[176:179]
v_mfma_f32_16x16x32_bf16 v[180:183], v[204:207], v[100:103], v[180:183]
ds_read_b64_tr_b16 v[220:221], v233 offset:4096
v_mfma_f32_16x16x32_bf16 v[184:187], v[204:207], v[104:107], v[184:187]
ds_read_b64_tr_b16 v[222:223], v233 offset:5120
v_mfma_f32_16x16x32_bf16 v[188:191], v[204:207], v[108:111], v[188:191]
s_add_u32 m0, s96, 65536
s_add_u32 s62, s12, 0
buffer_load_dwordx4 v242, s[40:43], s62 offen  lds
s_add_u32 m0, s96, 66560
s_add_u32 s62, s12, 8192
buffer_load_dwordx4 v242, s[40:43], s62 offen  lds
s_add_u32 s62, s13, 167936
buffer_load_dwordx4 v[72:75], v243, s[48:51], s62 offen offset:0 sc1
buffer_load_dwordx4 v[76:79], v243, s[48:51], s62 offen offset:256 sc1
s_waitcnt lgkmcnt(6)
v_mfma_f32_16x16x32_bf16 v[128:131], v[208:211], v[112:115], v[128:131]
v_mfma_f32_16x16x32_bf16 v[132:135], v[208:211], v[116:119], v[132:135]
ds_read_b64_tr_b16 v[192:193], v238 offset:8192
v_mfma_f32_16x16x32_bf16 v[136:139], v[208:211], v[120:123], v[136:139]
ds_read_b64_tr_b16 v[194:195], v238 offset:9216
v_mfma_f32_16x16x32_bf16 v[140:143], v[208:211], v[124:127], v[140:143]
s_add_u32 m0, s96, 67584
s_add_u32 s62, s12, 16384
buffer_load_dwordx4 v242, s[40:43], s62 offen  lds
s_add_u32 m0, s96, 68608
s_add_u32 s62, s12, 24576
buffer_load_dwordx4 v242, s[40:43], s62 offen  lds
s_add_u32 s62, s13, 20480
buffer_load_dwordx4 v[80:83], v243, s[48:51], s62 offen offset:2048 sc1
buffer_load_dwordx4 v[84:87], v243, s[48:51], s62 offen offset:2304 sc1
s_waitcnt lgkmcnt(6)
v_mfma_f32_16x16x32_bf16 v[144:147], v[212:215], v[112:115], v[144:147]
v_mfma_f32_16x16x32_bf16 v[148:151], v[212:215], v[116:119], v[148:151]
ds_read_b64_tr_b16 v[196:197], v239 offset:8192
v_mfma_f32_16x16x32_bf16 v[152:155], v[212:215], v[120:123], v[152:155]
ds_read_b64_tr_b16 v[198:199], v239 offset:9216
v_mfma_f32_16x16x32_bf16 v[156:159], v[212:215], v[124:127], v[156:159]
s_add_u32 s62, s13, 167936
buffer_load_dwordx4 v[88:91], v243, s[48:51], s62 offen offset:2048 sc1
buffer_load_dwordx4 v[92:95], v243, s[48:51], s62 offen offset:2304 sc1
s_waitcnt lgkmcnt(6)
)ASM" R"ASM(
v_mfma_f32_16x16x32_bf16 v[160:163], v[216:219], v[112:115], v[160:163]
v_mfma_f32_16x16x32_bf16 v[164:167], v[216:219], v[116:119], v[164:167]
ds_read_b64_tr_b16 v[200:201], v232 offset:8192
v_mfma_f32_16x16x32_bf16 v[168:171], v[216:219], v[120:123], v[168:171]
ds_read_b64_tr_b16 v[202:203], v232 offset:9216
v_mfma_f32_16x16x32_bf16 v[172:175], v[216:219], v[124:127], v[172:175]
s_waitcnt lgkmcnt(6)
v_mfma_f32_16x16x32_bf16 v[176:179], v[220:223], v[112:115], v[176:179]
v_mfma_f32_16x16x32_bf16 v[180:183], v[220:223], v[116:119], v[180:183]
ds_read_b64_tr_b16 v[204:205], v233 offset:8192
v_mfma_f32_16x16x32_bf16 v[184:187], v[220:223], v[120:123], v[184:187]
ds_read_b64_tr_b16 v[206:207], v233 offset:9216
v_mfma_f32_16x16x32_bf16 v[188:191], v[220:223], v[124:127], v[188:191]
s_add_u32 m0, s96, 69632
s_add_u32 s62, s12, 32768
buffer_load_dwordx4 v242, s[40:43], s62 offen  lds
s_add_u32 m0, s96, 70656
s_add_u32 s62, s12, 40960
buffer_load_dwordx4 v242, s[40:43], s62 offen  lds
s_waitcnt vmcnt(33)
s_waitcnt lgkmcnt(6)
v_mfma_f32_16x16x32_bf16 v[128:131], v[192:195], v[0:3], v[128:131]
v_mfma_f32_16x16x32_bf16 v[132:135], v[192:195], v[4:7], v[132:135]
ds_read_b64_tr_b16 v[208:209], v238 offset:12288
v_mfma_f32_16x16x32_bf16 v[136:139], v[192:195], v[8:11], v[136:139]
ds_read_b64_tr_b16 v[210:211], v238 offset:13312
v_mfma_f32_16x16x32_bf16 v[140:143], v[192:195], v[12:15], v[140:143]
s_add_u32 m0, s96, 71680
s_add_u32 s62, s12, 49152
buffer_load_dwordx4 v242, s[40:43], s62 offen  lds
s_add_u32 m0, s96, 72704
s_add_u32 s62, s12, 57344
buffer_load_dwordx4 v242, s[40:43], s62 offen  lds
s_waitcnt lgkmcnt(6)
v_mfma_f32_16x16x32_bf16 v[144:147], v[196:199], v[0:3], v[144:147]
v_mfma_f32_16x16x32_bf16 v[148:151], v[196:199], v[4:7], v[148:151]
ds_read_b64_tr_b16 v[212:213], v239 offset:12288
v_mfma_f32_16x16x32_bf16 v[152:155], v[196:199], v[8:11], v[152:155]
ds_read_b64_tr_b16 v[214:215], v239 offset:13312
v_mfma_f32_16x16x32_bf16 v[156:159], v[196:199], v[12:15], v[156:159]
s_waitcnt lgkmcnt(6)
v_mfma_f32_16x16x32_bf16 v[160:163], v[200:203], v[0:3], v[160:163]
v_mfma_f32_16x16x32_bf16 v[164:167], v[200:203], v[4:7], v[164:167]
ds_read_b64_tr_b16 v[216:217], v232 offset:12288
v_mfma_f32_16x16x32_bf16 v[168:171], v[200:203], v[8:11], v[168:171]
ds_read_b64_tr_b16 v[218:219], v232 offset:13312
v_mfma_f32_16x16x32_bf16 v[172:175], v[200:203], v[12:15], v[172:175]
s_waitcnt lgkmcnt(6)
v_mfma_f32_16x16x32_bf16 v[176:179], v[204:207], v[0:3], v[176:179]
v_mfma_f32_16x16x32_bf16 v[180:183], v[204:207], v[4:7], v[180:183]
ds_read_b64_tr_b16 v[220:221], v233 offset:12288
v_mfma_f32_16x16x32_bf16 v[184:187], v[204:207], v[8:11], v[184:187]
ds_read_b64_tr_b16 v[222:223], v233 offset:13312
v_mfma_f32_16x16x32_bf16 v[188:191], v[204:207], v[12:15], v[188:191]
s_add_u32 m0, s96, 73728
s_add_u32 s62, s12, 65536
buffer_load_dwordx4 v242, s[40:43], s62 offen  lds
s_add_u32 m0, s96, 74752
s_add_u32 s62, s12, 73728
buffer_load_dwordx4 v242, s[40:43], s62 offen  lds
s_setprio 3
s_setprio 3
s_lshl_b32 s62, s53, 8
v_add_u32 v246, s62, v245
ds_read_b32 v227, v246 offset:30720
s_waitcnt lgkmcnt(0)
s_setprio 3
v_cmp_gt_u32 vcc, 1, v227
s_setprio 0
s_nop 0
s_cbranch_vccnz .Lk_ls_41
.Lk_ld_42:
s_waitcnt vmcnt(31)
v_mfma_f32_16x16x32_bf16 v[128:131], v[208:211], v[16:19], v[128:131]
v_mfma_f32_16x16x32_bf16 v[132:135], v[208:211], v[20:23], v[132:135]
ds_read_b64_tr_b16 v[192:193], v234 offset:0
v_mfma_f32_16x16x32_bf16 v[136:139], v[208:211], v[24:27], v[136:139]
ds_read_b64_tr_b16 v[194:195], v234 offset:1024
v_mfma_f32_16x16x32_bf16 v[140:143], v[208:211], v[28:31], v[140:143]
s_add_u32 m0, s96, 75776
s_add_u32 s62, s12, 81920
buffer_load_dwordx4 v242, s[40:43], s62 offen  lds
s_add_u32 m0, s96, 76800
s_add_u32 s62, s12, 90112
buffer_load_dwordx4 v242, s[40:43], s62 offen  lds
v_mfma_f32_16x16x32_bf16 v[144:147], v[212:215], v[16:19], v[144:147]
v_mfma_f32_16x16x32_bf16 v[148:151], v[212:215], v[20:23], v[148:151]
ds_read_b64_tr_b16 v[196:197], v235 offset:0
v_mfma_f32_16x16x32_bf16 v[152:155], v[212:215], v[24:27], v[152:155]
ds_read_b64_tr_b16 v[198:199], v235 offset:1024
v_mfma_f32_16x16x32_bf16 v[156:159], v[212:215], v[28:31], v[156:159]
v_mfma_f32_16x16x32_bf16 v[160:163], v[216:219], v[16:19], v[160:163]
v_mfma_f32_16x16x32_bf16 v[164:167], v[216:219], v[20:23], v[164:167]
ds_read_b64_tr_b16 v[200:201], v236 offset:0
v_mfma_f32_16x16x32_bf16 v[168:171], v[216:219], v[24:27], v[168:171]
ds_read_b64_tr_b16 v[202:203], v236 offset:1024
v_mfma_f32_16x16x32_bf16 v[172:175], v[216:219], v[28:31], v[172:175]
v_mfma_f32_16x16x32_bf16 v[176:179], v[220:223], v[16:19], v[176:179]
v_mfma_f32_16x16x32_bf16 v[180:183], v[220:223], v[20:23], v[180:183]
ds_read_b64_tr_b16 v[204:205], v237 offset:0
v_mfma_f32_16x16x32_bf16 v[184:187], v[220:223], v[24:27], v[184:187]
ds_read_b64_tr_b16 v[206:207], v237 offset:1024
v_mfma_f32_16x16x32_bf16 v[188:191], v[220:223], v[28:31], v[188:191]
s_add_u32 m0, s96, 77824
s_add_u32 s62, s12, 98304
buffer_load_dwordx4 v242, s[40:43], s62 offen  lds
s_add_u32 m0, s96, 78848
s_add_u32 s62, s12, 106496
buffer_load_dwordx4 v242, s[40:43], s62 offen  lds
s_waitcnt vmcnt(29)
s_waitcnt lgkmcnt(6)
v_mfma_f32_16x16x32_bf16 v[128:131], v[192:195], v[32:35], v[128:131]
v_mfma_f32_16x16x32_bf16 v[132:135], v[192:195], v[36:39], v[132:135]
ds_read_b64_tr_b16 v[208:209], v234 offset:4096
v_mfma_f32_16x16x32_bf16 v[136:139], v[192:195], v[40:43], v[136:139]
ds_read_b64_tr_b16 v[210:211], v234 offset:5120
v_mfma_f32_16x16x32_bf16 v[140:143], v[192:195], v[44:47], v[140:143]
s_add_u32 m0, s96, 79872
s_add_u32 s62, s12, 114688
buffer_load_dwordx4 v242, s[40:43], s62 offen  lds
s_add_u32 m0, s96, 80896
s_add_u32 s62, s12, 122880
buffer_load_dwordx4 v242, s[40:43], s62 offen  lds
s_waitcnt lgkmcnt(6)
v_mfma_f32_16x16x32_bf16 v[144:147], v[196:199], v[32:35], v[144:147]
v_mfma_f32_16x16x32_bf16 v[148:151], v[196:199], v[36:39], v[148:151]
ds_read_b64_tr_b16 v[212:213], v235 offset:4096
v_mfma_f32_16x16x32_bf16 v[152:155], v[196:199], v[40:43], v[152:155]
ds_read_b64_tr_b16 v[214:215], v235 offset:5120
v_mfma_f32_16x16x32_bf16 v[156:159], v[196:199], v[44:47], v[156:159]
s_waitcnt lgkmcnt(6)
v_mfma_f32_16x16x32_bf16 v[160:163], v[200:203], v[32:35], v[160:163]
v_mfma_f32_16x16x32_bf16 v[164:167], v[200:203], v[36:39], v[164:167]
ds_read_b64_tr_b16 v[216:217], v236 offset:4096
v_mfma_f32_16x16x32_bf16 v[168:171], v[200:203], v[40:43], v[168:171]
ds_read_b64_tr_b16 v[218:219], v236 offset:5120
v_mfma_f32_16x16x32_bf16 v[172:175], v[200:203], v[44:47], v[172:175]
s_waitcnt lgkmcnt(6)
v_mfma_f32_16x16x32_bf16 v[176:179], v[204:207], v[32:35], v[176:179]
v_mfma_f32_16x16x32_bf16 v[180:183], v[204:207], v[36:39], v[180:183]
ds_read_b64_tr_b16 v[220:221], v237 offset:4096
v_mfma_f32_16x16x32_bf16 v[184:187], v[204:207], v[40:43], v[184:187]
ds_read_b64_tr_b16 v[222:223], v237 offset:5120
v_mfma_f32_16x16x32_bf16 v[188:191], v[204:207], v[44:47], v[188:191]
v_mov_b32 v250, v248
v_mov_b32 v251, v249
s_mov_b64 exec, 255
v_mov_b32 v250, 0
v_mov_b32 v251, 0
buffer_atomic_or_x2 v[250:251], v226, s[48:51], 0 offen offset:0 sc0 sc1
s_mov_b64 exec, -1
s_waitcnt vmcnt(24)
s_waitcnt lgkmcnt(6)
v_mfma_f32_16x16x32_bf16 v[128:131], v[208:211], v[48:51], v[128:131]
v_mfma_f32_16x16x32_bf16 v[132:135], v[208:211], v[52:55], v[132:135]
ds_read_b64_tr_b16 v[192:193], v234 offset:8192
v_mfma_f32_16x16x32_bf16 v[136:139], v[208:211], v[56:59], v[136:139]
ds_read_b64_tr_b16 v[194:195], v234 offset:9216
v_mfma_f32_16x16x32_bf16 v[140:143], v[208:211], v[60:63], v[140:143]
s_waitcnt lgkmcnt(6)
v_mfma_f32_16x16x32_bf16 v[144:147], v[212:215], v[48:51], v[144:147]
v_mfma_f32_16x16x32_bf16 v[148:151], v[212:215], v[52:55], v[148:151]
ds_read_b64_tr_b16 v[196:197], v235 offset:8192
v_mfma_f32_16x16x32_bf16 v[152:155], v[212:215], v[56:59], v[152:155]
ds_read_b64_tr_b16 v[198:199], v235 offset:9216
v_mfma_f32_16x16x32_bf16 v[156:159], v[212:215], v[60:63], v[156:159]
s_waitcnt lgkmcnt(6)
v_mfma_f32_16x16x32_bf16 v[160:163], v[216:219], v[48:51], v[160:163]
v_mfma_f32_16x16x32_bf16 v[164:167], v[216:219], v[52:55], v[164:167]
ds_read_b64_tr_b16 v[200:201], v236 offset:8192
v_mfma_f32_16x16x32_bf16 v[168:171], v[216:219], v[56:59], v[168:171]
ds_read_b64_tr_b16 v[202:203], v236 offset:9216
v_mfma_f32_16x16x32_bf16 v[172:175], v[216:219], v[60:63], v[172:175]
s_waitcnt lgkmcnt(6)
v_mfma_f32_16x16x32_bf16 v[176:179], v[220:223], v[48:51], v[176:179]
v_mfma_f32_16x16x32_bf16 v[180:183], v[220:223], v[52:55], v[180:183]
ds_read_b64_tr_b16 v[204:205], v237 offset:8192
v_mfma_f32_16x16x32_bf16 v[184:187], v[220:223], v[56:59], v[184:187]
ds_read_b64_tr_b16 v[206:207], v237 offset:9216
v_mfma_f32_16x16x32_bf16 v[188:191], v[220:223], v[60:63], v[188:191]
s_waitcnt vmcnt(18)
s_waitcnt lgkmcnt(6)
v_mfma_f32_16x16x32_bf16 v[128:131], v[192:195], v[64:67], v[128:131]
v_mfma_f32_16x16x32_bf16 v[132:135], v[192:195], v[68:71], v[132:135]
ds_read_b64_tr_b16 v[208:209], v234 offset:12288
v_mfma_f32_16x16x32_bf16 v[136:139], v[192:195], v[72:75], v[136:139]
ds_read_b64_tr_b16 v[210:211], v234 offset:13312
v_mfma_f32_16x16x32_bf16 v[140:143], v[192:195], v[76:79], v[140:143]
s_waitcnt lgkmcnt(6)
v_mfma_f32_16x16x32_bf16 v[144:147], v[196:199], v[64:67], v[144:147]
v_mfma_f32_16x16x32_bf16 v[148:151], v[196:199], v[68:71], v[148:151]
ds_read_b64_tr_b16 v[212:213], v235 offset:12288
v_mfma_f32_16x16x32_bf16 v[152:155], v[196:199], v[72:75], v[152:155]
ds_read_b64_tr_b16 v[214:215], v235 offset:13312
v_mfma_f32_16x16x32_bf16 v[156:159], v[196:199], v[76:79], v[156:159]
s_waitcnt lgkmcnt(6)
v_mfma_f32_16x16x32_bf16 v[160:163], v[200:203], v[64:67], v[160:163]
v_mfma_f32_16x16x32_bf16 v[164:167], v[200:203], v[68:71], v[164:167]
ds_read_b64_tr_b16 v[216:217], v236 offset:12288
v_mfma_f32_16x16x32_bf16 v[168:171], v[200:203], v[72:75], v[168:171]
)ASM" R"ASM(
ds_read_b64_tr_b16 v[218:219], v236 offset:13312
v_mfma_f32_16x16x32_bf16 v[172:175], v[200:203], v[76:79], v[172:175]
s_waitcnt lgkmcnt(6)
v_mfma_f32_16x16x32_bf16 v[176:179], v[204:207], v[64:67], v[176:179]
v_mfma_f32_16x16x32_bf16 v[180:183], v[204:207], v[68:71], v[180:183]
ds_read_b64_tr_b16 v[220:221], v237 offset:12288
v_mfma_f32_16x16x32_bf16 v[184:187], v[204:207], v[72:75], v[184:187]
ds_read_b64_tr_b16 v[222:223], v237 offset:13312
v_mfma_f32_16x16x32_bf16 v[188:191], v[204:207], v[76:79], v[188:191]
s_waitcnt vmcnt(12)
s_waitcnt lgkmcnt(6)
v_mfma_f32_16x16x32_bf16 v[128:131], v[208:211], v[80:83], v[128:131]
v_mfma_f32_16x16x32_bf16 v[132:135], v[208:211], v[84:87], v[132:135]
v_mfma_f32_16x16x32_bf16 v[136:139], v[208:211], v[88:91], v[136:139]
v_mfma_f32_16x16x32_bf16 v[140:143], v[208:211], v[92:95], v[140:143]
s_waitcnt vmcnt(8)
ds_read_b64_tr_b16 v[192:193], v238 offset:0
ds_read_b64_tr_b16 v[194:195], v238 offset:1024
s_waitcnt lgkmcnt(6)
v_mfma_f32_16x16x32_bf16 v[144:147], v[212:215], v[80:83], v[144:147]
v_mfma_f32_16x16x32_bf16 v[148:151], v[212:215], v[84:87], v[148:151]
ds_read_b64_tr_b16 v[196:197], v239 offset:0
v_mfma_f32_16x16x32_bf16 v[152:155], v[212:215], v[88:91], v[152:155]
ds_read_b64_tr_b16 v[198:199], v239 offset:1024
v_mfma_f32_16x16x32_bf16 v[156:159], v[212:215], v[92:95], v[156:159]
s_waitcnt lgkmcnt(6)
v_mfma_f32_16x16x32_bf16 v[160:163], v[216:219], v[80:83], v[160:163]
v_mfma_f32_16x16x32_bf16 v[164:167], v[216:219], v[84:87], v[164:167]
ds_read_b64_tr_b16 v[200:201], v232 offset:0
v_mfma_f32_16x16x32_bf16 v[168:171], v[216:219], v[88:91], v[168:171]
ds_read_b64_tr_b16 v[202:203], v232 offset:1024
v_mfma_f32_16x16x32_bf16 v[172:175], v[216:219], v[92:95], v[172:175]
s_waitcnt lgkmcnt(6)
v_mfma_f32_16x16x32_bf16 v[176:179], v[220:223], v[80:83], v[176:179]
v_mfma_f32_16x16x32_bf16 v[180:183], v[220:223], v[84:87], v[180:183]
ds_read_b64_tr_b16 v[204:205], v233 offset:0
v_mfma_f32_16x16x32_bf16 v[184:187], v[220:223], v[88:91], v[184:187]
ds_read_b64_tr_b16 v[206:207], v233 offset:1024
v_mfma_f32_16x16x32_bf16 v[188:191], v[220:223], v[92:95], v[188:191]
s_waitcnt vmcnt(0)
s_setprio 3
v_cmp_ne_u64 vcc, v[248:249], v[250:251]
s_setprio 0
s_nop 0
s_cbranch_vccnz .Lk_ps_43
.Lk_pd_44:
s_add_u32 s62, s14, 0
buffer_load_dwordx4 v[96:99], v243, s[48:51], s62 offen offset:0 sc1
buffer_load_dwordx4 v[100:103], v243, s[48:51], s62 offen offset:256 sc1
s_add_u32 s62, s14, 147456
buffer_load_dwordx4 v[104:107], v243, s[48:51], s62 offen offset:0 sc1
buffer_load_dwordx4 v[108:111], v243, s[48:51], s62 offen offset:256 sc1
s_add_u32 s62, s14, 0
buffer_load_dwordx4 v[112:115], v243, s[48:51], s62 offen offset:2048 sc1
buffer_load_dwordx4 v[116:119], v243, s[48:51], s62 offen offset:2304 sc1
s_add_u32 s62, s14, 147456
buffer_load_dwordx4 v[120:123], v243, s[48:51], s62 offen offset:2048 sc1
buffer_load_dwordx4 v[124:127], v243, s[48:51], s62 offen offset:2304 sc1
s_add_u32 s62, s14, 4096
buffer_load_dwordx4 v[0:3], v243, s[48:51], s62 offen offset:0 sc1
buffer_load_dwordx4 v[4:7], v243, s[48:51], s62 offen offset:256 sc1
s_add_u32 s62, s14, 151552
buffer_load_dwordx4 v[8:11], v243, s[48:51], s62 offen offset:0 sc1
buffer_load_dwordx4 v[12:15], v243, s[48:51], s62 offen offset:256 sc1
s_add_u32 s62, s14, 4096
buffer_load_dwordx4 v[16:19], v243, s[48:51], s62 offen offset:2048 sc1
buffer_load_dwordx4 v[20:23], v243, s[48:51], s62 offen offset:2304 sc1
s_add_u32 s62, s14, 151552
buffer_load_dwordx4 v[24:27], v243, s[48:51], s62 offen offset:2048 sc1
buffer_load_dwordx4 v[28:31], v243, s[48:51], s62 offen offset:2304 sc1
v_and_b32 v240, 15, v224
v_lshrrev_b32 v241, 4, v224
v_xor_b32 v241, v241, v224
v_and_b32 v241, 7, v241
v_lshlrev_b32 v241, 4, v241
v_lshl_add_u32 v240, v240, 8, v241
v_add_u32 v240, s96, v240
v_xor_b32 v241, 64, v240
s_waitcnt vmcnt(12)
s_waitcnt lgkmcnt(6)
v_mfma_f32_16x16x32_bf16 v[128:131], v[192:195], v[96:99], v[128:131]
v_mfma_f32_16x16x32_bf16 v[132:135], v[192:195], v[100:103], v[132:135]
ds_read_b64_tr_b16 v[208:209], v238 offset:4096
v_mfma_f32_16x16x32_bf16 v[136:139], v[192:195], v[104:107], v[136:139]
ds_read_b64_tr_b16 v[210:211], v238 offset:5120
v_mfma_f32_16x16x32_bf16 v[140:143], v[192:195], v[108:111], v[140:143]
s_waitcnt lgkmcnt(6)
v_mfma_f32_16x16x32_bf16 v[144:147], v[196:199], v[96:99], v[144:147]
v_mfma_f32_16x16x32_bf16 v[148:151], v[196:199], v[100:103], v[148:151]
ds_read_b64_tr_b16 v[212:213], v239 offset:4096
v_mfma_f32_16x16x32_bf16 v[152:155], v[196:199], v[104:107], v[152:155]
ds_read_b64_tr_b16 v[214:215], v239 offset:5120
v_mfma_f32_16x16x32_bf16 v[156:159], v[196:199], v[108:111], v[156:159]
s_waitcnt lgkmcnt(6)
v_mfma_f32_16x16x32_bf16 v[160:163], v[200:203], v[96:99], v[160:163]
v_mfma_f32_16x16x32_bf16 v[164:167], v[200:203], v[100:103], v[164:167]
ds_read_b64_tr_b16 v[216:217], v232 offset:4096
v_mfma_f32_16x16x32_bf16 v[168:171], v[200:203], v[104:107], v[168:171]
ds_read_b64_tr_b16 v[218:219], v232 offset:5120
v_mfma_f32_16x16x32_bf16 v[172:175], v[200:203], v[108:111], v[172:175]
s_waitcnt lgkmcnt(6)
v_mfma_f32_16x16x32_bf16 v[176:179], v[204:207], v[96:99], v[176:179]
v_mfma_f32_16x16x32_bf16 v[180:183], v[204:207], v[100:103], v[180:183]
ds_read_b64_tr_b16 v[220:221], v233 offset:4096
v_mfma_f32_16x16x32_bf16 v[184:187], v[204:207], v[104:107], v[184:187]
ds_read_b64_tr_b16 v[222:223], v233 offset:5120
v_mfma_f32_16x16x32_bf16 v[188:191], v[204:207], v[108:111], v[188:191]
s_waitcnt vmcnt(8)
s_waitcnt lgkmcnt(6)
v_mfma_f32_16x16x32_bf16 v[128:131], v[208:211], v[112:115], v[128:131]
v_mfma_f32_16x16x32_bf16 v[132:135], v[208:211], v[116:119], v[132:135]
ds_read_b64_tr_b16 v[192:193], v238 offset:8192
v_mfma_f32_16x16x32_bf16 v[136:139], v[208:211], v[120:123], v[136:139]
ds_read_b64_tr_b16 v[194:195], v238 offset:9216
v_mfma_f32_16x16x32_bf16 v[140:143], v[208:211], v[124:127], v[140:143]
s_waitcnt lgkmcnt(6)
v_mfma_f32_16x16x32_bf16 v[144:147], v[212:215], v[112:115], v[144:147]
v_mfma_f32_16x16x32_bf16 v[148:151], v[212:215], v[116:119], v[148:151]
ds_read_b64_tr_b16 v[196:197], v239 offset:8192
v_mfma_f32_16x16x32_bf16 v[152:155], v[212:215], v[120:123], v[152:155]
ds_read_b64_tr_b16 v[198:199], v239 offset:9216
v_mfma_f32_16x16x32_bf16 v[156:159], v[212:215], v[124:127], v[156:159]
s_waitcnt lgkmcnt(6)
v_mfma_f32_16x16x32_bf16 v[160:163], v[216:219], v[112:115], v[160:163]
v_mfma_f32_16x16x32_bf16 v[164:167], v[216:219], v[116:119], v[164:167]
ds_read_b64_tr_b16 v[200:201], v232 offset:8192
v_mfma_f32_16x16x32_bf16 v[168:171], v[216:219], v[120:123], v[168:171]
ds_read_b64_tr_b16 v[202:203], v232 offset:9216
v_mfma_f32_16x16x32_bf16 v[172:175], v[216:219], v[124:127], v[172:175]
s_waitcnt lgkmcnt(6)
v_mfma_f32_16x16x32_bf16 v[176:179], v[220:223], v[112:115], v[176:179]
v_mfma_f32_16x16x32_bf16 v[180:183], v[220:223], v[116:119], v[180:183]
ds_read_b64_tr_b16 v[204:205], v233 offset:8192
v_mfma_f32_16x16x32_bf16 v[184:187], v[220:223], v[120:123], v[184:187]
ds_read_b64_tr_b16 v[206:207], v233 offset:9216
v_mfma_f32_16x16x32_bf16 v[188:191], v[220:223], v[124:127], v[188:191]
ds_read_b64_tr_b16 v[208:209], v238 offset:12288
ds_read_b64_tr_b16 v[210:211], v238 offset:13312
ds_read_b64_tr_b16 v[212:213], v239 offset:12288
ds_read_b64_tr_b16 v[214:215], v239 offset:13312
ds_read_b64_tr_b16 v[216:217], v232 offset:12288
ds_read_b64_tr_b16 v[218:219], v232 offset:13312
ds_read_b64_tr_b16 v[220:221], v233 offset:12288
s_waitcnt lgkmcnt(13)
ds_read_b64_tr_b16 v[222:223], v233 offset:13312
s_waitcnt vmcnt(4)
v_mfma_f32_16x16x32_bf16 v[128:131], v[192:195], v[0:3], v[128:131]
v_mfma_f32_16x16x32_bf16 v[132:135], v[192:195], v[4:7], v[132:135]
v_mfma_f32_16x16x32_bf16 v[136:139], v[192:195], v[8:11], v[136:139]
v_mfma_f32_16x16x32_bf16 v[140:143], v[192:195], v[12:15], v[140:143]
s_waitcnt vmcnt(0)
s_waitcnt lgkmcnt(6)
v_mfma_f32_16x16x32_bf16 v[128:131], v[208:211], v[16:19], v[128:131]
v_mfma_f32_16x16x32_bf16 v[132:135], v[208:211], v[20:23], v[132:135]
v_mfma_f32_16x16x32_bf16 v[136:139], v[208:211], v[24:27], v[136:139]
v_mfma_f32_16x16x32_bf16 v[140:143], v[208:211], v[28:31], v[140:143]
v_mfma_f32_16x16x32_bf16 v[144:147], v[196:199], v[0:3], v[144:147]
v_mfma_f32_16x16x32_bf16 v[148:151], v[196:199], v[4:7], v[148:151]
v_mfma_f32_16x16x32_bf16 v[152:155], v[196:199], v[8:11], v[152:155]
v_mfma_f32_16x16x32_bf16 v[156:159], v[196:199], v[12:15], v[156:159]
s_waitcnt lgkmcnt(4)
v_mfma_f32_16x16x32_bf16 v[144:147], v[212:215], v[16:19], v[144:147]
s_nop 2
ds_write_b128 v240, v[128:131] offset:0
v_mfma_f32_16x16x32_bf16 v[148:151], v[212:215], v[20:23], v[148:151]
ds_write_b128 v240, v[132:135] offset:4096
v_mfma_f32_16x16x32_bf16 v[152:155], v[212:215], v[24:27], v[152:155]
ds_write_b128 v240, v[136:139] offset:8192
v_mfma_f32_16x16x32_bf16 v[156:159], v[212:215], v[28:31], v[156:159]
ds_write_b128 v240, v[140:143] offset:12288
v_mfma_f32_16x16x32_bf16 v[160:163], v[200:203], v[0:3], v[160:163]
v_mfma_f32_16x16x32_bf16 v[164:167], v[200:203], v[4:7], v[164:167]
v_mfma_f32_16x16x32_bf16 v[168:171], v[200:203], v[8:11], v[168:171]
v_mfma_f32_16x16x32_bf16 v[172:175], v[200:203], v[12:15], v[172:175]
s_waitcnt lgkmcnt(6)
v_mfma_f32_16x16x32_bf16 v[160:163], v[216:219], v[16:19], v[160:163]
ds_write_b128 v241, v[144:147] offset:0
v_mfma_f32_16x16x32_bf16 v[164:167], v[216:219], v[20:23], v[164:167]
ds_write_b128 v241, v[148:151] offset:4096
v_mfma_f32_16x16x32_bf16 v[168:171], v[216:219], v[24:27], v[168:171]
ds_write_b128 v241, v[152:155] offset:8192
v_mfma_f32_16x16x32_bf16 v[172:175], v[216:219], v[28:31], v[172:175]
ds_write_b128 v241, v[156:159] offset:12288
v_mfma_f32_16x16x32_bf16 v[176:179], v[204:207], v[0:3], v[176:179]
v_mfma_f32_16x16x32_bf16 v[180:183], v[204:207], v[4:7], v[180:183]
v_mfma_f32_16x16x32_bf16 v[184:187], v[204:207], v[8:11], v[184:187]
v_mfma_f32_16x16x32_bf16 v[188:191], v[204:207], v[12:15], v[188:191]
s_waitcnt lgkmcnt(8)
v_mfma_f32_16x16x32_bf16 v[176:179], v[220:223], v[16:19], v[176:179]
ds_write_b128 v240, v[160:163] offset:128
v_mfma_f32_16x16x32_bf16 v[180:183], v[220:223], v[20:23], v[180:183]
ds_write_b128 v240, v[164:167] offset:4224
v_mfma_f32_16x16x32_bf16 v[184:187], v[220:223], v[24:27], v[184:187]
ds_write_b128 v240, v[168:171] offset:8320
v_mfma_f32_16x16x32_bf16 v[188:191], v[220:223], v[28:31], v[188:191]
ds_write_b128 v240, v[172:175] offset:12416
s_nop 4
ds_write_b128 v241, v[176:179] offset:128
s_nop 0
ds_write_b128 v241, v[180:183] offset:4224
)ASM" R"ASM(
s_nop 0
ds_write_b128 v241, v[184:187] offset:8320
s_waitcnt lgkmcnt(13)
ds_write_b128 v241, v[188:191] offset:12416
s_branch .Lk_final
.Lk_light:
s_nop 1
ds_add_u32 v245, v246 offset:28672
s_waitcnt lgkmcnt(7)
v_mfma_f32_32x32x16_bf16 v[128:143], v[192:195], v[0:3], 0
ds_read_b64_tr_b16 v[208:209], v236 offset:10240
ds_read_b64_tr_b16 v[210:211], v236 offset:10752
s_waitcnt lgkmcnt(7)
v_mfma_f32_32x32x16_bf16 v[144:159], v[196:199], v[0:3], 0
ds_read_b64_tr_b16 v[212:213], v237 offset:10240
ds_read_b64_tr_b16 v[214:215], v237 offset:10752
s_waitcnt lgkmcnt(7)
v_mfma_f32_32x32x16_bf16 v[160:175], v[200:203], v[0:3], 0
ds_read_b64_tr_b16 v[216:217], v236 offset:14336
ds_read_b64_tr_b16 v[218:219], v236 offset:14848
s_waitcnt lgkmcnt(7)
v_mfma_f32_32x32x16_bf16 v[176:191], v[204:207], v[0:3], 0
ds_read_b64_tr_b16 v[220:221], v237 offset:14336
ds_read_b64_tr_b16 v[222:223], v237 offset:14848
s_waitcnt lgkmcnt(6)
v_mfma_f32_32x32x16_bf16 v[128:143], v[208:211], v[4:7], v[128:143]
ds_read_b64_tr_b16 v[192:193], v236 offset:24576
ds_read_b64_tr_b16 v[194:195], v236 offset:25088
s_waitcnt lgkmcnt(6)
v_mfma_f32_32x32x16_bf16 v[144:159], v[212:215], v[4:7], v[144:159]
ds_read_b64_tr_b16 v[196:197], v237 offset:24576
ds_read_b64_tr_b16 v[198:199], v237 offset:25088
s_waitcnt lgkmcnt(6)
v_mfma_f32_32x32x16_bf16 v[160:175], v[216:219], v[4:7], v[160:175]
ds_read_b64_tr_b16 v[200:201], v236 offset:28672
ds_read_b64_tr_b16 v[202:203], v236 offset:29184
s_waitcnt lgkmcnt(6)
v_mfma_f32_32x32x16_bf16 v[176:191], v[220:223], v[4:7], v[176:191]
ds_read_b64_tr_b16 v[204:205], v237 offset:28672
ds_read_b64_tr_b16 v[206:207], v237 offset:29184
ds_read_b32 v227, v245 offset:28672
s_waitcnt lgkmcnt(7)
v_mfma_f32_32x32x16_bf16 v[128:143], v[192:195], v[8:11], v[128:143]
ds_read_b64_tr_b16 v[208:209], v236 offset:26624
ds_read_b64_tr_b16 v[210:211], v236 offset:27136
s_waitcnt lgkmcnt(7)
v_mfma_f32_32x32x16_bf16 v[144:159], v[196:199], v[8:11], v[144:159]
s_waitcnt lgkmcnt(2)
s_setprio 3
v_cmp_gt_u32 vcc, 4, v227
s_setprio 0
s_nop 0
s_cbranch_vccnz .Lk_ls_45
.Lk_ld_46:
ds_read_b64_tr_b16 v[212:213], v237 offset:26624
ds_read_b64_tr_b16 v[214:215], v237 offset:27136
v_mfma_f32_32x32x16_bf16 v[160:175], v[200:203], v[8:11], v[160:175]
ds_read_b64_tr_b16 v[216:217], v236 offset:30720
ds_read_b64_tr_b16 v[218:219], v236 offset:31232
v_mfma_f32_32x32x16_bf16 v[176:191], v[204:207], v[8:11], v[176:191]
ds_read_b64_tr_b16 v[220:221], v237 offset:30720
ds_read_b64_tr_b16 v[222:223], v237 offset:31232
s_waitcnt lgkmcnt(6)
v_mfma_f32_32x32x16_bf16 v[128:143], v[208:211], v[12:15], v[128:143]
ds_read_b64_tr_b16 v[192:193], v236 offset:40960
ds_read_b64_tr_b16 v[194:195], v236 offset:41472
s_waitcnt lgkmcnt(6)
v_mfma_f32_32x32x16_bf16 v[144:159], v[212:215], v[12:15], v[144:159]
ds_read_b64_tr_b16 v[196:197], v237 offset:40960
ds_read_b64_tr_b16 v[198:199], v237 offset:41472
s_waitcnt lgkmcnt(6)
v_mfma_f32_32x32x16_bf16 v[160:175], v[216:219], v[12:15], v[160:175]
ds_read_b64_tr_b16 v[200:201], v236 offset:45056
ds_read_b64_tr_b16 v[202:203], v236 offset:45568
s_waitcnt lgkmcnt(6)
v_mfma_f32_32x32x16_bf16 v[176:191], v[220:223], v[12:15], v[176:191]
ds_read_b64_tr_b16 v[204:205], v237 offset:45056
ds_read_b64_tr_b16 v[206:207], v237 offset:45568
s_waitcnt lgkmcnt(6)
v_mfma_f32_32x32x16_bf16 v[128:143], v[192:195], v[16:19], v[128:143]
ds_read_b64_tr_b16 v[208:209], v236 offset:43008
ds_read_b64_tr_b16 v[210:211], v236 offset:43520
s_waitcnt lgkmcnt(6)
v_mfma_f32_32x32x16_bf16 v[144:159], v[196:199], v[16:19], v[144:159]
ds_read_b64_tr_b16 v[212:213], v237 offset:43008
ds_read_b64_tr_b16 v[214:215], v237 offset:43520
s_waitcnt lgkmcnt(6)
v_mfma_f32_32x32x16_bf16 v[160:175], v[200:203], v[16:19], v[160:175]
ds_read_b64_tr_b16 v[216:217], v236 offset:47104
ds_read_b64_tr_b16 v[218:219], v236 offset:47616
s_waitcnt lgkmcnt(6)
v_mfma_f32_32x32x16_bf16 v[176:191], v[204:207], v[16:19], v[176:191]
ds_read_b64_tr_b16 v[220:221], v237 offset:47104
ds_read_b64_tr_b16 v[222:223], v237 offset:47616
s_waitcnt lgkmcnt(6)
v_mfma_f32_32x32x16_bf16 v[128:143], v[208:211], v[20:23], v[128:143]
ds_read_b64_tr_b16 v[192:193], v236 offset:57344
ds_read_b64_tr_b16 v[194:195], v236 offset:57856
s_waitcnt lgkmcnt(6)
v_mfma_f32_32x32x16_bf16 v[144:159], v[212:215], v[20:23], v[144:159]
ds_read_b64_tr_b16 v[196:197], v237 offset:57344
ds_read_b64_tr_b16 v[198:199], v237 offset:57856
s_waitcnt lgkmcnt(6)
v_mfma_f32_32x32x16_bf16 v[160:175], v[216:219], v[20:23], v[160:175]
ds_read_b64_tr_b16 v[200:201], v236 offset:61440
ds_read_b64_tr_b16 v[202:203], v236 offset:61952
s_waitcnt lgkmcnt(6)
v_mfma_f32_32x32x16_bf16 v[176:191], v[220:223], v[20:23], v[176:191]
ds_read_b64_tr_b16 v[204:205], v237 offset:61440
ds_read_b64_tr_b16 v[206:207], v237 offset:61952
s_waitcnt lgkmcnt(6)
v_mfma_f32_32x32x16_bf16 v[128:143], v[192:195], v[24:27], v[128:143]
ds_read_b64_tr_b16 v[208:209], v236 offset:59392
ds_read_b64_tr_b16 v[210:211], v236 offset:59904
s_waitcnt lgkmcnt(6)
v_mfma_f32_32x32x16_bf16 v[144:159], v[196:199], v[24:27], v[144:159]
ds_read_b64_tr_b16 v[212:213], v237 offset:59392
ds_read_b64_tr_b16 v[214:215], v237 offset:59904
s_waitcnt lgkmcnt(6)
v_mfma_f32_32x32x16_bf16 v[160:175], v[200:203], v[24:27], v[160:175]
ds_read_b32 v227, v245 offset:29440
ds_read_b64_tr_b16 v[216:217], v236 offset:63488
ds_read_b64_tr_b16 v[218:219], v236 offset:64000
s_waitcnt lgkmcnt(7)
v_mfma_f32_32x32x16_bf16 v[176:191], v[204:207], v[24:27], v[176:191]
ds_read_b64_tr_b16 v[220:221], v237 offset:63488
ds_read_b64_tr_b16 v[222:223], v237 offset:64000
s_waitcnt lgkmcnt(7)
v_mfma_f32_32x32x16_bf16 v[128:143], v[208:211], v[28:31], v[128:143]
s_waitcnt lgkmcnt(4)
s_setprio 3
v_cmp_gt_u32 vcc, 4, v227
s_setprio 0
s_nop 0
s_cbranch_vccnz .Lk_ls_47
.Lk_ld_48:
ds_read_b64_tr_b16 v[192:193], v234 offset:0
ds_read_b64_tr_b16 v[194:195], v234 offset:512
v_mfma_f32_32x32x16_bf16 v[144:159], v[212:215], v[28:31], v[144:159]
ds_read_b64_tr_b16 v[196:197], v235 offset:0
ds_read_b64_tr_b16 v[198:199], v235 offset:512
s_waitcnt lgkmcnt(6)
v_mfma_f32_32x32x16_bf16 v[160:175], v[216:219], v[28:31], v[160:175]
ds_read_b64_tr_b16 v[200:201], v234 offset:4096
ds_read_b64_tr_b16 v[202:203], v234 offset:4608
s_waitcnt lgkmcnt(6)
v_mfma_f32_32x32x16_bf16 v[176:191], v[220:223], v[28:31], v[176:191]
ds_read_b64_tr_b16 v[204:205], v235 offset:4096
ds_read_b64_tr_b16 v[206:207], v235 offset:4608
s_nop 15
s_nop 1
s_waitcnt lgkmcnt(4)
v_mfma_f32_32x32x16_bf16 v[128:143], v[192:195], v[32:35], v[128:143]
ds_read_b64_tr_b16 v[208:209], v234 offset:2048
ds_read_b64_tr_b16 v[210:211], v234 offset:2560
v_mfma_f32_32x32x16_bf16 v[144:159], v[196:199], v[32:35], v[144:159]
ds_read_b64_tr_b16 v[212:213], v235 offset:2048
ds_read_b64_tr_b16 v[214:215], v235 offset:2560
s_add_u32 m0, s96, 8192
s_add_u32 s62, s94, 0
buffer_load_dwordx4 v240, s[28:31], s62 offen  lds
s_add_u32 m0, s96, 9216
s_add_u32 s62, s94, 28672
buffer_load_dwordx4 v240, s[28:31], s62 offen  lds
s_nop 11
s_waitcnt lgkmcnt(4)
v_mfma_f32_32x32x16_bf16 v[160:175], v[200:203], v[32:35], v[160:175]
ds_read_b64_tr_b16 v[216:217], v234 offset:6144
ds_read_b64_tr_b16 v[218:219], v234 offset:6656
v_mfma_f32_32x32x16_bf16 v[176:191], v[204:207], v[32:35], v[176:191]
ds_read_b64_tr_b16 v[220:221], v235 offset:6144
ds_read_b64_tr_b16 v[222:223], v235 offset:6656
s_add_u32 m0, s96, 10240
s_add_u32 s62, s94, 57344
buffer_load_dwordx4 v240, s[28:31], s62 offen  lds
s_add_u32 m0, s96, 11264
s_add_u32 s62, s94, 86016
buffer_load_dwordx4 v240, s[28:31], s62 offen  lds
s_nop 11
s_waitcnt lgkmcnt(4)
v_mfma_f32_32x32x16_bf16 v[128:143], v[208:211], v[36:39], v[128:143]
ds_read_b64_tr_b16 v[192:193], v234 offset:16384
ds_read_b64_tr_b16 v[194:195], v234 offset:16896
v_mfma_f32_32x32x16_bf16 v[144:159], v[212:215], v[36:39], v[144:159]
ds_read_b64_tr_b16 v[196:197], v235 offset:16384
ds_read_b64_tr_b16 v[198:199], v235 offset:16896
s_add_u32 m0, s96, 12288
s_add_u32 s62, s94, 0
buffer_load_dwordx4 v240, s[32:35], s62 offen  lds
s_add_u32 m0, s96, 13312
s_add_u32 s62, s94, 28672
buffer_load_dwordx4 v240, s[32:35], s62 offen  lds
s_nop 11
s_waitcnt lgkmcnt(4)
v_mfma_f32_32x32x16_bf16 v[160:175], v[216:219], v[36:39], v[160:175]
ds_read_b64_tr_b16 v[200:201], v234 offset:20480
ds_read_b64_tr_b16 v[202:203], v234 offset:20992
v_mfma_f32_32x32x16_bf16 v[176:191], v[220:223], v[36:39], v[176:191]
ds_read_b64_tr_b16 v[204:205], v235 offset:20480
ds_read_b64_tr_b16 v[206:207], v235 offset:20992
)ASM" R"ASM(
s_add_u32 m0, s96, 14336
s_add_u32 s62, s94, 57344
buffer_load_dwordx4 v240, s[32:35], s62 offen  lds
s_add_u32 m0, s96, 15360
s_add_u32 s62, s94, 86016
buffer_load_dwordx4 v240, s[32:35], s62 offen  lds
s_nop 11
s_waitcnt lgkmcnt(4)
v_mfma_f32_32x32x16_bf16 v[128:143], v[192:195], v[40:43], v[128:143]
ds_read_b64_tr_b16 v[208:209], v234 offset:18432
ds_read_b64_tr_b16 v[210:211], v234 offset:18944
v_mfma_f32_32x32x16_bf16 v[144:159], v[196:199], v[40:43], v[144:159]
ds_read_b64_tr_b16 v[212:213], v235 offset:18432
ds_read_b64_tr_b16 v[214:215], v235 offset:18944
s_add_u32 m0, s96, 65536
s_add_u32 s62, s95, 0
buffer_load_dwordx4 v240, s[28:31], s62 offen  lds
s_add_u32 m0, s96, 66560
s_add_u32 s62, s95, 28672
buffer_load_dwordx4 v240, s[28:31], s62 offen  lds
s_nop 11
s_waitcnt lgkmcnt(4)
v_mfma_f32_32x32x16_bf16 v[160:175], v[200:203], v[40:43], v[160:175]
ds_read_b64_tr_b16 v[216:217], v234 offset:22528
ds_read_b64_tr_b16 v[218:219], v234 offset:23040
v_mfma_f32_32x32x16_bf16 v[176:191], v[204:207], v[40:43], v[176:191]
ds_read_b64_tr_b16 v[220:221], v235 offset:22528
ds_read_b64_tr_b16 v[222:223], v235 offset:23040
s_add_u32 m0, s96, 67584
s_add_u32 s62, s95, 57344
buffer_load_dwordx4 v240, s[28:31], s62 offen  lds
s_add_u32 m0, s96, 68608
s_add_u32 s62, s95, 86016
buffer_load_dwordx4 v240, s[28:31], s62 offen  lds
s_nop 11
s_waitcnt lgkmcnt(4)
v_mfma_f32_32x32x16_bf16 v[128:143], v[208:211], v[44:47], v[128:143]
ds_read_b64_tr_b16 v[192:193], v234 offset:32768
ds_read_b64_tr_b16 v[194:195], v234 offset:33280
v_mfma_f32_32x32x16_bf16 v[144:159], v[212:215], v[44:47], v[144:159]
ds_read_b64_tr_b16 v[196:197], v235 offset:32768
ds_read_b64_tr_b16 v[198:199], v235 offset:33280
s_add_u32 m0, s96, 69632
s_add_u32 s62, s95, 0
buffer_load_dwordx4 v240, s[32:35], s62 offen  lds
s_add_u32 m0, s96, 70656
s_add_u32 s62, s95, 28672
buffer_load_dwordx4 v240, s[32:35], s62 offen  lds
s_nop 11
s_waitcnt lgkmcnt(4)
v_mfma_f32_32x32x16_bf16 v[160:175], v[216:219], v[44:47], v[160:175]
ds_read_b64_tr_b16 v[200:201], v234 offset:36864
ds_read_b64_tr_b16 v[202:203], v234 offset:37376
v_mfma_f32_32x32x16_bf16 v[176:191], v[220:223], v[44:47], v[176:191]
ds_read_b64_tr_b16 v[204:205], v235 offset:36864
ds_read_b64_tr_b16 v[206:207], v235 offset:37376
s_add_u32 m0, s96, 71680
s_add_u32 s62, s95, 57344
buffer_load_dwordx4 v240, s[32:35], s62 offen  lds
s_add_u32 m0, s96, 72704
s_add_u32 s62, s95, 86016
buffer_load_dwordx4 v240, s[32:35], s62 offen  lds
s_nop 11
s_waitcnt lgkmcnt(4)
v_mfma_f32_32x32x16_bf16 v[128:143], v[192:195], v[48:51], v[128:143]
ds_read_b64_tr_b16 v[208:209], v234 offset:34816
ds_read_b64_tr_b16 v[210:211], v234 offset:35328
v_mfma_f32_32x32x16_bf16 v[144:159], v[196:199], v[48:51], v[144:159]
ds_read_b64_tr_b16 v[212:213], v235 offset:34816
ds_read_b64_tr_b16 v[214:215], v235 offset:35328
s_nop 15
s_nop 1
s_waitcnt lgkmcnt(4)
v_mfma_f32_32x32x16_bf16 v[160:175], v[200:203], v[48:51], v[160:175]
ds_read_b64_tr_b16 v[216:217], v234 offset:38912
ds_read_b64_tr_b16 v[218:219], v234 offset:39424
v_mfma_f32_32x32x16_bf16 v[176:191], v[204:207], v[48:51], v[176:191]
ds_read_b64_tr_b16 v[220:221], v235 offset:38912
ds_read_b64_tr_b16 v[222:223], v235 offset:39424
s_nop 15
s_nop 1
s_waitcnt lgkmcnt(4)
v_mfma_f32_32x32x16_bf16 v[128:143], v[208:211], v[52:55], v[128:143]
ds_read_b64_tr_b16 v[192:193], v234 offset:49152
ds_read_b64_tr_b16 v[194:195], v234 offset:49664
v_mfma_f32_32x32x16_bf16 v[144:159], v[212:215], v[52:55], v[144:159]
ds_read_b64_tr_b16 v[196:197], v235 offset:49152
ds_read_b64_tr_b16 v[198:199], v235 offset:49664
s_waitcnt vmcnt(8)
ds_add_u32 v245, v246 offset:28672
s_nop 15
s_waitcnt lgkmcnt(5)
v_mfma_f32_32x32x16_bf16 v[160:175], v[216:219], v[52:55], v[160:175]
ds_read_b64_tr_b16 v[200:201], v234 offset:53248
ds_read_b64_tr_b16 v[202:203], v234 offset:53760
v_mfma_f32_32x32x16_bf16 v[176:191], v[220:223], v[52:55], v[176:191]
ds_read_b64_tr_b16 v[204:205], v235 offset:53248
ds_read_b64_tr_b16 v[206:207], v235 offset:53760
s_nop 15
s_nop 1
s_waitcnt lgkmcnt(5)
v_mfma_f32_32x32x16_bf16 v[128:143], v[192:195], v[56:59], v[128:143]
ds_read_b64_tr_b16 v[208:209], v234 offset:51200
ds_read_b64_tr_b16 v[210:211], v234 offset:51712
v_mfma_f32_32x32x16_bf16 v[144:159], v[196:199], v[56:59], v[144:159]
ds_read_b64_tr_b16 v[212:213], v235 offset:51200
ds_read_b64_tr_b16 v[214:215], v235 offset:51712
ds_read_b32 v227, v245 offset:28672
s_nop 15
s_nop 0
s_waitcnt lgkmcnt(5)
v_mfma_f32_32x32x16_bf16 v[160:175], v[200:203], v[56:59], v[160:175]
ds_read_b64_tr_b16 v[216:217], v234 offset:55296
ds_read_b64_tr_b16 v[218:219], v234 offset:55808
v_mfma_f32_32x32x16_bf16 v[176:191], v[204:207], v[56:59], v[176:191]
s_waitcnt lgkmcnt(2)
s_setprio 3
v_cmp_gt_u32 vcc, 8, v227
s_setprio 0
s_nop 0
s_cbranch_vccnz .Lk_ls_49
.Lk_ld_50:
ds_read_b64_tr_b16 v[220:221], v235 offset:55296
ds_read_b64_tr_b16 v[222:223], v235 offset:55808
s_nop 10
v_mfma_f32_32x32x16_bf16 v[128:143], v[208:211], v[60:63], v[128:143]
ds_read_b64_tr_b16 v[192:193], v234 offset:8192
ds_read_b64_tr_b16 v[194:195], v234 offset:8704
v_mfma_f32_32x32x16_bf16 v[144:159], v[212:215], v[60:63], v[144:159]
ds_read_b64_tr_b16 v[196:197], v235 offset:8192
ds_read_b64_tr_b16 v[198:199], v235 offset:8704
s_nop 15
s_nop 1
s_waitcnt lgkmcnt(4)
v_mfma_f32_32x32x16_bf16 v[160:175], v[216:219], v[60:63], v[160:175]
ds_read_b64_tr_b16 v[200:201], v234 offset:12288
ds_read_b64_tr_b16 v[202:203], v234 offset:12800
v_mfma_f32_32x32x16_bf16 v[176:191], v[220:223], v[60:63], v[176:191]
ds_read_b64_tr_b16 v[204:205], v235 offset:12288
ds_read_b64_tr_b16 v[206:207], v235 offset:12800
s_waitcnt vmcnt(0)
ds_add_u32 v245, v246 offset:28672
s_nop 15
s_waitcnt lgkmcnt(5)
v_mfma_f32_32x32x16_bf16 v[128:143], v[192:195], v[64:67], v[128:143]
ds_read_b64_tr_b16 v[208:209], v234 offset:10240
ds_read_b64_tr_b16 v[210:211], v234 offset:10752
v_mfma_f32_32x32x16_bf16 v[144:159], v[196:199], v[64:67], v[144:159]
ds_read_b64_tr_b16 v[212:213], v235 offset:10240
ds_read_b64_tr_b16 v[214:215], v235 offset:10752
s_add_u32 m0, s96, 73728
s_add_u32 s62, s88, s4
buffer_load_dwordx4 v241, s[28:31], s62 offen  lds
s_nop 14
s_waitcnt lgkmcnt(5)
v_mfma_f32_32x32x16_bf16 v[160:175], v[200:203], v[64:67], v[160:175]
ds_read_b64_tr_b16 v[216:217], v234 offset:14336
ds_read_b64_tr_b16 v[218:219], v234 offset:14848
v_mfma_f32_32x32x16_bf16 v[176:191], v[204:207], v[64:67], v[176:191]
ds_read_b64_tr_b16 v[220:221], v235 offset:14336
ds_read_b64_tr_b16 v[222:223], v235 offset:14848
s_add_u32 m0, s96, 77952
s_add_u32 s62, s88, s4
buffer_load_dwordx4 v241, s[32:35], s62 offen  lds
s_nop 14
s_waitcnt lgkmcnt(4)
v_mfma_f32_32x32x16_bf16 v[128:143], v[208:211], v[68:71], v[128:143]
ds_read_b64_tr_b16 v[192:193], v234 offset:24576
ds_read_b64_tr_b16 v[194:195], v234 offset:25088
v_mfma_f32_32x32x16_bf16 v[144:159], v[212:215], v[68:71], v[144:159]
ds_read_b64_tr_b16 v[196:197], v235 offset:24576
ds_read_b64_tr_b16 v[198:199], v235 offset:25088
s_add_u32 m0, s96, 74752
s_add_u32 s62, s88, s5
buffer_load_dwordx4 v241, s[28:31], s62 offen  lds
s_nop 14
s_waitcnt lgkmcnt(4)
v_mfma_f32_32x32x16_bf16 v[160:175], v[216:219], v[68:71], v[160:175]
ds_read_b64_tr_b16 v[200:201], v234 offset:28672
ds_read_b64_tr_b16 v[202:203], v234 offset:29184
v_mfma_f32_32x32x16_bf16 v[176:191], v[220:223], v[68:71], v[176:191]
ds_read_b64_tr_b16 v[204:205], v235 offset:28672
ds_read_b64_tr_b16 v[206:207], v235 offset:29184
ds_read_b32 v227, v245 offset:28672
s_add_u32 m0, s96, 78976
s_add_u32 s62, s88, s5
buffer_load_dwordx4 v241, s[32:35], s62 offen  lds
s_nop 13
s_waitcnt lgkmcnt(5)
v_mfma_f32_32x32x16_bf16 v[128:143], v[192:195], v[72:75], v[128:143]
ds_read_b64_tr_b16 v[208:209], v234 offset:26624
ds_read_b64_tr_b16 v[210:211], v234 offset:27136
v_mfma_f32_32x32x16_bf16 v[144:159], v[196:199], v[72:75], v[144:159]
s_waitcnt lgkmcnt(2)
s_setprio 3
v_cmp_gt_u32 vcc, 12, v227
s_setprio 0
s_nop 0
s_cbranch_vccnz .Lk_ls_51
.Lk_ld_52:
)ASM" R"ASM(
ds_read_b64_tr_b16 v[212:213], v235 offset:26624
ds_read_b64_tr_b16 v[214:215], v235 offset:27136
s_add_u32 m0, s96, 0
s_add_u32 s62, s89, s4
buffer_load_dwordx4 v241, s[28:31], s62 offen  lds
s_nop 7
v_mfma_f32_32x32x16_bf16 v[160:175], v[200:203], v[72:75], v[160:175]
ds_read_b64_tr_b16 v[216:217], v234 offset:30720
ds_read_b64_tr_b16 v[218:219], v234 offset:31232
v_mfma_f32_32x32x16_bf16 v[176:191], v[204:207], v[72:75], v[176:191]
ds_read_b64_tr_b16 v[220:221], v235 offset:30720
ds_read_b64_tr_b16 v[222:223], v235 offset:31232
s_add_u32 m0, s96, 4224
s_add_u32 s62, s89, s4
buffer_load_dwordx4 v241, s[32:35], s62 offen  lds
s_nop 14
s_waitcnt lgkmcnt(4)
v_mfma_f32_32x32x16_bf16 v[128:143], v[208:211], v[76:79], v[128:143]
ds_read_b64_tr_b16 v[192:193], v234 offset:40960
ds_read_b64_tr_b16 v[194:195], v234 offset:41472
v_mfma_f32_32x32x16_bf16 v[144:159], v[212:215], v[76:79], v[144:159]
ds_read_b64_tr_b16 v[196:197], v235 offset:40960
ds_read_b64_tr_b16 v[198:199], v235 offset:41472
s_add_u32 m0, s96, 1024
s_add_u32 s62, s89, s5
buffer_load_dwordx4 v241, s[28:31], s62 offen  lds
s_nop 14
s_waitcnt lgkmcnt(4)
v_mfma_f32_32x32x16_bf16 v[160:175], v[216:219], v[76:79], v[160:175]
ds_read_b64_tr_b16 v[200:201], v234 offset:45056
ds_read_b64_tr_b16 v[202:203], v234 offset:45568
v_mfma_f32_32x32x16_bf16 v[176:191], v[220:223], v[76:79], v[176:191]
ds_read_b64_tr_b16 v[204:205], v235 offset:45056
ds_read_b64_tr_b16 v[206:207], v235 offset:45568
s_add_u32 m0, s96, 5248
s_add_u32 s62, s89, s5
buffer_load_dwordx4 v241, s[32:35], s62 offen  lds
s_nop 14
s_waitcnt lgkmcnt(4)
v_mfma_f32_32x32x16_bf16 v[128:143], v[192:195], v[80:83], v[128:143]
ds_read_b64_tr_b16 v[208:209], v234 offset:43008
ds_read_b64_tr_b16 v[210:211], v234 offset:43520
v_mfma_f32_32x32x16_bf16 v[144:159], v[196:199], v[80:83], v[144:159]
ds_read_b64_tr_b16 v[212:213], v235 offset:43008
ds_read_b64_tr_b16 v[214:215], v235 offset:43520
s_nop 15
s_nop 1
s_waitcnt lgkmcnt(4)
v_mfma_f32_32x32x16_bf16 v[160:175], v[200:203], v[80:83], v[160:175]
ds_read_b64_tr_b16 v[216:217], v234 offset:47104
ds_read_b64_tr_b16 v[218:219], v234 offset:47616
v_mfma_f32_32x32x16_bf16 v[176:191], v[204:207], v[80:83], v[176:191]
ds_read_b64_tr_b16 v[220:221], v235 offset:47104
ds_read_b64_tr_b16 v[222:223], v235 offset:47616
s_nop 15
s_nop 1
s_waitcnt lgkmcnt(4)
v_mfma_f32_32x32x16_bf16 v[128:143], v[208:211], v[84:87], v[128:143]
ds_read_b64_tr_b16 v[192:193], v234 offset:57344
ds_read_b64_tr_b16 v[194:195], v234 offset:57856
v_mfma_f32_32x32x16_bf16 v[144:159], v[212:215], v[84:87], v[144:159]
ds_read_b64_tr_b16 v[196:197], v235 offset:57344
ds_read_b64_tr_b16 v[198:199], v235 offset:57856
s_nop 15
s_nop 1
s_waitcnt lgkmcnt(4)
v_mfma_f32_32x32x16_bf16 v[160:175], v[216:219], v[84:87], v[160:175]
ds_read_b64_tr_b16 v[200:201], v234 offset:61440
ds_read_b64_tr_b16 v[202:203], v234 offset:61952
v_mfma_f32_32x32x16_bf16 v[176:191], v[220:223], v[84:87], v[176:191]
ds_read_b64_tr_b16 v[204:205], v235 offset:61440
ds_read_b64_tr_b16 v[206:207], v235 offset:61952
s_nop 15
s_nop 1
s_waitcnt lgkmcnt(4)
v_mfma_f32_32x32x16_bf16 v[128:143], v[192:195], v[88:91], v[128:143]
ds_read_b64_tr_b16 v[208:209], v234 offset:59392
ds_read_b64_tr_b16 v[210:211], v234 offset:59904
v_mfma_f32_32x32x16_bf16 v[144:159], v[196:199], v[88:91], v[144:159]
ds_read_b64_tr_b16 v[212:213], v235 offset:59392
ds_read_b64_tr_b16 v[214:215], v235 offset:59904
s_nop 15
s_nop 1
s_waitcnt lgkmcnt(4)
v_mfma_f32_32x32x16_bf16 v[160:175], v[200:203], v[88:91], v[160:175]
ds_read_b64_tr_b16 v[216:217], v234 offset:63488
ds_read_b64_tr_b16 v[218:219], v234 offset:64000
v_mfma_f32_32x32x16_bf16 v[176:191], v[204:207], v[88:91], v[176:191]
ds_read_b64_tr_b16 v[220:221], v235 offset:63488
ds_read_b64_tr_b16 v[222:223], v235 offset:64000
s_nop 15
s_nop 1
s_waitcnt lgkmcnt(4)
v_mfma_f32_32x32x16_bf16 v[128:143], v[208:211], v[92:95], v[128:143]
ds_read_b64_tr_b16 v[192:193], v236 offset:0
ds_read_b64_tr_b16 v[194:195], v236 offset:512
v_mfma_f32_32x32x16_bf16 v[144:159], v[212:215], v[92:95], v[144:159]
ds_read_b64_tr_b16 v[196:197], v237 offset:0
ds_read_b64_tr_b16 v[198:199], v237 offset:512
s_nop 15
s_nop 1
s_waitcnt lgkmcnt(4)
v_mfma_f32_32x32x16_bf16 v[160:175], v[216:219], v[92:95], v[160:175]
ds_read_b64_tr_b16 v[200:201], v236 offset:4096
ds_read_b64_tr_b16 v[202:203], v236 offset:4608
v_mfma_f32_32x32x16_bf16 v[176:191], v[220:223], v[92:95], v[176:191]
ds_read_b64_tr_b16 v[204:205], v237 offset:4096
ds_read_b64_tr_b16 v[206:207], v237 offset:4608
s_waitcnt vmcnt(4)
ds_add_u32 v245, v246 offset:28672
s_nop 15
s_waitcnt lgkmcnt(5)
v_mfma_f32_32x32x16_bf16 v[128:143], v[192:195], v[96:99], v[128:143]
ds_read_b64_tr_b16 v[208:209], v236 offset:2048
ds_read_b64_tr_b16 v[210:211], v236 offset:2560
v_mfma_f32_32x32x16_bf16 v[144:159], v[196:199], v[96:99], v[144:159]
ds_read_b64_tr_b16 v[212:213], v237 offset:2048
ds_read_b64_tr_b16 v[214:215], v237 offset:2560
s_nop 15
s_nop 1
s_waitcnt lgkmcnt(5)
v_mfma_f32_32x32x16_bf16 v[160:175], v[200:203], v[96:99], v[160:175]
ds_read_b64_tr_b16 v[216:217], v236 offset:6144
ds_read_b64_tr_b16 v[218:219], v236 offset:6656
v_mfma_f32_32x32x16_bf16 v[176:191], v[204:207], v[96:99], v[176:191]
ds_read_b64_tr_b16 v[220:221], v237 offset:6144
ds_read_b64_tr_b16 v[222:223], v237 offset:6656
s_nop 15
s_nop 1
s_waitcnt lgkmcnt(4)
v_mfma_f32_32x32x16_bf16 v[128:143], v[208:211], v[100:103], v[128:143]
ds_read_b64_tr_b16 v[192:193], v236 offset:16384
ds_read_b64_tr_b16 v[194:195], v236 offset:16896
v_mfma_f32_32x32x16_bf16 v[144:159], v[212:215], v[100:103], v[144:159]
ds_read_b64_tr_b16 v[196:197], v237 offset:16384
ds_read_b64_tr_b16 v[198:199], v237 offset:16896
s_nop 15
s_nop 1
s_waitcnt lgkmcnt(4)
v_mfma_f32_32x32x16_bf16 v[160:175], v[216:219], v[100:103], v[160:175]
ds_read_b64_tr_b16 v[200:201], v236 offset:20480
ds_read_b64_tr_b16 v[202:203], v236 offset:20992
v_mfma_f32_32x32x16_bf16 v[176:191], v[220:223], v[100:103], v[176:191]
ds_read_b64_tr_b16 v[204:205], v237 offset:20480
ds_read_b64_tr_b16 v[206:207], v237 offset:20992
ds_read_b32 v227, v245 offset:28672
s_nop 15
s_nop 0
s_waitcnt lgkmcnt(5)
v_mfma_f32_32x32x16_bf16 v[128:143], v[192:195], v[104:107], v[128:143]
ds_read_b64_tr_b16 v[208:209], v236 offset:18432
ds_read_b64_tr_b16 v[210:211], v236 offset:18944
v_mfma_f32_32x32x16_bf16 v[144:159], v[196:199], v[104:107], v[144:159]
s_waitcnt lgkmcnt(2)
s_setprio 3
v_cmp_gt_u32 vcc, 16, v227
s_setprio 0
s_nop 0
s_cbranch_vccnz .Lk_ls_53
.Lk_ld_54:
ds_read_b64_tr_b16 v[212:213], v237 offset:18432
ds_read_b64_tr_b16 v[214:215], v237 offset:18944
s_add_u32 m0, s96, 8192
s_add_u32 s62, s90, s4
buffer_load_dwordx4 v241, s[28:31], s62 offen  lds
s_nop 7
v_mfma_f32_32x32x16_bf16 v[160:175], v[200:203], v[104:107], v[160:175]
ds_read_b64_tr_b16 v[216:217], v236 offset:22528
ds_read_b64_tr_b16 v[218:219], v236 offset:23040
v_mfma_f32_32x32x16_bf16 v[176:191], v[204:207], v[104:107], v[176:191]
ds_read_b64_tr_b16 v[220:221], v237 offset:22528
ds_read_b64_tr_b16 v[222:223], v237 offset:23040
s_add_u32 m0, s96, 12416
s_add_u32 s62, s90, s4
buffer_load_dwordx4 v241, s[32:35], s62 offen  lds
s_nop 14
s_waitcnt lgkmcnt(4)
v_mfma_f32_32x32x16_bf16 v[128:143], v[208:211], v[108:111], v[128:143]
ds_read_b64_tr_b16 v[192:193], v236 offset:32768
ds_read_b64_tr_b16 v[194:195], v236 offset:33280
v_mfma_f32_32x32x16_bf16 v[144:159], v[212:215], v[108:111], v[144:159]
ds_read_b64_tr_b16 v[196:197], v237 offset:32768
ds_read_b64_tr_b16 v[198:199], v237 offset:33280
s_add_u32 m0, s96, 9216
s_add_u32 s62, s90, s5
buffer_load_dwordx4 v241, s[28:31], s62 offen  lds
s_nop 14
s_waitcnt lgkmcnt(4)
v_mfma_f32_32x32x16_bf16 v[160:175], v[216:219], v[108:111], v[160:175]
ds_read_b64_tr_b16 v[200:201], v236 offset:36864
ds_read_b64_tr_b16 v[202:203], v236 offset:37376
v_mfma_f32_32x32x16_bf16 v[176:191], v[220:223], v[108:111], v[176:191]
ds_read_b64_tr_b16 v[204:205], v237 offset:36864
ds_read_b64_tr_b16 v[206:207], v237 offset:37376
s_add_u32 m0, s96, 13440
s_add_u32 s62, s90, s5
buffer_load_dwordx4 v241, s[32:35], s62 offen  lds
s_nop 14
s_waitcnt lgkmcnt(4)
v_mfma_f32_32x32x16_bf16 v[128:143], v[192:195], v[112:115], v[128:143]
)ASM" R"ASM(
ds_read_b64_tr_b16 v[208:209], v236 offset:34816
ds_read_b64_tr_b16 v[210:211], v236 offset:35328
v_mfma_f32_32x32x16_bf16 v[144:159], v[196:199], v[112:115], v[144:159]
ds_read_b64_tr_b16 v[212:213], v237 offset:34816
ds_read_b64_tr_b16 v[214:215], v237 offset:35328
s_mov_b64 exec, s[60:61]
buffer_load_dword v247, v247, s[24:27], s18 offen
s_mov_b64 exec, -1
s_nop 14
s_waitcnt lgkmcnt(4)
v_mfma_f32_32x32x16_bf16 v[160:175], v[200:203], v[112:115], v[160:175]
ds_read_b64_tr_b16 v[216:217], v236 offset:38912
ds_read_b64_tr_b16 v[218:219], v236 offset:39424
v_mfma_f32_32x32x16_bf16 v[176:191], v[204:207], v[112:115], v[176:191]
ds_read_b64_tr_b16 v[220:221], v237 offset:38912
ds_read_b64_tr_b16 v[222:223], v237 offset:39424
s_nop 15
s_nop 1
s_waitcnt lgkmcnt(4)
v_mfma_f32_32x32x16_bf16 v[128:143], v[208:211], v[116:119], v[128:143]
ds_read_b64_tr_b16 v[192:193], v236 offset:49152
ds_read_b64_tr_b16 v[194:195], v236 offset:49664
v_mfma_f32_32x32x16_bf16 v[144:159], v[212:215], v[116:119], v[144:159]
ds_read_b64_tr_b16 v[196:197], v237 offset:49152
ds_read_b64_tr_b16 v[198:199], v237 offset:49664
s_nop 15
s_nop 1
s_waitcnt lgkmcnt(4)
v_mfma_f32_32x32x16_bf16 v[160:175], v[216:219], v[116:119], v[160:175]
ds_read_b64_tr_b16 v[200:201], v236 offset:53248
ds_read_b64_tr_b16 v[202:203], v236 offset:53760
v_mfma_f32_32x32x16_bf16 v[176:191], v[220:223], v[116:119], v[176:191]
ds_read_b64_tr_b16 v[204:205], v237 offset:53248
ds_read_b64_tr_b16 v[206:207], v237 offset:53760
s_nop 15
s_nop 1
s_waitcnt lgkmcnt(4)
v_mfma_f32_32x32x16_bf16 v[128:143], v[192:195], v[120:123], v[128:143]
ds_read_b64_tr_b16 v[208:209], v236 offset:51200
ds_read_b64_tr_b16 v[210:211], v236 offset:51712
v_mfma_f32_32x32x16_bf16 v[144:159], v[196:199], v[120:123], v[144:159]
ds_read_b64_tr_b16 v[212:213], v237 offset:51200
ds_read_b64_tr_b16 v[214:215], v237 offset:51712
s_nop 15
s_nop 1
s_waitcnt lgkmcnt(4)
v_mfma_f32_32x32x16_bf16 v[160:175], v[200:203], v[120:123], v[160:175]
ds_read_b64_tr_b16 v[216:217], v236 offset:55296
ds_read_b64_tr_b16 v[218:219], v236 offset:55808
v_mfma_f32_32x32x16_bf16 v[176:191], v[204:207], v[120:123], v[176:191]
ds_read_b64_tr_b16 v[220:221], v237 offset:55296
ds_read_b64_tr_b16 v[222:223], v237 offset:55808
s_nop 15
s_nop 1
s_waitcnt lgkmcnt(4)
v_mfma_f32_32x32x16_bf16 v[128:143], v[208:211], v[124:127], v[128:143]
v_mfma_f32_32x32x16_bf16 v[144:159], v[212:215], v[124:127], v[144:159]
s_nop 15
s_nop 3
s_waitcnt lgkmcnt(0)
v_mfma_f32_32x32x16_bf16 v[160:175], v[216:219], v[124:127], v[160:175]
v_mfma_f32_32x32x16_bf16 v[176:191], v[220:223], v[124:127], v[176:191]
ds_read_b64_tr_b16 v[192:193], v238 offset:8192
ds_read_b64_tr_b16 v[194:195], v238 offset:8448
ds_read_b64_tr_b16 v[196:197], v238 offset:12416
ds_read_b64_tr_b16 v[198:199], v238 offset:12672
ds_read_b64_tr_b16 v[200:201], v238 offset:9216
ds_read_b64_tr_b16 v[202:203], v238 offset:9472
ds_read_b64_tr_b16 v[204:205], v238 offset:13440
ds_read_b64_tr_b16 v[206:207], v238 offset:13696
s_setprio 3
v_pk_mul_f32 v[128:129], v[128:129], v[232:233]
v_pk_mul_f32 v[130:131], v[130:131], v[232:233]
v_pk_mul_f32 v[132:133], v[132:133], v[232:233]
v_pk_mul_f32 v[134:135], v[134:135], v[232:233]
v_exp_f32 v128, v128
v_exp_f32 v129, v129
v_exp_f32 v130, v130
v_exp_f32 v131, v131
v_exp_f32 v132, v132
v_exp_f32 v133, v133
v_exp_f32 v134, v134
v_exp_f32 v135, v135
v_pk_fma_f32 v[128:129], v[128:129], v[230:231], v[230:231]
v_pk_fma_f32 v[130:131], v[130:131], v[230:231], v[230:231]
v_pk_fma_f32 v[132:133], v[132:133], v[230:231], v[230:231]
v_pk_fma_f32 v[134:135], v[134:135], v[230:231], v[230:231]
v_rcp_f32 v128, v128
v_rcp_f32 v129, v129
v_rcp_f32 v130, v130
v_rcp_f32 v131, v131
v_rcp_f32 v132, v132
v_rcp_f32 v133, v133
v_rcp_f32 v134, v134
v_rcp_f32 v135, v135
v_pk_mul_f32 v[160:161], v[160:161], v[128:129]
v_pk_mul_f32 v[162:163], v[162:163], v[130:131]
v_pk_mul_f32 v[164:165], v[164:165], v[132:133]
v_pk_mul_f32 v[166:167], v[166:167], v[134:135]
v_cvt_pk_bf16_f32 v128, v160, v161
v_cvt_pk_bf16_f32 v129, v162, v163
v_cvt_pk_bf16_f32 v130, v164, v165
v_cvt_pk_bf16_f32 v131, v166, v167
buffer_store_dwordx4 v[128:131], v244, s[48:51], s7 offen offset:0 sc1
v_pk_mul_f32 v[136:137], v[136:137], v[232:233]
v_pk_mul_f32 v[138:139], v[138:139], v[232:233]
v_pk_mul_f32 v[140:141], v[140:141], v[232:233]
v_pk_mul_f32 v[142:143], v[142:143], v[232:233]
v_exp_f32 v136, v136
v_exp_f32 v137, v137
v_exp_f32 v138, v138
v_exp_f32 v139, v139
v_exp_f32 v140, v140
v_exp_f32 v141, v141
v_exp_f32 v142, v142
v_exp_f32 v143, v143
v_pk_fma_f32 v[136:137], v[136:137], v[230:231], v[230:231]
v_pk_fma_f32 v[138:139], v[138:139], v[230:231], v[230:231]
v_pk_fma_f32 v[140:141], v[140:141], v[230:231], v[230:231]
v_pk_fma_f32 v[142:143], v[142:143], v[230:231], v[230:231]
v_rcp_f32 v136, v136
v_rcp_f32 v137, v137
v_rcp_f32 v138, v138
v_rcp_f32 v139, v139
v_rcp_f32 v140, v140
v_rcp_f32 v141, v141
v_rcp_f32 v142, v142
v_rcp_f32 v143, v143
v_pk_mul_f32 v[168:169], v[168:169], v[136:137]
v_pk_mul_f32 v[170:171], v[170:171], v[138:139]
v_pk_mul_f32 v[172:173], v[172:173], v[140:141]
v_pk_mul_f32 v[174:175], v[174:175], v[142:143]
v_cvt_pk_bf16_f32 v136, v168, v169
v_cvt_pk_bf16_f32 v137, v170, v171
v_cvt_pk_bf16_f32 v138, v172, v173
v_cvt_pk_bf16_f32 v139, v174, v175
buffer_store_dwordx4 v[136:139], v244, s[48:51], s7 offen offset:1024 sc1
v_pk_mul_f32 v[144:145], v[144:145], v[232:233]
v_pk_mul_f32 v[146:147], v[146:147], v[232:233]
v_pk_mul_f32 v[148:149], v[148:149], v[232:233]
v_pk_mul_f32 v[150:151], v[150:151], v[232:233]
v_exp_f32 v144, v144
v_exp_f32 v145, v145
v_exp_f32 v146, v146
v_exp_f32 v147, v147
v_exp_f32 v148, v148
v_exp_f32 v149, v149
v_exp_f32 v150, v150
v_exp_f32 v151, v151
v_pk_fma_f32 v[144:145], v[144:145], v[230:231], v[230:231]
v_pk_fma_f32 v[146:147], v[146:147], v[230:231], v[230:231]
v_pk_fma_f32 v[148:149], v[148:149], v[230:231], v[230:231]
v_pk_fma_f32 v[150:151], v[150:151], v[230:231], v[230:231]
v_rcp_f32 v144, v144
v_rcp_f32 v145, v145
v_rcp_f32 v146, v146
v_rcp_f32 v147, v147
v_rcp_f32 v148, v148
v_rcp_f32 v149, v149
v_rcp_f32 v150, v150
v_rcp_f32 v151, v151
v_pk_mul_f32 v[176:177], v[176:177], v[144:145]
v_pk_mul_f32 v[178:179], v[178:179], v[146:147]
v_pk_mul_f32 v[180:181], v[180:181], v[148:149]
v_pk_mul_f32 v[182:183], v[182:183], v[150:151]
v_cvt_pk_bf16_f32 v144, v176, v177
v_cvt_pk_bf16_f32 v145, v178, v179
v_cvt_pk_bf16_f32 v146, v180, v181
v_cvt_pk_bf16_f32 v147, v182, v183
buffer_store_dwordx4 v[144:147], v244, s[48:51], s7 offen offset:2048 sc1
v_pk_mul_f32 v[152:153], v[152:153], v[232:233]
v_pk_mul_f32 v[154:155], v[154:155], v[232:233]
v_pk_mul_f32 v[156:157], v[156:157], v[232:233]
v_pk_mul_f32 v[158:159], v[158:159], v[232:233]
v_exp_f32 v152, v152
v_exp_f32 v153, v153
v_exp_f32 v154, v154
v_exp_f32 v155, v155
v_exp_f32 v156, v156
v_exp_f32 v157, v157
v_exp_f32 v158, v158
v_exp_f32 v159, v159
v_pk_fma_f32 v[152:153], v[152:153], v[230:231], v[230:231]
v_pk_fma_f32 v[154:155], v[154:155], v[230:231], v[230:231]
v_pk_fma_f32 v[156:157], v[156:157], v[230:231], v[230:231]
v_pk_fma_f32 v[158:159], v[158:159], v[230:231], v[230:231]
v_rcp_f32 v152, v152
v_rcp_f32 v153, v153
v_rcp_f32 v154, v154
v_rcp_f32 v155, v155
v_rcp_f32 v156, v156
v_rcp_f32 v157, v157
v_rcp_f32 v158, v158
v_rcp_f32 v159, v159
v_pk_mul_f32 v[184:185], v[184:185], v[152:153]
v_pk_mul_f32 v[186:187], v[186:187], v[154:155]
v_pk_mul_f32 v[188:189], v[188:189], v[156:157]
v_pk_mul_f32 v[190:191], v[190:191], v[158:159]
v_cvt_pk_bf16_f32 v152, v184, v185
v_cvt_pk_bf16_f32 v153, v186, v187
)ASM" R"ASM(
v_cvt_pk_bf16_f32 v154, v188, v189
v_cvt_pk_bf16_f32 v155, v190, v191
buffer_store_dwordx4 v[152:155], v244, s[48:51], s7 offen offset:3072 sc1
v_and_b32 v243, 15, v224
v_lshrrev_b32 v176, 4, v224
v_lshlrev_b32 v243, 4, v243
v_lshl_add_u32 v243, v176, 9, v243
s_setprio 0
s_waitcnt vmcnt(9)
ds_add_u32 v245, v246 offset:28672
s_waitcnt lgkmcnt(5)
v_mfma_f32_32x32x16_bf16 v[128:143], v[192:195], v[0:3], 0
ds_read_b64_tr_b16 v[208:209], v238 offset:24576
ds_read_b64_tr_b16 v[210:211], v238 offset:24832
v_mfma_f32_32x32x16_bf16 v[144:159], v[196:199], v[0:3], 0
ds_read_b64_tr_b16 v[212:213], v238 offset:28800
ds_read_b64_tr_b16 v[214:215], v238 offset:29056
s_nop 15
s_nop 1
s_waitcnt lgkmcnt(5)
v_mfma_f32_32x32x16_bf16 v[128:143], v[200:203], v[4:7], v[128:143]
ds_read_b64_tr_b16 v[216:217], v238 offset:25600
ds_read_b64_tr_b16 v[218:219], v238 offset:25856
v_mfma_f32_32x32x16_bf16 v[144:159], v[204:207], v[4:7], v[144:159]
ds_read_b64_tr_b16 v[220:221], v238 offset:29824
ds_read_b64_tr_b16 v[222:223], v238 offset:30080
s_nop 15
s_nop 1
s_waitcnt lgkmcnt(4)
v_mfma_f32_32x32x16_bf16 v[128:143], v[208:211], v[8:11], v[128:143]
ds_read_b64_tr_b16 v[192:193], v238 offset:40960
ds_read_b64_tr_b16 v[194:195], v238 offset:41216
v_mfma_f32_32x32x16_bf16 v[144:159], v[212:215], v[8:11], v[144:159]
ds_read_b64_tr_b16 v[196:197], v238 offset:45184
ds_read_b64_tr_b16 v[198:199], v238 offset:45440
s_nop 15
s_nop 1
s_waitcnt lgkmcnt(4)
v_mfma_f32_32x32x16_bf16 v[128:143], v[216:219], v[12:15], v[128:143]
ds_read_b64_tr_b16 v[200:201], v238 offset:41984
ds_read_b64_tr_b16 v[202:203], v238 offset:42240
v_mfma_f32_32x32x16_bf16 v[144:159], v[220:223], v[12:15], v[144:159]
ds_read_b64_tr_b16 v[204:205], v238 offset:46208
ds_read_b64_tr_b16 v[206:207], v238 offset:46464
ds_read_b32 v227, v245 offset:28672
s_waitcnt vmcnt(0)
s_mov_b64 exec, 1
buffer_store_dwordx2 v[248:249], off, s[48:51], s16 offset:0 sc1
s_mov_b64 exec, -1
s_nop 12
s_waitcnt lgkmcnt(5)
v_mfma_f32_32x32x16_bf16 v[128:143], v[192:195], v[16:19], v[128:143]
ds_read_b64_tr_b16 v[208:209], v238 offset:57344
ds_read_b64_tr_b16 v[210:211], v238 offset:57600
v_mfma_f32_32x32x16_bf16 v[144:159], v[196:199], v[16:19], v[144:159]
s_waitcnt lgkmcnt(2)
s_setprio 3
v_cmp_gt_u32 vcc, 20, v227
s_setprio 0
s_nop 0
s_cbranch_vccnz .Lk_ls_55
.Lk_ld_56:
ds_read_b64_tr_b16 v[212:213], v238 offset:61568
ds_read_b64_tr_b16 v[214:215], v238 offset:61824
s_add_u32 m0, s96, 65536
s_add_u32 s62, s91, s4
buffer_load_dwordx4 v241, s[28:31], s62 offen  lds
s_nop 7
v_mfma_f32_32x32x16_bf16 v[128:143], v[200:203], v[20:23], v[128:143]
ds_read_b64_tr_b16 v[216:217], v238 offset:58368
ds_read_b64_tr_b16 v[218:219], v238 offset:58624
v_mfma_f32_32x32x16_bf16 v[144:159], v[204:207], v[20:23], v[144:159]
ds_read_b64_tr_b16 v[220:221], v238 offset:62592
ds_read_b64_tr_b16 v[222:223], v238 offset:62848
s_add_u32 m0, s96, 69760
s_add_u32 s62, s91, s4
buffer_load_dwordx4 v241, s[32:35], s62 offen  lds
s_nop 14
s_waitcnt lgkmcnt(4)
v_mfma_f32_32x32x16_bf16 v[128:143], v[208:211], v[24:27], v[128:143]
ds_read_b64_tr_b16 v[192:193], v239 offset:0
ds_read_b64_tr_b16 v[194:195], v239 offset:256
v_mfma_f32_32x32x16_bf16 v[144:159], v[212:215], v[24:27], v[144:159]
ds_read_b64_tr_b16 v[196:197], v239 offset:4224
ds_read_b64_tr_b16 v[198:199], v239 offset:4480
s_add_u32 m0, s96, 66560
s_add_u32 s62, s91, s5
buffer_load_dwordx4 v241, s[28:31], s62 offen  lds
s_nop 14
s_waitcnt lgkmcnt(4)
v_mfma_f32_32x32x16_bf16 v[128:143], v[216:219], v[28:31], v[128:143]
ds_read_b64_tr_b16 v[200:201], v239 offset:1024
ds_read_b64_tr_b16 v[202:203], v239 offset:1280
v_mfma_f32_32x32x16_bf16 v[144:159], v[220:223], v[28:31], v[144:159]
ds_read_b64_tr_b16 v[204:205], v239 offset:5248
ds_read_b64_tr_b16 v[206:207], v239 offset:5504
s_add_u32 m0, s96, 70784
s_add_u32 s62, s91, s5
buffer_load_dwordx4 v241, s[32:35], s62 offen  lds
ds_add_u32 v245, v246 offset:28672
s_nop 13
s_waitcnt lgkmcnt(5)
v_mfma_f32_32x32x16_bf16 v[128:143], v[192:195], v[32:35], v[128:143]
ds_read_b64_tr_b16 v[208:209], v239 offset:16384
ds_read_b64_tr_b16 v[210:211], v239 offset:16640
v_mfma_f32_32x32x16_bf16 v[144:159], v[196:199], v[32:35], v[144:159]
ds_read_b64_tr_b16 v[212:213], v239 offset:20608
ds_read_b64_tr_b16 v[214:215], v239 offset:20864
s_nop 15
s_nop 1
s_waitcnt lgkmcnt(5)
v_mfma_f32_32x32x16_bf16 v[128:143], v[200:203], v[36:39], v[128:143]
ds_read_b64_tr_b16 v[216:217], v239 offset:17408
ds_read_b64_tr_b16 v[218:219], v239 offset:17664
v_mfma_f32_32x32x16_bf16 v[144:159], v[204:207], v[36:39], v[144:159]
ds_read_b64_tr_b16 v[220:221], v239 offset:21632
ds_read_b64_tr_b16 v[222:223], v239 offset:21888
s_nop 15
s_nop 1
s_waitcnt lgkmcnt(4)
v_mfma_f32_32x32x16_bf16 v[128:143], v[208:211], v[40:43], v[128:143]
ds_read_b64_tr_b16 v[192:193], v239 offset:32768
ds_read_b64_tr_b16 v[194:195], v239 offset:33024
v_mfma_f32_32x32x16_bf16 v[144:159], v[212:215], v[40:43], v[144:159]
ds_read_b64_tr_b16 v[196:197], v239 offset:36992
ds_read_b64_tr_b16 v[198:199], v239 offset:37248
s_nop 15
s_nop 1
s_waitcnt lgkmcnt(4)
v_mfma_f32_32x32x16_bf16 v[128:143], v[216:219], v[44:47], v[128:143]
ds_read_b64_tr_b16 v[200:201], v239 offset:33792
ds_read_b64_tr_b16 v[202:203], v239 offset:34048
v_mfma_f32_32x32x16_bf16 v[144:159], v[220:223], v[44:47], v[144:159]
ds_read_b64_tr_b16 v[204:205], v239 offset:38016
ds_read_b64_tr_b16 v[206:207], v239 offset:38272
ds_read_b32 v227, v245 offset:28672
s_nop 15
s_nop 0
s_waitcnt lgkmcnt(5)
v_mfma_f32_32x32x16_bf16 v[128:143], v[192:195], v[48:51], v[128:143]
ds_read_b64_tr_b16 v[208:209], v239 offset:49152
ds_read_b64_tr_b16 v[210:211], v239 offset:49408
v_mfma_f32_32x32x16_bf16 v[144:159], v[196:199], v[48:51], v[144:159]
s_waitcnt lgkmcnt(2)
s_setprio 3
v_cmp_gt_u32 vcc, 24, v227
s_setprio 0
s_nop 0
s_cbranch_vccnz .Lk_ls_57
.Lk_ld_58:
ds_read_b64_tr_b16 v[212:213], v239 offset:53376
ds_read_b64_tr_b16 v[214:215], v239 offset:53632
s_add_u32 m0, s96, 73728
s_add_u32 s62, s11, 0
buffer_load_dwordx4 v242, s[36:39], s62 offen  lds
s_add_u32 m0, s96, 74752
s_add_u32 s62, s11, 8192
buffer_load_dwordx4 v242, s[36:39], s62 offen  lds
s_add_u32 m0, s96, 75776
s_add_u32 s62, s11, 16384
buffer_load_dwordx4 v242, s[36:39], s62 offen  lds
s_add_u32 m0, s96, 76800
s_add_u32 s62, s11, 24576
buffer_load_dwordx4 v242, s[36:39], s62 offen  lds
s_add_u32 m0, s96, 77824
s_add_u32 s62, s11, 32768
buffer_load_dwordx4 v242, s[36:39], s62 offen  lds
v_mfma_f32_32x32x16_bf16 v[128:143], v[200:203], v[52:55], v[128:143]
ds_read_b64_tr_b16 v[216:217], v239 offset:50176
ds_read_b64_tr_b16 v[218:219], v239 offset:50432
v_mfma_f32_32x32x16_bf16 v[144:159], v[204:207], v[52:55], v[144:159]
ds_read_b64_tr_b16 v[220:221], v239 offset:54400
ds_read_b64_tr_b16 v[222:223], v239 offset:54656
buffer_load_dwordx2 v[250:251], v225, s[48:51], 0 offen offset:0 sc1
s_add_u32 m0, s96, 78848
s_add_u32 s62, s11, 40960
buffer_load_dwordx4 v242, s[36:39], s62 offen  lds
s_nop 13
s_waitcnt lgkmcnt(4)
v_mfma_f32_32x32x16_bf16 v[128:143], v[208:211], v[56:59], v[128:143]
ds_read_b64_tr_b16 v[192:193], v239 offset:8192
ds_read_b64_tr_b16 v[194:195], v239 offset:8448
v_mfma_f32_32x32x16_bf16 v[144:159], v[212:215], v[56:59], v[144:159]
ds_read_b64_tr_b16 v[196:197], v239 offset:12416
ds_read_b64_tr_b16 v[198:199], v239 offset:12672
s_add_u32 m0, s96, 79872
s_add_u32 s62, s11, 49152
buffer_load_dwordx4 v242, s[36:39], s62 offen  lds
s_nop 14
s_waitcnt lgkmcnt(4)
v_mfma_f32_32x32x16_bf16 v[128:143], v[216:219], v[60:63], v[128:143]
ds_read_b64_tr_b16 v[200:201], v239 offset:9216
ds_read_b64_tr_b16 v[202:203], v239 offset:9472
v_mfma_f32_32x32x16_bf16 v[144:159], v[220:223], v[60:63], v[144:159]
ds_read_b64_tr_b16 v[204:205], v239 offset:13440
ds_read_b64_tr_b16 v[206:207], v239 offset:13696
s_add_u32 m0, s96, 80896
s_add_u32 s62, s11, 57344
buffer_load_dwordx4 v242, s[36:39], s62 offen  lds
s_waitcnt vmcnt(9)
)ASM" R"ASM(
ds_add_u32 v245, v246 offset:28672
s_waitcnt vmcnt(3)
s_setprio 3
v_cmp_ne_u64 vcc, v[248:249], v[250:251]
s_setprio 0
s_nop 0
s_cbranch_vccnz .Lk_ps_59
.Lk_pd_60:
s_nop 5
s_waitcnt lgkmcnt(5)
v_mfma_f32_32x32x16_bf16 v[128:143], v[192:195], v[64:67], v[128:143]
ds_read_b64_tr_b16 v[208:209], v239 offset:24576
ds_read_b64_tr_b16 v[210:211], v239 offset:24832
v_mfma_f32_32x32x16_bf16 v[144:159], v[196:199], v[64:67], v[144:159]
ds_read_b64_tr_b16 v[212:213], v239 offset:28800
ds_read_b64_tr_b16 v[214:215], v239 offset:29056
s_add_u32 s62, s13, 0
buffer_load_dwordx4 v[0:3], v243, s[48:51], s62 offen offset:0 sc1
buffer_load_dwordx4 v[4:7], v243, s[48:51], s62 offen offset:256 sc1
s_add_u32 s62, s13, 147456
buffer_load_dwordx4 v[8:11], v243, s[48:51], s62 offen offset:0 sc1
buffer_load_dwordx4 v[12:15], v243, s[48:51], s62 offen offset:256 sc1
s_add_u32 s62, s13, 0
buffer_load_dwordx4 v[16:19], v243, s[48:51], s62 offen offset:2048 sc1
buffer_load_dwordx4 v[20:23], v243, s[48:51], s62 offen offset:2304 sc1
s_add_u32 s62, s13, 147456
buffer_load_dwordx4 v[24:27], v243, s[48:51], s62 offen offset:2048 sc1
buffer_load_dwordx4 v[28:31], v243, s[48:51], s62 offen offset:2304 sc1
s_add_u32 s62, s13, 8192
buffer_load_dwordx4 v[32:35], v243, s[48:51], s62 offen offset:0 sc1
buffer_load_dwordx4 v[36:39], v243, s[48:51], s62 offen offset:256 sc1
s_add_u32 s62, s13, 155648
buffer_load_dwordx4 v[40:43], v243, s[48:51], s62 offen offset:0 sc1
buffer_load_dwordx4 v[44:47], v243, s[48:51], s62 offen offset:256 sc1
s_add_u32 s62, s13, 8192
buffer_load_dwordx4 v[48:51], v243, s[48:51], s62 offen offset:2048 sc1
buffer_load_dwordx4 v[52:55], v243, s[48:51], s62 offen offset:2304 sc1
s_add_u32 s62, s13, 155648
buffer_load_dwordx4 v[56:59], v243, s[48:51], s62 offen offset:2048 sc1
buffer_load_dwordx4 v[60:63], v243, s[48:51], s62 offen offset:2304 sc1
s_waitcnt lgkmcnt(5)
v_mfma_f32_32x32x16_bf16 v[128:143], v[200:203], v[68:71], v[128:143]
ds_read_b64_tr_b16 v[216:217], v239 offset:25600
ds_read_b64_tr_b16 v[218:219], v239 offset:25856
v_mfma_f32_32x32x16_bf16 v[144:159], v[204:207], v[68:71], v[144:159]
ds_read_b64_tr_b16 v[220:221], v239 offset:29824
ds_read_b64_tr_b16 v[222:223], v239 offset:30080
s_nop 15
s_nop 1
s_waitcnt lgkmcnt(4)
v_mfma_f32_32x32x16_bf16 v[128:143], v[208:211], v[72:75], v[128:143]
ds_read_b64_tr_b16 v[192:193], v239 offset:40960
ds_read_b64_tr_b16 v[194:195], v239 offset:41216
v_mfma_f32_32x32x16_bf16 v[144:159], v[212:215], v[72:75], v[144:159]
ds_read_b64_tr_b16 v[196:197], v239 offset:45184
ds_read_b64_tr_b16 v[198:199], v239 offset:45440
s_nop 15
s_nop 1
s_waitcnt lgkmcnt(4)
v_mfma_f32_32x32x16_bf16 v[128:143], v[216:219], v[76:79], v[128:143]
ds_read_b64_tr_b16 v[200:201], v239 offset:41984
ds_read_b64_tr_b16 v[202:203], v239 offset:42240
v_mfma_f32_32x32x16_bf16 v[144:159], v[220:223], v[76:79], v[144:159]
ds_read_b64_tr_b16 v[204:205], v239 offset:46208
ds_read_b64_tr_b16 v[206:207], v239 offset:46464
ds_read_b32 v227, v245 offset:28672
s_nop 15
s_nop 0
s_waitcnt lgkmcnt(5)
v_mfma_f32_32x32x16_bf16 v[128:143], v[192:195], v[80:83], v[128:143]
ds_read_b64_tr_b16 v[208:209], v239 offset:57344
ds_read_b64_tr_b16 v[210:211], v239 offset:57600
v_mfma_f32_32x32x16_bf16 v[144:159], v[196:199], v[80:83], v[144:159]
s_waitcnt lgkmcnt(2)
s_setprio 3
v_cmp_gt_u32 vcc, 28, v227
s_setprio 0
s_nop 0
s_cbranch_vccnz .Lk_ls_61
.Lk_ld_62:
ds_read_b64_tr_b16 v[212:213], v239 offset:61568
ds_read_b64_tr_b16 v[214:215], v239 offset:61824
s_add_u32 m0, s96, 0
s_add_u32 s62, s11, 131072
buffer_load_dwordx4 v242, s[36:39], s62 offen  lds
s_add_u32 m0, s96, 1024
s_add_u32 s62, s11, 139264
buffer_load_dwordx4 v242, s[36:39], s62 offen  lds
s_add_u32 m0, s96, 2048
s_add_u32 s62, s11, 147456
buffer_load_dwordx4 v242, s[36:39], s62 offen  lds
s_add_u32 m0, s96, 3072
s_add_u32 s62, s11, 155648
buffer_load_dwordx4 v242, s[36:39], s62 offen  lds
s_add_u32 m0, s96, 4096
s_add_u32 s62, s11, 163840
buffer_load_dwordx4 v242, s[36:39], s62 offen  lds
v_mfma_f32_32x32x16_bf16 v[128:143], v[200:203], v[84:87], v[128:143]
ds_read_b64_tr_b16 v[216:217], v239 offset:58368
ds_read_b64_tr_b16 v[218:219], v239 offset:58624
v_mfma_f32_32x32x16_bf16 v[144:159], v[204:207], v[84:87], v[144:159]
ds_read_b64_tr_b16 v[220:221], v239 offset:62592
ds_read_b64_tr_b16 v[222:223], v239 offset:62848
s_add_u32 m0, s96, 5120
s_add_u32 s62, s11, 172032
buffer_load_dwordx4 v242, s[36:39], s62 offen  lds
s_nop 14
s_waitcnt lgkmcnt(4)
v_mfma_f32_32x32x16_bf16 v[128:143], v[208:211], v[88:91], v[128:143]
ds_read_b64_tr_b16 v[192:193], v238 offset:0
ds_read_b64_tr_b16 v[194:195], v238 offset:256
v_mfma_f32_32x32x16_bf16 v[144:159], v[212:215], v[88:91], v[144:159]
ds_read_b64_tr_b16 v[196:197], v238 offset:4224
ds_read_b64_tr_b16 v[198:199], v238 offset:4480
s_add_u32 m0, s96, 6144
s_add_u32 s62, s11, 180224
buffer_load_dwordx4 v242, s[36:39], s62 offen  lds
s_nop 14
s_waitcnt lgkmcnt(4)
v_mfma_f32_32x32x16_bf16 v[128:143], v[216:219], v[92:95], v[128:143]
ds_read_b64_tr_b16 v[200:201], v238 offset:1024
ds_read_b64_tr_b16 v[202:203], v238 offset:1280
v_mfma_f32_32x32x16_bf16 v[144:159], v[220:223], v[92:95], v[144:159]
ds_read_b64_tr_b16 v[204:205], v238 offset:5248
ds_read_b64_tr_b16 v[206:207], v238 offset:5504
s_add_u32 m0, s96, 7168
s_add_u32 s62, s11, 188416
buffer_load_dwordx4 v242, s[36:39], s62 offen  lds
ds_add_u32 v245, v246 offset:28672
s_nop 13
s_waitcnt lgkmcnt(5)
v_mfma_f32_32x32x16_bf16 v[128:143], v[192:195], v[96:99], v[128:143]
ds_read_b64_tr_b16 v[208:209], v238 offset:16384
ds_read_b64_tr_b16 v[210:211], v238 offset:16640
v_mfma_f32_32x32x16_bf16 v[144:159], v[196:199], v[96:99], v[144:159]
ds_read_b64_tr_b16 v[212:213], v238 offset:20608
ds_read_b64_tr_b16 v[214:215], v238 offset:20864
s_add_u32 s62, s13, 16384
buffer_load_dwordx4 v[64:67], v243, s[48:51], s62 offen offset:0 sc1
buffer_load_dwordx4 v[68:71], v243, s[48:51], s62 offen offset:256 sc1
s_add_u32 s62, s13, 163840
buffer_load_dwordx4 v[72:75], v243, s[48:51], s62 offen offset:0 sc1
buffer_load_dwordx4 v[76:79], v243, s[48:51], s62 offen offset:256 sc1
s_add_u32 s62, s13, 16384
buffer_load_dwordx4 v[80:83], v243, s[48:51], s62 offen offset:2048 sc1
buffer_load_dwordx4 v[84:87], v243, s[48:51], s62 offen offset:2304 sc1
s_add_u32 s62, s13, 163840
buffer_load_dwordx4 v[88:91], v243, s[48:51], s62 offen offset:2048 sc1
buffer_load_dwordx4 v[92:95], v243, s[48:51], s62 offen offset:2304 sc1
s_nop 5
s_waitcnt lgkmcnt(5)
v_mfma_f32_32x32x16_bf16 v[128:143], v[200:203], v[100:103], v[128:143]
ds_read_b64_tr_b16 v[216:217], v238 offset:17408
ds_read_b64_tr_b16 v[218:219], v238 offset:17664
v_mfma_f32_32x32x16_bf16 v[144:159], v[204:207], v[100:103], v[144:159]
ds_read_b64_tr_b16 v[220:221], v238 offset:21632
ds_read_b64_tr_b16 v[222:223], v238 offset:21888
s_nop 15
s_nop 1
s_waitcnt lgkmcnt(4)
v_mfma_f32_32x32x16_bf16 v[128:143], v[208:211], v[104:107], v[128:143]
ds_read_b64_tr_b16 v[192:193], v238 offset:32768
ds_read_b64_tr_b16 v[194:195], v238 offset:33024
v_mfma_f32_32x32x16_bf16 v[144:159], v[212:215], v[104:107], v[144:159]
ds_read_b64_tr_b16 v[196:197], v238 offset:36992
ds_read_b64_tr_b16 v[198:199], v238 offset:37248
s_nop 15
s_nop 1
s_waitcnt lgkmcnt(4)
v_mfma_f32_32x32x16_bf16 v[128:143], v[216:219], v[108:111], v[128:143]
ds_read_b64_tr_b16 v[200:201], v238 offset:33792
ds_read_b64_tr_b16 v[202:203], v238 offset:34048
v_mfma_f32_32x32x16_bf16 v[144:159], v[220:223], v[108:111], v[144:159]
ds_read_b64_tr_b16 v[204:205], v238 offset:38016
ds_read_b64_tr_b16 v[206:207], v238 offset:38272
ds_read_b32 v227, v245 offset:28672
s_nop 15
s_nop 0
s_waitcnt lgkmcnt(5)
v_mfma_f32_32x32x16_bf16 v[128:143], v[192:195], v[112:115], v[128:143]
ds_read_b64_tr_b16 v[208:209], v238 offset:49152
ds_read_b64_tr_b16 v[210:211], v238 offset:49408
v_mfma_f32_32x32x16_bf16 v[144:159], v[196:199], v[112:115], v[144:159]
s_waitcnt lgkmcnt(2)
s_setprio 3
v_cmp_gt_u32 vcc, 32, v227
s_setprio 0
s_nop 0
s_cbranch_vccnz .Lk_ls_63
.Lk_ld_64:
ds_read_b64_tr_b16 v[212:213], v238 offset:53376
ds_read_b64_tr_b16 v[214:215], v238 offset:53632
s_add_u32 m0, s96, 8192
s_add_u32 s62, s11, 262144
buffer_load_dwordx4 v242, s[36:39], s62 offen  lds
s_add_u32 m0, s96, 9216
s_add_u32 s62, s11, 270336
buffer_load_dwordx4 v242, s[36:39], s62 offen  lds
s_add_u32 m0, s96, 10240
s_add_u32 s62, s11, 278528
)ASM" R"ASM(
buffer_load_dwordx4 v242, s[36:39], s62 offen  lds
s_add_u32 m0, s96, 11264
s_add_u32 s62, s11, 286720
buffer_load_dwordx4 v242, s[36:39], s62 offen  lds
s_add_u32 m0, s96, 12288
s_add_u32 s62, s11, 294912
buffer_load_dwordx4 v242, s[36:39], s62 offen  lds
v_mfma_f32_32x32x16_bf16 v[128:143], v[200:203], v[116:119], v[128:143]
ds_read_b64_tr_b16 v[216:217], v238 offset:50176
ds_read_b64_tr_b16 v[218:219], v238 offset:50432
v_mfma_f32_32x32x16_bf16 v[144:159], v[204:207], v[116:119], v[144:159]
ds_read_b64_tr_b16 v[220:221], v238 offset:54400
ds_read_b64_tr_b16 v[222:223], v238 offset:54656
s_add_u32 m0, s96, 13312
s_add_u32 s62, s11, 303104
buffer_load_dwordx4 v242, s[36:39], s62 offen  lds
s_nop 14
s_waitcnt lgkmcnt(4)
v_mfma_f32_32x32x16_bf16 v[128:143], v[208:211], v[120:123], v[128:143]
v_mfma_f32_32x32x16_bf16 v[144:159], v[212:215], v[120:123], v[144:159]
s_add_u32 m0, s96, 14336
s_add_u32 s62, s11, 311296
buffer_load_dwordx4 v242, s[36:39], s62 offen  lds
s_nop 15
s_nop 0
s_waitcnt lgkmcnt(0)
v_mfma_f32_32x32x16_bf16 v[128:143], v[216:219], v[124:127], v[128:143]
v_mfma_f32_32x32x16_bf16 v[144:159], v[220:223], v[124:127], v[144:159]
s_add_u32 m0, s96, 15360
s_add_u32 s62, s11, 319488
buffer_load_dwordx4 v242, s[36:39], s62 offen  lds
ds_add_u32 v245, v246 offset:28672
s_add_u32 s62, s13, 24576
buffer_load_dwordx4 v[96:99], v243, s[48:51], s62 offen offset:0 sc1
buffer_load_dwordx4 v[100:103], v243, s[48:51], s62 offen offset:256 sc1
s_add_u32 s62, s13, 172032
buffer_load_dwordx4 v[104:107], v243, s[48:51], s62 offen offset:0 sc1
buffer_load_dwordx4 v[108:111], v243, s[48:51], s62 offen offset:256 sc1
s_add_u32 s62, s13, 24576
buffer_load_dwordx4 v[112:115], v243, s[48:51], s62 offen offset:2048 sc1
buffer_load_dwordx4 v[116:119], v243, s[48:51], s62 offen offset:2304 sc1
s_add_u32 s62, s13, 172032
buffer_load_dwordx4 v[120:123], v243, s[48:51], s62 offen offset:2048 sc1
buffer_load_dwordx4 v[124:127], v243, s[48:51], s62 offen offset:2304 sc1
s_setprio 3
v_pk_mul_f32 v[128:129], v[128:129], v[232:233]
v_pk_mul_f32 v[130:131], v[130:131], v[232:233]
v_pk_mul_f32 v[132:133], v[132:133], v[232:233]
v_pk_mul_f32 v[134:135], v[134:135], v[232:233]
v_exp_f32 v128, v128
v_exp_f32 v129, v129
v_exp_f32 v130, v130
v_exp_f32 v131, v131
v_exp_f32 v132, v132
v_exp_f32 v133, v133
v_exp_f32 v134, v134
v_exp_f32 v135, v135
v_pk_fma_f32 v[128:129], v[128:129], v[230:231], v[230:231]
v_pk_fma_f32 v[130:131], v[130:131], v[230:231], v[230:231]
v_pk_fma_f32 v[132:133], v[132:133], v[230:231], v[230:231]
v_pk_fma_f32 v[134:135], v[134:135], v[230:231], v[230:231]
v_rcp_f32 v128, v128
v_rcp_f32 v129, v129
v_rcp_f32 v130, v130
v_rcp_f32 v131, v131
v_rcp_f32 v132, v132
v_rcp_f32 v133, v133
v_rcp_f32 v134, v134
v_rcp_f32 v135, v135
v_pk_mul_f32 v[144:145], v[144:145], v[128:129]
v_pk_mul_f32 v[146:147], v[146:147], v[130:131]
v_pk_mul_f32 v[148:149], v[148:149], v[132:133]
v_pk_mul_f32 v[150:151], v[150:151], v[134:135]
v_cvt_pk_bf16_f32 v128, v144, v145
v_cvt_pk_bf16_f32 v129, v146, v147
v_cvt_pk_bf16_f32 v130, v148, v149
v_cvt_pk_bf16_f32 v131, v150, v151
buffer_store_dwordx4 v[128:131], v244, s[48:51], s8 offen offset:0 sc1
v_pk_mul_f32 v[136:137], v[136:137], v[232:233]
v_pk_mul_f32 v[138:139], v[138:139], v[232:233]
v_pk_mul_f32 v[140:141], v[140:141], v[232:233]
v_pk_mul_f32 v[142:143], v[142:143], v[232:233]
v_exp_f32 v136, v136
v_exp_f32 v137, v137
v_exp_f32 v138, v138
v_exp_f32 v139, v139
v_exp_f32 v140, v140
v_exp_f32 v141, v141
v_exp_f32 v142, v142
v_exp_f32 v143, v143
v_pk_fma_f32 v[136:137], v[136:137], v[230:231], v[230:231]
v_pk_fma_f32 v[138:139], v[138:139], v[230:231], v[230:231]
v_pk_fma_f32 v[140:141], v[140:141], v[230:231], v[230:231]
v_pk_fma_f32 v[142:143], v[142:143], v[230:231], v[230:231]
v_rcp_f32 v136, v136
v_rcp_f32 v137, v137
v_rcp_f32 v138, v138
v_rcp_f32 v139, v139
v_rcp_f32 v140, v140
v_rcp_f32 v141, v141
v_rcp_f32 v142, v142
v_rcp_f32 v143, v143
v_pk_mul_f32 v[152:153], v[152:153], v[136:137]
v_pk_mul_f32 v[154:155], v[154:155], v[138:139]
v_pk_mul_f32 v[156:157], v[156:157], v[140:141]
v_pk_mul_f32 v[158:159], v[158:159], v[142:143]
v_cvt_pk_bf16_f32 v136, v152, v153
v_cvt_pk_bf16_f32 v137, v154, v155
v_cvt_pk_bf16_f32 v138, v156, v157
v_cvt_pk_bf16_f32 v139, v158, v159
buffer_store_dwordx4 v[136:139], v244, s[48:51], s9 offen offset:0 sc1
v_bfe_u32 v176, v224, 2, 3
v_lshrrev_b32 v177, 5, v224
v_lshl_add_u32 v176, v177, 4, v176
v_bfe_u32 v177, v224, 3, 2
v_lshl_add_u32 v177, v176, 2, v177
v_and_b32 v178, 3, v224
v_lshlrev_b32 v178, 3, v178
v_lshl_add_u32 v234, v177, 5, v178
v_add_u32 v234, s96, v234
v_xor_b32 v235, 32, v234
v_xor_b32 v236, 64, v234
v_xor_b32 v237, 96, v234
v_add_u32 v238, 0x10000, v234
v_add_u32 v239, 0x10000, v235
v_add_u32 v232, 0x10000, v236
v_add_u32 v233, 0x10000, v237
s_setprio 0
ds_read_b32 v227, v245 offset:28672
s_waitcnt vmcnt(50)
ds_read_b64_tr_b16 v[192:193], v238 offset:8192
ds_read_b64_tr_b16 v[194:195], v238 offset:9216
ds_read_b64_tr_b16 v[196:197], v239 offset:8192
ds_read_b64_tr_b16 v[198:199], v239 offset:9216
ds_read_b64_tr_b16 v[200:201], v232 offset:8192
ds_read_b64_tr_b16 v[202:203], v232 offset:9216
ds_read_b64_tr_b16 v[204:205], v233 offset:8192
ds_read_b64_tr_b16 v[206:207], v233 offset:9216
s_waitcnt lgkmcnt(8)
s_setprio 3
v_cmp_gt_u32 vcc, 36, v227
s_setprio 0
s_nop 0
s_cbranch_vccnz .Lk_ls_65
.Lk_ld_66:
s_waitcnt vmcnt(46)
s_waitcnt lgkmcnt(6)
v_mfma_f32_16x16x32_bf16 v[128:131], v[192:195], v[0:3], 0
v_mfma_f32_16x16x32_bf16 v[132:135], v[192:195], v[4:7], 0
ds_read_b64_tr_b16 v[208:209], v238 offset:12288
v_mfma_f32_16x16x32_bf16 v[136:139], v[192:195], v[8:11], 0
ds_read_b64_tr_b16 v[210:211], v238 offset:13312
v_mfma_f32_16x16x32_bf16 v[140:143], v[192:195], v[12:15], 0
s_add_u32 m0, s96, 65536
s_add_u32 s62, s11, 393216
buffer_load_dwordx4 v242, s[36:39], s62 offen  lds
s_nop 14
s_waitcnt lgkmcnt(6)
v_mfma_f32_16x16x32_bf16 v[144:147], v[196:199], v[0:3], 0
v_mfma_f32_16x16x32_bf16 v[148:151], v[196:199], v[4:7], 0
ds_read_b64_tr_b16 v[212:213], v239 offset:12288
v_mfma_f32_16x16x32_bf16 v[152:155], v[196:199], v[8:11], 0
ds_read_b64_tr_b16 v[214:215], v239 offset:13312
v_mfma_f32_16x16x32_bf16 v[156:159], v[196:199], v[12:15], 0
s_add_u32 m0, s96, 66560
s_add_u32 s62, s11, 401408
buffer_load_dwordx4 v242, s[36:39], s62 offen  lds
s_nop 14
s_waitcnt lgkmcnt(6)
v_mfma_f32_16x16x32_bf16 v[160:163], v[200:203], v[0:3], 0
v_mfma_f32_16x16x32_bf16 v[164:167], v[200:203], v[4:7], 0
ds_read_b64_tr_b16 v[216:217], v232 offset:12288
v_mfma_f32_16x16x32_bf16 v[168:171], v[200:203], v[8:11], 0
ds_read_b64_tr_b16 v[218:219], v232 offset:13312
v_mfma_f32_16x16x32_bf16 v[172:175], v[200:203], v[12:15], 0
s_add_u32 m0, s96, 67584
s_add_u32 s62, s11, 409600
buffer_load_dwordx4 v242, s[36:39], s62 offen  lds
s_nop 14
s_waitcnt lgkmcnt(6)
v_mfma_f32_16x16x32_bf16 v[176:179], v[204:207], v[0:3], 0
v_mfma_f32_16x16x32_bf16 v[180:183], v[204:207], v[4:7], 0
ds_read_b64_tr_b16 v[220:221], v233 offset:12288
v_mfma_f32_16x16x32_bf16 v[184:187], v[204:207], v[8:11], 0
ds_read_b64_tr_b16 v[222:223], v233 offset:13312
v_mfma_f32_16x16x32_bf16 v[188:191], v[204:207], v[12:15], 0
s_add_u32 m0, s96, 68608
s_add_u32 s62, s11, 417792
buffer_load_dwordx4 v242, s[36:39], s62 offen  lds
s_waitcnt vmcnt(46)
s_nop 13
s_waitcnt lgkmcnt(6)
v_mfma_f32_16x16x32_bf16 v[128:131], v[208:211], v[16:19], v[128:131]
v_mfma_f32_16x16x32_bf16 v[132:135], v[208:211], v[20:23], v[132:135]
v_mfma_f32_16x16x32_bf16 v[136:139], v[208:211], v[24:27], v[136:139]
v_mfma_f32_16x16x32_bf16 v[140:143], v[208:211], v[28:31], v[140:143]
s_waitcnt vmcnt(30)
ds_read_b64_tr_b16 v[192:193], v234 offset:0
ds_read_b64_tr_b16 v[194:195], v234 offset:1024
s_add_u32 m0, s96, 69632
)ASM" R"ASM(
s_add_u32 s62, s11, 425984
buffer_load_dwordx4 v242, s[36:39], s62 offen  lds
s_nop 13
s_waitcnt lgkmcnt(6)
v_mfma_f32_16x16x32_bf16 v[144:147], v[212:215], v[16:19], v[144:147]
v_mfma_f32_16x16x32_bf16 v[148:151], v[212:215], v[20:23], v[148:151]
ds_read_b64_tr_b16 v[196:197], v235 offset:0
v_mfma_f32_16x16x32_bf16 v[152:155], v[212:215], v[24:27], v[152:155]
ds_read_b64_tr_b16 v[198:199], v235 offset:1024
v_mfma_f32_16x16x32_bf16 v[156:159], v[212:215], v[28:31], v[156:159]
s_add_u32 m0, s96, 70656
s_add_u32 s62, s11, 434176
buffer_load_dwordx4 v242, s[36:39], s62 offen  lds
s_waitcnt vmcnt(6)
s_mov_b64 exec, 1
buffer_store_dwordx2 v[248:249], off, s[48:51], s16 offset:8 sc1
s_mov_b64 exec, -1
s_nop 10
s_waitcnt lgkmcnt(6)
v_mfma_f32_16x16x32_bf16 v[160:163], v[216:219], v[16:19], v[160:163]
v_mfma_f32_16x16x32_bf16 v[164:167], v[216:219], v[20:23], v[164:167]
ds_read_b64_tr_b16 v[200:201], v236 offset:0
v_mfma_f32_16x16x32_bf16 v[168:171], v[216:219], v[24:27], v[168:171]
ds_read_b64_tr_b16 v[202:203], v236 offset:1024
v_mfma_f32_16x16x32_bf16 v[172:175], v[216:219], v[28:31], v[172:175]
s_add_u32 m0, s96, 71680
s_add_u32 s62, s11, 442368
buffer_load_dwordx4 v242, s[36:39], s62 offen  lds
s_nop 14
s_waitcnt lgkmcnt(6)
v_mfma_f32_16x16x32_bf16 v[176:179], v[220:223], v[16:19], v[176:179]
v_mfma_f32_16x16x32_bf16 v[180:183], v[220:223], v[20:23], v[180:183]
ds_read_b64_tr_b16 v[204:205], v237 offset:0
v_mfma_f32_16x16x32_bf16 v[184:187], v[220:223], v[24:27], v[184:187]
ds_read_b64_tr_b16 v[206:207], v237 offset:1024
v_mfma_f32_16x16x32_bf16 v[188:191], v[220:223], v[28:31], v[188:191]
s_add_u32 m0, s96, 72704
s_add_u32 s62, s11, 450560
buffer_load_dwordx4 v242, s[36:39], s62 offen  lds
s_nop 14
s_waitcnt lgkmcnt(6)
v_mfma_f32_16x16x32_bf16 v[128:131], v[192:195], v[32:35], v[128:131]
v_mfma_f32_16x16x32_bf16 v[132:135], v[192:195], v[36:39], v[132:135]
ds_read_b64_tr_b16 v[208:209], v234 offset:4096
v_mfma_f32_16x16x32_bf16 v[136:139], v[192:195], v[40:43], v[136:139]
ds_read_b64_tr_b16 v[210:211], v234 offset:5120
v_mfma_f32_16x16x32_bf16 v[140:143], v[192:195], v[44:47], v[140:143]
s_add_u32 m0, s96, 73728
s_add_u32 s62, s11, 65536
buffer_load_dwordx4 v242, s[36:39], s62 offen  lds
s_add_u32 m0, s96, 74752
s_add_u32 s62, s11, 73728
buffer_load_dwordx4 v242, s[36:39], s62 offen  lds
s_nop 11
s_waitcnt lgkmcnt(6)
v_mfma_f32_16x16x32_bf16 v[144:147], v[196:199], v[32:35], v[144:147]
v_mfma_f32_16x16x32_bf16 v[148:151], v[196:199], v[36:39], v[148:151]
ds_read_b64_tr_b16 v[212:213], v235 offset:4096
v_mfma_f32_16x16x32_bf16 v[152:155], v[196:199], v[40:43], v[152:155]
ds_read_b64_tr_b16 v[214:215], v235 offset:5120
v_mfma_f32_16x16x32_bf16 v[156:159], v[196:199], v[44:47], v[156:159]
s_add_u32 m0, s96, 75776
s_add_u32 s62, s11, 81920
buffer_load_dwordx4 v242, s[36:39], s62 offen  lds
s_add_u32 m0, s96, 76800
s_add_u32 s62, s11, 90112
buffer_load_dwordx4 v242, s[36:39], s62 offen  lds
s_nop 11
s_waitcnt lgkmcnt(6)
v_mfma_f32_16x16x32_bf16 v[160:163], v[200:203], v[32:35], v[160:163]
v_mfma_f32_16x16x32_bf16 v[164:167], v[200:203], v[36:39], v[164:167]
ds_read_b64_tr_b16 v[216:217], v236 offset:4096
v_mfma_f32_16x16x32_bf16 v[168:171], v[200:203], v[40:43], v[168:171]
ds_read_b64_tr_b16 v[218:219], v236 offset:5120
v_mfma_f32_16x16x32_bf16 v[172:175], v[200:203], v[44:47], v[172:175]
s_add_u32 m0, s96, 77824
s_add_u32 s62, s11, 98304
buffer_load_dwordx4 v242, s[36:39], s62 offen  lds
s_add_u32 m0, s96, 78848
s_add_u32 s62, s11, 106496
buffer_load_dwordx4 v242, s[36:39], s62 offen  lds
s_nop 11
s_waitcnt lgkmcnt(6)
v_mfma_f32_16x16x32_bf16 v[176:179], v[204:207], v[32:35], v[176:179]
v_mfma_f32_16x16x32_bf16 v[180:183], v[204:207], v[36:39], v[180:183]
ds_read_b64_tr_b16 v[220:221], v237 offset:4096
v_mfma_f32_16x16x32_bf16 v[184:187], v[204:207], v[40:43], v[184:187]
ds_read_b64_tr_b16 v[222:223], v237 offset:5120
v_mfma_f32_16x16x32_bf16 v[188:191], v[204:207], v[44:47], v[188:191]
s_add_u32 m0, s96, 79872
s_add_u32 s62, s11, 114688
buffer_load_dwordx4 v242, s[36:39], s62 offen  lds
s_add_u32 m0, s96, 80896
s_add_u32 s62, s11, 122880
buffer_load_dwordx4 v242, s[36:39], s62 offen  lds
s_nop 11
s_waitcnt lgkmcnt(6)
v_mfma_f32_16x16x32_bf16 v[128:131], v[208:211], v[48:51], v[128:131]
v_mfma_f32_16x16x32_bf16 v[132:135], v[208:211], v[52:55], v[132:135]
ds_read_b64_tr_b16 v[192:193], v234 offset:8192
v_mfma_f32_16x16x32_bf16 v[136:139], v[208:211], v[56:59], v[136:139]
ds_read_b64_tr_b16 v[194:195], v234 offset:9216
v_mfma_f32_16x16x32_bf16 v[140:143], v[208:211], v[60:63], v[140:143]
s_add_u32 m0, s96, 0
s_add_u32 s62, s11, 196608
buffer_load_dwordx4 v242, s[36:39], s62 offen  lds
s_add_u32 m0, s96, 1024
s_add_u32 s62, s11, 204800
buffer_load_dwordx4 v242, s[36:39], s62 offen  lds
s_nop 11
s_waitcnt lgkmcnt(6)
v_mfma_f32_16x16x32_bf16 v[144:147], v[212:215], v[48:51], v[144:147]
v_mfma_f32_16x16x32_bf16 v[148:151], v[212:215], v[52:55], v[148:151]
ds_read_b64_tr_b16 v[196:197], v235 offset:8192
v_mfma_f32_16x16x32_bf16 v[152:155], v[212:215], v[56:59], v[152:155]
ds_read_b64_tr_b16 v[198:199], v235 offset:9216
v_mfma_f32_16x16x32_bf16 v[156:159], v[212:215], v[60:63], v[156:159]
s_add_u32 m0, s96, 2048
s_add_u32 s62, s11, 212992
buffer_load_dwordx4 v242, s[36:39], s62 offen  lds
s_add_u32 m0, s96, 3072
s_add_u32 s62, s11, 221184
buffer_load_dwordx4 v242, s[36:39], s62 offen  lds
s_nop 11
s_waitcnt lgkmcnt(6)
v_mfma_f32_16x16x32_bf16 v[160:163], v[216:219], v[48:51], v[160:163]
v_mfma_f32_16x16x32_bf16 v[164:167], v[216:219], v[52:55], v[164:167]
ds_read_b64_tr_b16 v[200:201], v236 offset:8192
v_mfma_f32_16x16x32_bf16 v[168:171], v[216:219], v[56:59], v[168:171]
ds_read_b64_tr_b16 v[202:203], v236 offset:9216
v_mfma_f32_16x16x32_bf16 v[172:175], v[216:219], v[60:63], v[172:175]
s_nop 15
s_nop 1
s_waitcnt lgkmcnt(6)
v_mfma_f32_16x16x32_bf16 v[176:179], v[220:223], v[48:51], v[176:179]
v_mfma_f32_16x16x32_bf16 v[180:183], v[220:223], v[52:55], v[180:183]
ds_read_b64_tr_b16 v[204:205], v237 offset:8192
v_mfma_f32_16x16x32_bf16 v[184:187], v[220:223], v[56:59], v[184:187]
ds_read_b64_tr_b16 v[206:207], v237 offset:9216
v_mfma_f32_16x16x32_bf16 v[188:191], v[220:223], v[60:63], v[188:191]
s_add_u32 m0, s96, 4096
s_add_u32 s62, s11, 229376
buffer_load_dwordx4 v242, s[36:39], s62 offen  lds
s_add_u32 m0, s96, 5120
s_add_u32 s62, s11, 237568
buffer_load_dwordx4 v242, s[36:39], s62 offen  lds
s_nop 11
s_waitcnt lgkmcnt(6)
v_mfma_f32_16x16x32_bf16 v[128:131], v[192:195], v[64:67], v[128:131]
v_mfma_f32_16x16x32_bf16 v[132:135], v[192:195], v[68:71], v[132:135]
ds_read_b64_tr_b16 v[208:209], v234 offset:12288
v_mfma_f32_16x16x32_bf16 v[136:139], v[192:195], v[72:75], v[136:139]
ds_read_b64_tr_b16 v[210:211], v234 offset:13312
v_mfma_f32_16x16x32_bf16 v[140:143], v[192:195], v[76:79], v[140:143]
s_add_u32 m0, s96, 6144
s_add_u32 s62, s11, 245760
buffer_load_dwordx4 v242, s[36:39], s62 offen  lds
s_add_u32 m0, s96, 7168
s_add_u32 s62, s11, 253952
buffer_load_dwordx4 v242, s[36:39], s62 offen  lds
s_nop 11
s_waitcnt lgkmcnt(6)
v_mfma_f32_16x16x32_bf16 v[144:147], v[196:199], v[64:67], v[144:147]
v_mfma_f32_16x16x32_bf16 v[148:151], v[196:199], v[68:71], v[148:151]
ds_read_b64_tr_b16 v[212:213], v235 offset:12288
v_mfma_f32_16x16x32_bf16 v[152:155], v[196:199], v[72:75], v[152:155]
ds_read_b64_tr_b16 v[214:215], v235 offset:13312
v_mfma_f32_16x16x32_bf16 v[156:159], v[196:199], v[76:79], v[156:159]
s_nop 15
s_nop 1
s_waitcnt lgkmcnt(6)
v_mfma_f32_16x16x32_bf16 v[160:163], v[200:203], v[64:67], v[160:163]
v_mfma_f32_16x16x32_bf16 v[164:167], v[200:203], v[68:71], v[164:167]
ds_read_b64_tr_b16 v[216:217], v236 offset:12288
v_mfma_f32_16x16x32_bf16 v[168:171], v[200:203], v[72:75], v[168:171]
ds_read_b64_tr_b16 v[218:219], v236 offset:13312
v_mfma_f32_16x16x32_bf16 v[172:175], v[200:203], v[76:79], v[172:175]
s_nop 15
s_nop 1
s_waitcnt lgkmcnt(6)
v_mfma_f32_16x16x32_bf16 v[176:179], v[204:207], v[64:67], v[176:179]
v_mfma_f32_16x16x32_bf16 v[180:183], v[204:207], v[68:71], v[180:183]
ds_read_b64_tr_b16 v[220:221], v237 offset:12288
v_mfma_f32_16x16x32_bf16 v[184:187], v[204:207], v[72:75], v[184:187]
ds_read_b64_tr_b16 v[222:223], v237 offset:13312
v_mfma_f32_16x16x32_bf16 v[188:191], v[204:207], v[76:79], v[188:191]
s_add_u32 m0, s96, 8192
s_add_u32 s62, s11, 327680
buffer_load_dwordx4 v242, s[36:39], s62 offen  lds
s_add_u32 m0, s96, 9216
s_add_u32 s62, s11, 335872
buffer_load_dwordx4 v242, s[36:39], s62 offen  lds
buffer_load_dwordx2 v[250:251], v225, s[48:51], 0 offen offset:8 sc1
s_nop 10
s_waitcnt lgkmcnt(6)
v_mfma_f32_16x16x32_bf16 v[128:131], v[208:211], v[80:83], v[128:131]
v_mfma_f32_16x16x32_bf16 v[132:135], v[208:211], v[84:87], v[132:135]
v_mfma_f32_16x16x32_bf16 v[136:139], v[208:211], v[88:91], v[136:139]
v_mfma_f32_16x16x32_bf16 v[140:143], v[208:211], v[92:95], v[140:143]
s_waitcnt vmcnt(19)
)ASM" R"ASM(
ds_read_b64_tr_b16 v[192:193], v238 offset:0
ds_read_b64_tr_b16 v[194:195], v238 offset:1024
s_add_u32 m0, s96, 10240
s_add_u32 s62, s11, 344064
buffer_load_dwordx4 v242, s[36:39], s62 offen  lds
s_add_u32 m0, s96, 11264
s_add_u32 s62, s11, 352256
buffer_load_dwordx4 v242, s[36:39], s62 offen  lds
s_nop 10
s_waitcnt lgkmcnt(6)
v_mfma_f32_16x16x32_bf16 v[144:147], v[212:215], v[80:83], v[144:147]
v_mfma_f32_16x16x32_bf16 v[148:151], v[212:215], v[84:87], v[148:151]
ds_read_b64_tr_b16 v[196:197], v239 offset:0
v_mfma_f32_16x16x32_bf16 v[152:155], v[212:215], v[88:91], v[152:155]
ds_read_b64_tr_b16 v[198:199], v239 offset:1024
v_mfma_f32_16x16x32_bf16 v[156:159], v[212:215], v[92:95], v[156:159]
s_nop 15
s_nop 1
s_waitcnt lgkmcnt(6)
v_mfma_f32_16x16x32_bf16 v[160:163], v[216:219], v[80:83], v[160:163]
v_mfma_f32_16x16x32_bf16 v[164:167], v[216:219], v[84:87], v[164:167]
ds_read_b64_tr_b16 v[200:201], v232 offset:0
v_mfma_f32_16x16x32_bf16 v[168:171], v[216:219], v[88:91], v[168:171]
ds_read_b64_tr_b16 v[202:203], v232 offset:1024
v_mfma_f32_16x16x32_bf16 v[172:175], v[216:219], v[92:95], v[172:175]
s_nop 15
s_nop 1
s_waitcnt lgkmcnt(6)
v_mfma_f32_16x16x32_bf16 v[176:179], v[220:223], v[80:83], v[176:179]
v_mfma_f32_16x16x32_bf16 v[180:183], v[220:223], v[84:87], v[180:183]
ds_read_b64_tr_b16 v[204:205], v233 offset:0
v_mfma_f32_16x16x32_bf16 v[184:187], v[220:223], v[88:91], v[184:187]
ds_read_b64_tr_b16 v[206:207], v233 offset:1024
v_mfma_f32_16x16x32_bf16 v[188:191], v[220:223], v[92:95], v[188:191]
s_add_u32 m0, s96, 12288
s_add_u32 s62, s11, 360448
buffer_load_dwordx4 v242, s[36:39], s62 offen  lds
s_add_u32 m0, s96, 13312
s_add_u32 s62, s11, 368640
buffer_load_dwordx4 v242, s[36:39], s62 offen  lds
buffer_load_dwordx4 v[228:231], v253, s[20:23], s59 offen
s_waitcnt vmcnt(5)
s_setprio 3
v_cmp_ne_u64 vcc, v[248:249], v[250:251]
s_setprio 0
s_nop 0
s_cbranch_vccnz .Lk_ps_67
.Lk_pd_68:
s_nop 3
s_waitcnt lgkmcnt(6)
v_mfma_f32_16x16x32_bf16 v[128:131], v[192:195], v[96:99], v[128:131]
v_mfma_f32_16x16x32_bf16 v[132:135], v[192:195], v[100:103], v[132:135]
ds_read_b64_tr_b16 v[208:209], v238 offset:4096
v_mfma_f32_16x16x32_bf16 v[136:139], v[192:195], v[104:107], v[136:139]
ds_read_b64_tr_b16 v[210:211], v238 offset:5120
v_mfma_f32_16x16x32_bf16 v[140:143], v[192:195], v[108:111], v[140:143]
s_add_u32 m0, s96, 14336
s_add_u32 s62, s11, 376832
buffer_load_dwordx4 v242, s[36:39], s62 offen  lds
s_add_u32 m0, s96, 15360
s_add_u32 s62, s11, 385024
buffer_load_dwordx4 v242, s[36:39], s62 offen  lds
s_add_u32 s62, s13, 4096
buffer_load_dwordx4 v[0:3], v243, s[48:51], s62 offen offset:0 sc1
buffer_load_dwordx4 v[4:7], v243, s[48:51], s62 offen offset:256 sc1
s_nop 8
s_waitcnt lgkmcnt(6)
v_mfma_f32_16x16x32_bf16 v[144:147], v[196:199], v[96:99], v[144:147]
v_mfma_f32_16x16x32_bf16 v[148:151], v[196:199], v[100:103], v[148:151]
ds_read_b64_tr_b16 v[212:213], v239 offset:4096
v_mfma_f32_16x16x32_bf16 v[152:155], v[196:199], v[104:107], v[152:155]
ds_read_b64_tr_b16 v[214:215], v239 offset:5120
v_mfma_f32_16x16x32_bf16 v[156:159], v[196:199], v[108:111], v[156:159]
s_add_u32 s62, s13, 151552
buffer_load_dwordx4 v[8:11], v243, s[48:51], s62 offen offset:0 sc1
buffer_load_dwordx4 v[12:15], v243, s[48:51], s62 offen offset:256 sc1
s_nop 14
s_waitcnt lgkmcnt(6)
v_mfma_f32_16x16x32_bf16 v[160:163], v[200:203], v[96:99], v[160:163]
v_mfma_f32_16x16x32_bf16 v[164:167], v[200:203], v[100:103], v[164:167]
ds_read_b64_tr_b16 v[216:217], v232 offset:4096
v_mfma_f32_16x16x32_bf16 v[168:171], v[200:203], v[104:107], v[168:171]
ds_read_b64_tr_b16 v[218:219], v232 offset:5120
v_mfma_f32_16x16x32_bf16 v[172:175], v[200:203], v[108:111], v[172:175]
s_add_u32 s62, s13, 4096
buffer_load_dwordx4 v[16:19], v243, s[48:51], s62 offen offset:2048 sc1
buffer_load_dwordx4 v[20:23], v243, s[48:51], s62 offen offset:2304 sc1
s_nop 14
s_waitcnt lgkmcnt(6)
v_mfma_f32_16x16x32_bf16 v[176:179], v[204:207], v[96:99], v[176:179]
v_mfma_f32_16x16x32_bf16 v[180:183], v[204:207], v[100:103], v[180:183]
ds_read_b64_tr_b16 v[220:221], v233 offset:4096
v_mfma_f32_16x16x32_bf16 v[184:187], v[204:207], v[104:107], v[184:187]
ds_read_b64_tr_b16 v[222:223], v233 offset:5120
v_mfma_f32_16x16x32_bf16 v[188:191], v[204:207], v[108:111], v[188:191]
s_add_u32 m0, s96, 65536
s_add_u32 s62, s12, 0
buffer_load_dwordx4 v242, s[40:43], s62 offen  lds
s_add_u32 m0, s96, 66560
s_add_u32 s62, s12, 8192
buffer_load_dwordx4 v242, s[40:43], s62 offen  lds
s_add_u32 s62, s13, 151552
buffer_load_dwordx4 v[24:27], v243, s[48:51], s62 offen offset:2048 sc1
buffer_load_dwordx4 v[28:31], v243, s[48:51], s62 offen offset:2304 sc1
s_nop 8
s_waitcnt lgkmcnt(6)
v_mfma_f32_16x16x32_bf16 v[128:131], v[208:211], v[112:115], v[128:131]
v_mfma_f32_16x16x32_bf16 v[132:135], v[208:211], v[116:119], v[132:135]
ds_read_b64_tr_b16 v[192:193], v238 offset:8192
v_mfma_f32_16x16x32_bf16 v[136:139], v[208:211], v[120:123], v[136:139]
ds_read_b64_tr_b16 v[194:195], v238 offset:9216
v_mfma_f32_16x16x32_bf16 v[140:143], v[208:211], v[124:127], v[140:143]
s_add_u32 m0, s96, 67584
s_add_u32 s62, s12, 16384
buffer_load_dwordx4 v242, s[40:43], s62 offen  lds
s_add_u32 m0, s96, 68608
s_add_u32 s62, s12, 24576
buffer_load_dwordx4 v242, s[40:43], s62 offen  lds
s_add_u32 s62, s13, 12288
buffer_load_dwordx4 v[32:35], v243, s[48:51], s62 offen offset:0 sc1
buffer_load_dwordx4 v[36:39], v243, s[48:51], s62 offen offset:256 sc1
s_nop 8
s_waitcnt lgkmcnt(6)
v_mfma_f32_16x16x32_bf16 v[144:147], v[212:215], v[112:115], v[144:147]
v_mfma_f32_16x16x32_bf16 v[148:151], v[212:215], v[116:119], v[148:151]
ds_read_b64_tr_b16 v[196:197], v239 offset:8192
v_mfma_f32_16x16x32_bf16 v[152:155], v[212:215], v[120:123], v[152:155]
ds_read_b64_tr_b16 v[198:199], v239 offset:9216
v_mfma_f32_16x16x32_bf16 v[156:159], v[212:215], v[124:127], v[156:159]
s_add_u32 s62, s13, 159744
buffer_load_dwordx4 v[40:43], v243, s[48:51], s62 offen offset:0 sc1
buffer_load_dwordx4 v[44:47], v243, s[48:51], s62 offen offset:256 sc1
s_nop 14
s_waitcnt lgkmcnt(6)
v_mfma_f32_16x16x32_bf16 v[160:163], v[216:219], v[112:115], v[160:163]
v_mfma_f32_16x16x32_bf16 v[164:167], v[216:219], v[116:119], v[164:167]
ds_read_b64_tr_b16 v[200:201], v232 offset:8192
v_mfma_f32_16x16x32_bf16 v[168:171], v[216:219], v[120:123], v[168:171]
ds_read_b64_tr_b16 v[202:203], v232 offset:9216
v_mfma_f32_16x16x32_bf16 v[172:175], v[216:219], v[124:127], v[172:175]
s_add_u32 s62, s13, 12288
buffer_load_dwordx4 v[48:51], v243, s[48:51], s62 offen offset:2048 sc1
buffer_load_dwordx4 v[52:55], v243, s[48:51], s62 offen offset:2304 sc1
s_nop 14
s_waitcnt lgkmcnt(6)
v_mfma_f32_16x16x32_bf16 v[176:179], v[220:223], v[112:115], v[176:179]
v_mfma_f32_16x16x32_bf16 v[180:183], v[220:223], v[116:119], v[180:183]
ds_read_b64_tr_b16 v[204:205], v233 offset:8192
v_mfma_f32_16x16x32_bf16 v[184:187], v[220:223], v[120:123], v[184:187]
ds_read_b64_tr_b16 v[206:207], v233 offset:9216
v_mfma_f32_16x16x32_bf16 v[188:191], v[220:223], v[124:127], v[188:191]
s_add_u32 m0, s96, 69632
s_add_u32 s62, s12, 32768
buffer_load_dwordx4 v242, s[40:43], s62 offen  lds
s_add_u32 m0, s96, 70656
s_add_u32 s62, s12, 40960
buffer_load_dwordx4 v242, s[40:43], s62 offen  lds
s_add_u32 s62, s13, 159744
buffer_load_dwordx4 v[56:59], v243, s[48:51], s62 offen offset:2048 sc1
buffer_load_dwordx4 v[60:63], v243, s[48:51], s62 offen offset:2304 sc1
s_waitcnt vmcnt(18)
s_nop 7
s_waitcnt lgkmcnt(6)
v_mfma_f32_16x16x32_bf16 v[128:131], v[192:195], v[0:3], v[128:131]
v_mfma_f32_16x16x32_bf16 v[132:135], v[192:195], v[4:7], v[132:135]
ds_read_b64_tr_b16 v[208:209], v238 offset:12288
v_mfma_f32_16x16x32_bf16 v[136:139], v[192:195], v[8:11], v[136:139]
ds_read_b64_tr_b16 v[210:211], v238 offset:13312
v_mfma_f32_16x16x32_bf16 v[140:143], v[192:195], v[12:15], v[140:143]
s_add_u32 m0, s96, 71680
s_add_u32 s62, s12, 49152
buffer_load_dwordx4 v242, s[40:43], s62 offen  lds
s_add_u32 m0, s96, 72704
s_add_u32 s62, s12, 57344
buffer_load_dwordx4 v242, s[40:43], s62 offen  lds
s_add_u32 s62, s13, 20480
buffer_load_dwordx4 v[64:67], v243, s[48:51], s62 offen offset:0 sc1
buffer_load_dwordx4 v[68:71], v243, s[48:51], s62 offen offset:256 sc1
s_nop 8
s_waitcnt lgkmcnt(6)
v_mfma_f32_16x16x32_bf16 v[144:147], v[196:199], v[0:3], v[144:147]
v_mfma_f32_16x16x32_bf16 v[148:151], v[196:199], v[4:7], v[148:151]
ds_read_b64_tr_b16 v[212:213], v239 offset:12288
v_mfma_f32_16x16x32_bf16 v[152:155], v[196:199], v[8:11], v[152:155]
ds_read_b64_tr_b16 v[214:215], v239 offset:13312
v_mfma_f32_16x16x32_bf16 v[156:159], v[196:199], v[12:15], v[156:159]
s_add_u32 s62, s13, 167936
buffer_load_dwordx4 v[72:75], v243, s[48:51], s62 offen offset:0 sc1
buffer_load_dwordx4 v[76:79], v243, s[48:51], s62 offen offset:256 sc1
s_nop 14
s_waitcnt lgkmcnt(6)
v_mfma_f32_16x16x32_bf16 v[160:163], v[200:203], v[0:3], v[160:163]
v_mfma_f32_16x16x32_bf16 v[164:167], v[200:203], v[4:7], v[164:167]
ds_read_b64_tr_b16 v[216:217], v232 offset:12288
v_mfma_f32_16x16x32_bf16 v[168:171], v[200:203], v[8:11], v[168:171]
ds_read_b64_tr_b16 v[218:219], v232 offset:13312
v_mfma_f32_16x16x32_bf16 v[172:175], v[200:203], v[12:15], v[172:175]
s_add_u32 s62, s13, 20480
buffer_load_dwordx4 v[80:83], v243, s[48:51], s62 offen offset:2048 sc1
buffer_load_dwordx4 v[84:87], v243, s[48:51], s62 offen offset:2304 sc1
)ASM" R"ASM(
s_nop 14
s_waitcnt lgkmcnt(6)
v_mfma_f32_16x16x32_bf16 v[176:179], v[204:207], v[0:3], v[176:179]
v_mfma_f32_16x16x32_bf16 v[180:183], v[204:207], v[4:7], v[180:183]
ds_read_b64_tr_b16 v[220:221], v233 offset:12288
v_mfma_f32_16x16x32_bf16 v[184:187], v[204:207], v[8:11], v[184:187]
ds_read_b64_tr_b16 v[222:223], v233 offset:13312
v_mfma_f32_16x16x32_bf16 v[188:191], v[204:207], v[12:15], v[188:191]
s_add_u32 m0, s96, 73728
s_add_u32 s62, s12, 65536
buffer_load_dwordx4 v242, s[40:43], s62 offen  lds
s_add_u32 m0, s96, 74752
s_add_u32 s62, s12, 73728
buffer_load_dwordx4 v242, s[40:43], s62 offen  lds
s_add_u32 s62, s13, 167936
buffer_load_dwordx4 v[88:91], v243, s[48:51], s62 offen offset:2048 sc1
buffer_load_dwordx4 v[92:95], v243, s[48:51], s62 offen offset:2304 sc1
s_setprio 3
s_setprio 3
s_lshl_b32 s62, s53, 8
v_add_u32 v246, s62, v245
ds_read_b32 v227, v246 offset:30720
s_waitcnt lgkmcnt(0)
s_setprio 3
v_cmp_gt_u32 vcc, 1, v227
s_setprio 0
s_nop 0
s_cbranch_vccnz .Lk_ls_69
.Lk_ld_70:
s_waitcnt vmcnt(24)
v_mfma_f32_16x16x32_bf16 v[128:131], v[208:211], v[16:19], v[128:131]
v_mfma_f32_16x16x32_bf16 v[132:135], v[208:211], v[20:23], v[132:135]
ds_read_b64_tr_b16 v[192:193], v234 offset:0
v_mfma_f32_16x16x32_bf16 v[136:139], v[208:211], v[24:27], v[136:139]
ds_read_b64_tr_b16 v[194:195], v234 offset:1024
v_mfma_f32_16x16x32_bf16 v[140:143], v[208:211], v[28:31], v[140:143]
s_add_u32 m0, s96, 75776
s_add_u32 s62, s12, 81920
buffer_load_dwordx4 v242, s[40:43], s62 offen  lds
s_add_u32 m0, s96, 76800
s_add_u32 s62, s12, 90112
buffer_load_dwordx4 v242, s[40:43], s62 offen  lds
v_mfma_f32_16x16x32_bf16 v[144:147], v[212:215], v[16:19], v[144:147]
v_mfma_f32_16x16x32_bf16 v[148:151], v[212:215], v[20:23], v[148:151]
ds_read_b64_tr_b16 v[196:197], v235 offset:0
v_mfma_f32_16x16x32_bf16 v[152:155], v[212:215], v[24:27], v[152:155]
ds_read_b64_tr_b16 v[198:199], v235 offset:1024
v_mfma_f32_16x16x32_bf16 v[156:159], v[212:215], v[28:31], v[156:159]
v_mfma_f32_16x16x32_bf16 v[160:163], v[216:219], v[16:19], v[160:163]
v_mfma_f32_16x16x32_bf16 v[164:167], v[216:219], v[20:23], v[164:167]
ds_read_b64_tr_b16 v[200:201], v236 offset:0
v_mfma_f32_16x16x32_bf16 v[168:171], v[216:219], v[24:27], v[168:171]
ds_read_b64_tr_b16 v[202:203], v236 offset:1024
v_mfma_f32_16x16x32_bf16 v[172:175], v[216:219], v[28:31], v[172:175]
v_mfma_f32_16x16x32_bf16 v[176:179], v[220:223], v[16:19], v[176:179]
v_mfma_f32_16x16x32_bf16 v[180:183], v[220:223], v[20:23], v[180:183]
ds_read_b64_tr_b16 v[204:205], v237 offset:0
v_mfma_f32_16x16x32_bf16 v[184:187], v[220:223], v[24:27], v[184:187]
ds_read_b64_tr_b16 v[206:207], v237 offset:1024
v_mfma_f32_16x16x32_bf16 v[188:191], v[220:223], v[28:31], v[188:191]
s_add_u32 m0, s96, 77824
s_add_u32 s62, s12, 98304
buffer_load_dwordx4 v242, s[40:43], s62 offen  lds
s_add_u32 m0, s96, 78848
s_add_u32 s62, s12, 106496
buffer_load_dwordx4 v242, s[40:43], s62 offen  lds
s_waitcnt vmcnt(22)
s_waitcnt lgkmcnt(6)
v_mfma_f32_16x16x32_bf16 v[128:131], v[192:195], v[32:35], v[128:131]
v_mfma_f32_16x16x32_bf16 v[132:135], v[192:195], v[36:39], v[132:135]
ds_read_b64_tr_b16 v[208:209], v234 offset:4096
v_mfma_f32_16x16x32_bf16 v[136:139], v[192:195], v[40:43], v[136:139]
ds_read_b64_tr_b16 v[210:211], v234 offset:5120
v_mfma_f32_16x16x32_bf16 v[140:143], v[192:195], v[44:47], v[140:143]
s_add_u32 m0, s96, 79872
s_add_u32 s62, s12, 114688
buffer_load_dwordx4 v242, s[40:43], s62 offen  lds
s_add_u32 m0, s96, 80896
s_add_u32 s62, s12, 122880
buffer_load_dwordx4 v242, s[40:43], s62 offen  lds
s_waitcnt lgkmcnt(6)
v_mfma_f32_16x16x32_bf16 v[144:147], v[196:199], v[32:35], v[144:147]
v_mfma_f32_16x16x32_bf16 v[148:151], v[196:199], v[36:39], v[148:151]
ds_read_b64_tr_b16 v[212:213], v235 offset:4096
v_mfma_f32_16x16x32_bf16 v[152:155], v[196:199], v[40:43], v[152:155]
ds_read_b64_tr_b16 v[214:215], v235 offset:5120
v_mfma_f32_16x16x32_bf16 v[156:159], v[196:199], v[44:47], v[156:159]
s_waitcnt lgkmcnt(6)
v_mfma_f32_16x16x32_bf16 v[160:163], v[200:203], v[32:35], v[160:163]
v_mfma_f32_16x16x32_bf16 v[164:167], v[200:203], v[36:39], v[164:167]
ds_read_b64_tr_b16 v[216:217], v236 offset:4096
v_mfma_f32_16x16x32_bf16 v[168:171], v[200:203], v[40:43], v[168:171]
ds_read_b64_tr_b16 v[218:219], v236 offset:5120
v_mfma_f32_16x16x32_bf16 v[172:175], v[200:203], v[44:47], v[172:175]
s_waitcnt lgkmcnt(6)
v_mfma_f32_16x16x32_bf16 v[176:179], v[204:207], v[32:35], v[176:179]
v_mfma_f32_16x16x32_bf16 v[180:183], v[204:207], v[36:39], v[180:183]
ds_read_b64_tr_b16 v[220:221], v237 offset:4096
v_mfma_f32_16x16x32_bf16 v[184:187], v[204:207], v[40:43], v[184:187]
ds_read_b64_tr_b16 v[222:223], v237 offset:5120
v_mfma_f32_16x16x32_bf16 v[188:191], v[204:207], v[44:47], v[188:191]
v_mov_b32 v250, v248
v_mov_b32 v251, v249
s_mov_b64 exec, 255
v_mov_b32 v250, 0
v_mov_b32 v251, 0
buffer_atomic_or_x2 v[250:251], v226, s[48:51], 0 offen offset:0 sc0 sc1
s_mov_b64 exec, -1
s_waitcnt vmcnt(18)
s_waitcnt lgkmcnt(6)
v_mfma_f32_16x16x32_bf16 v[128:131], v[208:211], v[48:51], v[128:131]
v_mfma_f32_16x16x32_bf16 v[132:135], v[208:211], v[52:55], v[132:135]
ds_read_b64_tr_b16 v[192:193], v234 offset:8192
v_mfma_f32_16x16x32_bf16 v[136:139], v[208:211], v[56:59], v[136:139]
ds_read_b64_tr_b16 v[194:195], v234 offset:9216
v_mfma_f32_16x16x32_bf16 v[140:143], v[208:211], v[60:63], v[140:143]
s_waitcnt lgkmcnt(6)
v_mfma_f32_16x16x32_bf16 v[144:147], v[212:215], v[48:51], v[144:147]
v_mfma_f32_16x16x32_bf16 v[148:151], v[212:215], v[52:55], v[148:151]
ds_read_b64_tr_b16 v[196:197], v235 offset:8192
v_mfma_f32_16x16x32_bf16 v[152:155], v[212:215], v[56:59], v[152:155]
ds_read_b64_tr_b16 v[198:199], v235 offset:9216
v_mfma_f32_16x16x32_bf16 v[156:159], v[212:215], v[60:63], v[156:159]
s_waitcnt lgkmcnt(6)
v_mfma_f32_16x16x32_bf16 v[160:163], v[216:219], v[48:51], v[160:163]
v_mfma_f32_16x16x32_bf16 v[164:167], v[216:219], v[52:55], v[164:167]
ds_read_b64_tr_b16 v[200:201], v236 offset:8192
v_mfma_f32_16x16x32_bf16 v[168:171], v[216:219], v[56:59], v[168:171]
ds_read_b64_tr_b16 v[202:203], v236 offset:9216
v_mfma_f32_16x16x32_bf16 v[172:175], v[216:219], v[60:63], v[172:175]
s_waitcnt lgkmcnt(6)
v_mfma_f32_16x16x32_bf16 v[176:179], v[220:223], v[48:51], v[176:179]
v_mfma_f32_16x16x32_bf16 v[180:183], v[220:223], v[52:55], v[180:183]
ds_read_b64_tr_b16 v[204:205], v237 offset:8192
v_mfma_f32_16x16x32_bf16 v[184:187], v[220:223], v[56:59], v[184:187]
ds_read_b64_tr_b16 v[206:207], v237 offset:9216
v_mfma_f32_16x16x32_bf16 v[188:191], v[220:223], v[60:63], v[188:191]
s_waitcnt vmcnt(12)
s_waitcnt lgkmcnt(6)
v_mfma_f32_16x16x32_bf16 v[128:131], v[192:195], v[64:67], v[128:131]
v_mfma_f32_16x16x32_bf16 v[132:135], v[192:195], v[68:71], v[132:135]
ds_read_b64_tr_b16 v[208:209], v234 offset:12288
v_mfma_f32_16x16x32_bf16 v[136:139], v[192:195], v[72:75], v[136:139]
ds_read_b64_tr_b16 v[210:211], v234 offset:13312
v_mfma_f32_16x16x32_bf16 v[140:143], v[192:195], v[76:79], v[140:143]
s_waitcnt lgkmcnt(6)
v_mfma_f32_16x16x32_bf16 v[144:147], v[196:199], v[64:67], v[144:147]
v_mfma_f32_16x16x32_bf16 v[148:151], v[196:199], v[68:71], v[148:151]
ds_read_b64_tr_b16 v[212:213], v235 offset:12288
v_mfma_f32_16x16x32_bf16 v[152:155], v[196:199], v[72:75], v[152:155]
ds_read_b64_tr_b16 v[214:215], v235 offset:13312
v_mfma_f32_16x16x32_bf16 v[156:159], v[196:199], v[76:79], v[156:159]
s_waitcnt lgkmcnt(6)
v_mfma_f32_16x16x32_bf16 v[160:163], v[200:203], v[64:67], v[160:163]
v_mfma_f32_16x16x32_bf16 v[164:167], v[200:203], v[68:71], v[164:167]
ds_read_b64_tr_b16 v[216:217], v236 offset:12288
v_mfma_f32_16x16x32_bf16 v[168:171], v[200:203], v[72:75], v[168:171]
ds_read_b64_tr_b16 v[218:219], v236 offset:13312
v_mfma_f32_16x16x32_bf16 v[172:175], v[200:203], v[76:79], v[172:175]
s_waitcnt lgkmcnt(6)
v_mfma_f32_16x16x32_bf16 v[176:179], v[204:207], v[64:67], v[176:179]
v_mfma_f32_16x16x32_bf16 v[180:183], v[204:207], v[68:71], v[180:183]
ds_read_b64_tr_b16 v[220:221], v237 offset:12288
v_mfma_f32_16x16x32_bf16 v[184:187], v[204:207], v[72:75], v[184:187]
ds_read_b64_tr_b16 v[222:223], v237 offset:13312
v_mfma_f32_16x16x32_bf16 v[188:191], v[204:207], v[76:79], v[188:191]
s_waitcnt vmcnt(6)
s_waitcnt lgkmcnt(6)
v_mfma_f32_16x16x32_bf16 v[128:131], v[208:211], v[80:83], v[128:131]
v_mfma_f32_16x16x32_bf16 v[132:135], v[208:211], v[84:87], v[132:135]
ds_read_b64_tr_b16 v[192:193], v238 offset:0
v_mfma_f32_16x16x32_bf16 v[136:139], v[208:211], v[88:91], v[136:139]
ds_read_b64_tr_b16 v[194:195], v238 offset:1024
v_mfma_f32_16x16x32_bf16 v[140:143], v[208:211], v[92:95], v[140:143]
s_waitcnt lgkmcnt(6)
v_mfma_f32_16x16x32_bf16 v[144:147], v[212:215], v[80:83], v[144:147]
v_mfma_f32_16x16x32_bf16 v[148:151], v[212:215], v[84:87], v[148:151]
ds_read_b64_tr_b16 v[196:197], v239 offset:0
v_mfma_f32_16x16x32_bf16 v[152:155], v[212:215], v[88:91], v[152:155]
ds_read_b64_tr_b16 v[198:199], v239 offset:1024
v_mfma_f32_16x16x32_bf16 v[156:159], v[212:215], v[92:95], v[156:159]
s_waitcnt lgkmcnt(6)
v_mfma_f32_16x16x32_bf16 v[160:163], v[216:219], v[80:83], v[160:163]
v_mfma_f32_16x16x32_bf16 v[164:167], v[216:219], v[84:87], v[164:167]
ds_read_b64_tr_b16 v[200:201], v232 offset:0
v_mfma_f32_16x16x32_bf16 v[168:171], v[216:219], v[88:91], v[168:171]
ds_read_b64_tr_b16 v[202:203], v232 offset:1024
v_mfma_f32_16x16x32_bf16 v[172:175], v[216:219], v[92:95], v[172:175]
s_waitcnt lgkmcnt(6)
v_mfma_f32_16x16x32_bf16 v[176:179], v[220:223], v[80:83], v[176:179]
v_mfma_f32_16x16x32_bf16 v[180:183], v[220:223], v[84:87], v[180:183]
ds_read_b64_tr_b16 v[204:205], v233 offset:0
v_mfma_f32_16x16x32_bf16 v[184:187], v[220:223], v[88:91], v[184:187]
ds_read_b64_tr_b16 v[206:207], v233 offset:1024
v_mfma_f32_16x16x32_bf16 v[188:191], v[220:223], v[92:95], v[188:191]
s_waitcnt vmcnt(0)
s_setprio 3
v_cmp_ne_u64 vcc, v[248:249], v[250:251]
s_setprio 0
s_nop 0
)ASM" R"ASM(
s_cbranch_vccnz .Lk_ps_71
.Lk_pd_72:
s_add_u32 s62, s14, 0
buffer_load_dwordx4 v[96:99], v243, s[48:51], s62 offen offset:0 sc1
buffer_load_dwordx4 v[100:103], v243, s[48:51], s62 offen offset:256 sc1
s_add_u32 s62, s14, 147456
buffer_load_dwordx4 v[104:107], v243, s[48:51], s62 offen offset:0 sc1
buffer_load_dwordx4 v[108:111], v243, s[48:51], s62 offen offset:256 sc1
s_add_u32 s62, s14, 0
buffer_load_dwordx4 v[112:115], v243, s[48:51], s62 offen offset:2048 sc1
buffer_load_dwordx4 v[116:119], v243, s[48:51], s62 offen offset:2304 sc1
s_add_u32 s62, s14, 147456
buffer_load_dwordx4 v[120:123], v243, s[48:51], s62 offen offset:2048 sc1
buffer_load_dwordx4 v[124:127], v243, s[48:51], s62 offen offset:2304 sc1
s_add_u32 s62, s14, 4096
buffer_load_dwordx4 v[0:3], v243, s[48:51], s62 offen offset:0 sc1
buffer_load_dwordx4 v[4:7], v243, s[48:51], s62 offen offset:256 sc1
s_add_u32 s62, s14, 151552
buffer_load_dwordx4 v[8:11], v243, s[48:51], s62 offen offset:0 sc1
buffer_load_dwordx4 v[12:15], v243, s[48:51], s62 offen offset:256 sc1
s_add_u32 s62, s14, 4096
buffer_load_dwordx4 v[16:19], v243, s[48:51], s62 offen offset:2048 sc1
buffer_load_dwordx4 v[20:23], v243, s[48:51], s62 offen offset:2304 sc1
s_add_u32 s62, s14, 151552
buffer_load_dwordx4 v[24:27], v243, s[48:51], s62 offen offset:2048 sc1
buffer_load_dwordx4 v[28:31], v243, s[48:51], s62 offen offset:2304 sc1
v_and_b32 v240, 15, v224
v_lshrrev_b32 v241, 4, v224
v_xor_b32 v241, v241, v224
v_and_b32 v241, 7, v241
v_lshlrev_b32 v241, 4, v241
v_lshl_add_u32 v240, v240, 8, v241
v_add_u32 v240, s96, v240
v_xor_b32 v241, 64, v240
s_waitcnt vmcnt(12)
s_waitcnt lgkmcnt(6)
v_mfma_f32_16x16x32_bf16 v[128:131], v[192:195], v[96:99], v[128:131]
v_mfma_f32_16x16x32_bf16 v[132:135], v[192:195], v[100:103], v[132:135]
ds_read_b64_tr_b16 v[208:209], v238 offset:4096
v_mfma_f32_16x16x32_bf16 v[136:139], v[192:195], v[104:107], v[136:139]
ds_read_b64_tr_b16 v[210:211], v238 offset:5120
v_mfma_f32_16x16x32_bf16 v[140:143], v[192:195], v[108:111], v[140:143]
s_waitcnt lgkmcnt(6)
v_mfma_f32_16x16x32_bf16 v[144:147], v[196:199], v[96:99], v[144:147]
v_mfma_f32_16x16x32_bf16 v[148:151], v[196:199], v[100:103], v[148:151]
ds_read_b64_tr_b16 v[212:213], v239 offset:4096
v_mfma_f32_16x16x32_bf16 v[152:155], v[196:199], v[104:107], v[152:155]
ds_read_b64_tr_b16 v[214:215], v239 offset:5120
v_mfma_f32_16x16x32_bf16 v[156:159], v[196:199], v[108:111], v[156:159]
s_waitcnt lgkmcnt(6)
v_mfma_f32_16x16x32_bf16 v[160:163], v[200:203], v[96:99], v[160:163]
v_mfma_f32_16x16x32_bf16 v[164:167], v[200:203], v[100:103], v[164:167]
ds_read_b64_tr_b16 v[216:217], v232 offset:4096
v_mfma_f32_16x16x32_bf16 v[168:171], v[200:203], v[104:107], v[168:171]
ds_read_b64_tr_b16 v[218:219], v232 offset:5120
v_mfma_f32_16x16x32_bf16 v[172:175], v[200:203], v[108:111], v[172:175]
s_waitcnt lgkmcnt(6)
v_mfma_f32_16x16x32_bf16 v[176:179], v[204:207], v[96:99], v[176:179]
v_mfma_f32_16x16x32_bf16 v[180:183], v[204:207], v[100:103], v[180:183]
ds_read_b64_tr_b16 v[220:221], v233 offset:4096
v_mfma_f32_16x16x32_bf16 v[184:187], v[204:207], v[104:107], v[184:187]
ds_read_b64_tr_b16 v[222:223], v233 offset:5120
v_mfma_f32_16x16x32_bf16 v[188:191], v[204:207], v[108:111], v[188:191]
s_waitcnt vmcnt(8)
s_waitcnt lgkmcnt(6)
v_mfma_f32_16x16x32_bf16 v[128:131], v[208:211], v[112:115], v[128:131]
v_mfma_f32_16x16x32_bf16 v[132:135], v[208:211], v[116:119], v[132:135]
ds_read_b64_tr_b16 v[192:193], v238 offset:8192
v_mfma_f32_16x16x32_bf16 v[136:139], v[208:211], v[120:123], v[136:139]
ds_read_b64_tr_b16 v[194:195], v238 offset:9216
v_mfma_f32_16x16x32_bf16 v[140:143], v[208:211], v[124:127], v[140:143]
s_waitcnt lgkmcnt(6)
v_mfma_f32_16x16x32_bf16 v[144:147], v[212:215], v[112:115], v[144:147]
v_mfma_f32_16x16x32_bf16 v[148:151], v[212:215], v[116:119], v[148:151]
ds_read_b64_tr_b16 v[196:197], v239 offset:8192
v_mfma_f32_16x16x32_bf16 v[152:155], v[212:215], v[120:123], v[152:155]
ds_read_b64_tr_b16 v[198:199], v239 offset:9216
v_mfma_f32_16x16x32_bf16 v[156:159], v[212:215], v[124:127], v[156:159]
s_waitcnt lgkmcnt(6)
v_mfma_f32_16x16x32_bf16 v[160:163], v[216:219], v[112:115], v[160:163]
v_mfma_f32_16x16x32_bf16 v[164:167], v[216:219], v[116:119], v[164:167]
ds_read_b64_tr_b16 v[200:201], v232 offset:8192
v_mfma_f32_16x16x32_bf16 v[168:171], v[216:219], v[120:123], v[168:171]
ds_read_b64_tr_b16 v[202:203], v232 offset:9216
v_mfma_f32_16x16x32_bf16 v[172:175], v[216:219], v[124:127], v[172:175]
s_waitcnt lgkmcnt(6)
v_mfma_f32_16x16x32_bf16 v[176:179], v[220:223], v[112:115], v[176:179]
v_mfma_f32_16x16x32_bf16 v[180:183], v[220:223], v[116:119], v[180:183]
ds_read_b64_tr_b16 v[204:205], v233 offset:8192
v_mfma_f32_16x16x32_bf16 v[184:187], v[220:223], v[120:123], v[184:187]
ds_read_b64_tr_b16 v[206:207], v233 offset:9216
v_mfma_f32_16x16x32_bf16 v[188:191], v[220:223], v[124:127], v[188:191]
ds_read_b64_tr_b16 v[208:209], v238 offset:12288
ds_read_b64_tr_b16 v[210:211], v238 offset:13312
ds_read_b64_tr_b16 v[212:213], v239 offset:12288
ds_read_b64_tr_b16 v[214:215], v239 offset:13312
ds_read_b64_tr_b16 v[216:217], v232 offset:12288
ds_read_b64_tr_b16 v[218:219], v232 offset:13312
ds_read_b64_tr_b16 v[220:221], v233 offset:12288
s_waitcnt lgkmcnt(13)
ds_read_b64_tr_b16 v[222:223], v233 offset:13312
s_waitcnt vmcnt(4)
v_mfma_f32_16x16x32_bf16 v[128:131], v[192:195], v[0:3], v[128:131]
v_mfma_f32_16x16x32_bf16 v[132:135], v[192:195], v[4:7], v[132:135]
v_mfma_f32_16x16x32_bf16 v[136:139], v[192:195], v[8:11], v[136:139]
v_mfma_f32_16x16x32_bf16 v[140:143], v[192:195], v[12:15], v[140:143]
s_waitcnt vmcnt(0)
s_waitcnt lgkmcnt(6)
v_mfma_f32_16x16x32_bf16 v[128:131], v[208:211], v[16:19], v[128:131]
v_mfma_f32_16x16x32_bf16 v[132:135], v[208:211], v[20:23], v[132:135]
v_mfma_f32_16x16x32_bf16 v[136:139], v[208:211], v[24:27], v[136:139]
v_mfma_f32_16x16x32_bf16 v[140:143], v[208:211], v[28:31], v[140:143]
v_mfma_f32_16x16x32_bf16 v[144:147], v[196:199], v[0:3], v[144:147]
v_mfma_f32_16x16x32_bf16 v[148:151], v[196:199], v[4:7], v[148:151]
v_mfma_f32_16x16x32_bf16 v[152:155], v[196:199], v[8:11], v[152:155]
v_mfma_f32_16x16x32_bf16 v[156:159], v[196:199], v[12:15], v[156:159]
s_waitcnt lgkmcnt(4)
v_mfma_f32_16x16x32_bf16 v[144:147], v[212:215], v[16:19], v[144:147]
s_nop 2
ds_write_b128 v240, v[128:131] offset:0
v_mfma_f32_16x16x32_bf16 v[148:151], v[212:215], v[20:23], v[148:151]
ds_write_b128 v240, v[132:135] offset:4096
v_mfma_f32_16x16x32_bf16 v[152:155], v[212:215], v[24:27], v[152:155]
ds_write_b128 v240, v[136:139] offset:8192
v_mfma_f32_16x16x32_bf16 v[156:159], v[212:215], v[28:31], v[156:159]
ds_write_b128 v240, v[140:143] offset:12288
v_mfma_f32_16x16x32_bf16 v[160:163], v[200:203], v[0:3], v[160:163]
v_mfma_f32_16x16x32_bf16 v[164:167], v[200:203], v[4:7], v[164:167]
v_mfma_f32_16x16x32_bf16 v[168:171], v[200:203], v[8:11], v[168:171]
v_mfma_f32_16x16x32_bf16 v[172:175], v[200:203], v[12:15], v[172:175]
s_waitcnt lgkmcnt(6)
v_mfma_f32_16x16x32_bf16 v[160:163], v[216:219], v[16:19], v[160:163]
ds_write_b128 v241, v[144:147] offset:0
v_mfma_f32_16x16x32_bf16 v[164:167], v[216:219], v[20:23], v[164:167]
ds_write_b128 v241, v[148:151] offset:4096
v_mfma_f32_16x16x32_bf16 v[168:171], v[216:219], v[24:27], v[168:171]
ds_write_b128 v241, v[152:155] offset:8192
v_mfma_f32_16x16x32_bf16 v[172:175], v[216:219], v[28:31], v[172:175]
ds_write_b128 v241, v[156:159] offset:12288
v_mfma_f32_16x16x32_bf16 v[176:179], v[204:207], v[0:3], v[176:179]
v_mfma_f32_16x16x32_bf16 v[180:183], v[204:207], v[4:7], v[180:183]
v_mfma_f32_16x16x32_bf16 v[184:187], v[204:207], v[8:11], v[184:187]
v_mfma_f32_16x16x32_bf16 v[188:191], v[204:207], v[12:15], v[188:191]
s_waitcnt lgkmcnt(8)
v_mfma_f32_16x16x32_bf16 v[176:179], v[220:223], v[16:19], v[176:179]
ds_write_b128 v240, v[160:163] offset:128
v_mfma_f32_16x16x32_bf16 v[180:183], v[220:223], v[20:23], v[180:183]
ds_write_b128 v240, v[164:167] offset:4224
v_mfma_f32_16x16x32_bf16 v[184:187], v[220:223], v[24:27], v[184:187]
ds_write_b128 v240, v[168:171] offset:8320
v_mfma_f32_16x16x32_bf16 v[188:191], v[220:223], v[28:31], v[188:191]
ds_write_b128 v240, v[172:175] offset:12416
s_nop 4
ds_write_b128 v241, v[176:179] offset:128
s_nop 0
ds_write_b128 v241, v[180:183] offset:4224
s_nop 0
ds_write_b128 v241, v[184:187] offset:8320
s_waitcnt lgkmcnt(13)
ds_write_b128 v241, v[188:191] offset:12416
.Lk_final:
s_mov_b32 s64, 0xffff0000
s_waitcnt vmcnt(0)
v_lshlrev_b32 v48, 16, v228
v_and_b32 v49, s64, v228
v_lshlrev_b32 v50, 16, v229
v_and_b32 v51, s64, v229
v_lshlrev_b32 v52, 16, v230
v_and_b32 v53, s64, v230
v_lshlrev_b32 v54, 16, v231
v_and_b32 v55, s64, v231
s_waitcnt lgkmcnt(0)
s_barrier
ds_read_b128 v[64:67], v254 offset:0
ds_read_b128 v[68:71], v255 offset:0
ds_read_b128 v[72:75], v254 offset:16384
ds_read_b128 v[76:79], v255 offset:16384
ds_read_b128 v[80:83], v254 offset:32768
ds_read_b128 v[84:87], v255 offset:32768
ds_read_b128 v[88:91], v254 offset:49152
ds_read_b128 v[92:95], v255 offset:49152
s_waitcnt lgkmcnt(6)
v_pk_add_f32 v[48:49], v[48:49], v[64:65]
v_pk_add_f32 v[50:51], v[50:51], v[66:67]
v_pk_add_f32 v[52:53], v[52:53], v[68:69]
v_pk_add_f32 v[54:55], v[54:55], v[70:71]
s_waitcnt lgkmcnt(4)
v_pk_add_f32 v[48:49], v[48:49], v[72:73]
v_pk_add_f32 v[50:51], v[50:51], v[74:75]
v_pk_add_f32 v[52:53], v[52:53], v[76:77]
v_pk_add_f32 v[54:55], v[54:55], v[78:79]
s_waitcnt lgkmcnt(2)
v_pk_add_f32 v[48:49], v[48:49], v[80:81]
v_pk_add_f32 v[50:51], v[50:51], v[82:83]
v_pk_add_f32 v[52:53], v[52:53], v[84:85]
v_pk_add_f32 v[54:55], v[54:55], v[86:87]
s_waitcnt lgkmcnt(0)
v_pk_add_f32 v[48:49], v[48:49], v[88:89]
v_pk_add_f32 v[50:51], v[50:51], v[90:91]
v_pk_add_f32 v[52:53], v[52:53], v[92:93]
)ASM" R"ASM(
v_pk_add_f32 v[54:55], v[54:55], v[94:95]
v_cvt_pk_bf16_f32 v8, v48, v49
v_cvt_pk_bf16_f32 v9, v50, v51
v_cvt_pk_bf16_f32 v10, v52, v53
v_cvt_pk_bf16_f32 v11, v54, v55
buffer_store_dwordx4 v[8:11], v253, s[44:47], s59 offen 
s_or_b32 s62, s2, s52
s_cmp_lg_u32 s62, 0
s_cbranch_scc1 .Lk_noep
s_mov_b64 exec, 1
buffer_store_dwordx2 v[248:249], off, s[48:51], 0 offset:0 sc1
s_mov_b64 exec, -1
.Lk_noep:
s_endpgm
.Lk_att:
s_lshl_b32 s62, s53, 8
v_add_u32 v244, s62, v245
ds_write_b32 v244, v250 offset:0
ds_write_b32 v244, v250 offset:28672
ds_write_b32 v244, v250 offset:29696
ds_write_b32 v244, v250 offset:30720
ds_write_b32 v245, v250 offset:32512
v_mov_b32 v239, v245
s_and_b32 s94, s54, 7
s_mov_b32 s48, s18
s_and_b32 s49, s19, 0xffff
s_mov_b32 s50, 14680064
s_mov_b32 s51, 0x20000
s_mov_b32 s28, s10
s_and_b32 s29, s11, 0xffff
s_mov_b32 s30, 1835008
s_mov_b32 s31, 0x20000
s_mov_b32 s32, s12
s_and_b32 s33, s13, 0xffff
s_mov_b32 s34, 1835008
s_mov_b32 s35, 0x20000
s_mov_b32 s44, s16
s_and_b32 s45, s17, 0xffff
s_mov_b32 s46, 2097152
s_mov_b32 s47, 0x20000
s_mov_b32 s36, s18
s_and_b32 s37, s19, 0xffff
s_mov_b32 s38, 14680064
s_mov_b32 s39, 0x20000
v_lshrrev_b32 v244, 3, v224
v_mul_u32_u24 v170, 3584, v244
v_lshrrev_b32 v244, 2, v224
v_and_b32 v244, 4, v244
v_xor_b32 v244, v244, v224
v_and_b32 v244, 7, v244
v_lshl_add_u32 v170, v244, 4, v170
v_lshrrev_b32 v244, 2, v224
v_mul_u32_u24 v169, 3072, v244
v_and_b32 v244, 3, v224
v_lshl_add_u32 v169, v244, 4, v169
s_lshl_b32 s93, s53, 5
s_add_u32 s84, s56, 0
s_and_b32 s84, s84, 3
s_lshl_b32 s84, s84, 7
s_add_u32 s84, s84, s93
s_mul_i32 s92, s84, 3584
s_mul_i32 s84, s84, 3072
s_add_u32 s85, s56, 1
s_and_b32 s85, s85, 3
s_lshl_b32 s85, s85, 7
s_add_u32 s85, s85, s93
s_mul_i32 s6, s85, 3584
s_mul_i32 s85, s85, 3072
s_add_u32 s86, s56, 2
s_and_b32 s86, s86, 3
s_lshl_b32 s86, s86, 7
s_add_u32 s86, s86, s93
s_mul_i32 s86, s86, 3072
s_add_u32 s87, s56, 3
s_and_b32 s87, s87, 3
s_lshl_b32 s87, s87, 7
s_add_u32 s87, s87, s93
s_mul_i32 s87, s87, 3072
s_lshr_b32 s62, s55, 2
s_mul_i32 s62, s62, 7
s_and_b32 s63, s55, 3
s_lshl_b32 s63, s63, 1
s_add_u32 s62, s62, s63
s_lshl_b32 s99, s62, 7
s_add_u32 s92, s92, s99
s_add_u32 s6, s6, s99
s_waitcnt vmcnt(12)
ds_read_b128 v[0:3], v136 offset:0
ds_read_b128 v[4:7], v137 offset:0
ds_read_b128 v[8:11], v138 offset:0
ds_read_b128 v[12:15], v139 offset:0
s_waitcnt vmcnt(8)
ds_read_b128 v[16:19], v136 offset:4096
ds_read_b128 v[20:23], v137 offset:4096
ds_read_b128 v[24:27], v138 offset:4096
ds_read_b128 v[28:31], v139 offset:4096
s_waitcnt lgkmcnt(4)
v_dot2c_f32_bf16 v250, v233, v0
v_dot2c_f32_bf16 v227, v0, v0
v_dot2c_f32_bf16 v251, v233, v1
v_dot2c_f32_bf16 v232, v1, v1
v_dot2c_f32_bf16 v250, v233, v2
v_dot2c_f32_bf16 v227, v2, v2
v_dot2c_f32_bf16 v251, v233, v3
v_dot2c_f32_bf16 v232, v3, v3
v_dot2c_f32_bf16 v250, v233, v4
v_dot2c_f32_bf16 v227, v4, v4
v_dot2c_f32_bf16 v251, v233, v5
v_dot2c_f32_bf16 v232, v5, v5
v_dot2c_f32_bf16 v250, v233, v6
v_dot2c_f32_bf16 v227, v6, v6
v_dot2c_f32_bf16 v251, v233, v7
v_dot2c_f32_bf16 v232, v7, v7
v_dot2c_f32_bf16 v250, v233, v8
v_dot2c_f32_bf16 v227, v8, v8
v_dot2c_f32_bf16 v251, v233, v9
v_dot2c_f32_bf16 v232, v9, v9
v_dot2c_f32_bf16 v250, v233, v10
v_dot2c_f32_bf16 v227, v10, v10
v_dot2c_f32_bf16 v251, v233, v11
v_dot2c_f32_bf16 v232, v11, v11
v_dot2c_f32_bf16 v250, v233, v12
v_dot2c_f32_bf16 v227, v12, v12
v_dot2c_f32_bf16 v251, v233, v13
v_dot2c_f32_bf16 v232, v13, v13
v_dot2c_f32_bf16 v250, v233, v14
v_dot2c_f32_bf16 v227, v14, v14
v_dot2c_f32_bf16 v251, v233, v15
v_dot2c_f32_bf16 v232, v15, v15
s_waitcnt vmcnt(4)
ds_read_b128 v[32:35], v136 offset:8192
ds_read_b128 v[36:39], v137 offset:8192
ds_read_b128 v[40:43], v138 offset:8192
ds_read_b128 v[44:47], v139 offset:8192
s_waitcnt lgkmcnt(4)
v_dot2c_f32_bf16 v250, v233, v16
v_dot2c_f32_bf16 v227, v16, v16
v_dot2c_f32_bf16 v251, v233, v17
v_dot2c_f32_bf16 v232, v17, v17
v_dot2c_f32_bf16 v250, v233, v18
v_dot2c_f32_bf16 v227, v18, v18
v_dot2c_f32_bf16 v251, v233, v19
v_dot2c_f32_bf16 v232, v19, v19
v_dot2c_f32_bf16 v250, v233, v20
v_dot2c_f32_bf16 v227, v20, v20
v_dot2c_f32_bf16 v251, v233, v21
v_dot2c_f32_bf16 v232, v21, v21
v_dot2c_f32_bf16 v250, v233, v22
v_dot2c_f32_bf16 v227, v22, v22
v_dot2c_f32_bf16 v251, v233, v23
v_dot2c_f32_bf16 v232, v23, v23
v_dot2c_f32_bf16 v250, v233, v24
v_dot2c_f32_bf16 v227, v24, v24
v_dot2c_f32_bf16 v251, v233, v25
v_dot2c_f32_bf16 v232, v25, v25
v_dot2c_f32_bf16 v250, v233, v26
v_dot2c_f32_bf16 v227, v26, v26
v_dot2c_f32_bf16 v251, v233, v27
v_dot2c_f32_bf16 v232, v27, v27
v_dot2c_f32_bf16 v250, v233, v28
v_dot2c_f32_bf16 v227, v28, v28
v_dot2c_f32_bf16 v251, v233, v29
v_dot2c_f32_bf16 v232, v29, v29
v_dot2c_f32_bf16 v250, v233, v30
v_dot2c_f32_bf16 v227, v30, v30
v_dot2c_f32_bf16 v251, v233, v31
v_dot2c_f32_bf16 v232, v31, v31
s_waitcnt vmcnt(0)
ds_read_b128 v[48:51], v136 offset:12288
ds_read_b128 v[52:55], v137 offset:12288
ds_read_b128 v[56:59], v138 offset:12288
ds_read_b128 v[60:63], v139 offset:12288
s_waitcnt lgkmcnt(4)
v_dot2c_f32_bf16 v250, v233, v32
v_dot2c_f32_bf16 v227, v32, v32
v_dot2c_f32_bf16 v251, v233, v33
v_dot2c_f32_bf16 v232, v33, v33
v_dot2c_f32_bf16 v250, v233, v34
v_dot2c_f32_bf16 v227, v34, v34
v_dot2c_f32_bf16 v251, v233, v35
v_dot2c_f32_bf16 v232, v35, v35
v_dot2c_f32_bf16 v250, v233, v36
v_dot2c_f32_bf16 v227, v36, v36
v_dot2c_f32_bf16 v251, v233, v37
v_dot2c_f32_bf16 v232, v37, v37
v_dot2c_f32_bf16 v250, v233, v38
v_dot2c_f32_bf16 v227, v38, v38
v_dot2c_f32_bf16 v251, v233, v39
v_dot2c_f32_bf16 v232, v39, v39
v_dot2c_f32_bf16 v250, v233, v40
v_dot2c_f32_bf16 v227, v40, v40
v_dot2c_f32_bf16 v251, v233, v41
v_dot2c_f32_bf16 v232, v41, v41
v_dot2c_f32_bf16 v250, v233, v42
v_dot2c_f32_bf16 v227, v42, v42
v_dot2c_f32_bf16 v251, v233, v43
v_dot2c_f32_bf16 v232, v43, v43
v_dot2c_f32_bf16 v250, v233, v44
v_dot2c_f32_bf16 v227, v44, v44
v_dot2c_f32_bf16 v251, v233, v45
)ASM" R"ASM(
v_dot2c_f32_bf16 v232, v45, v45
v_dot2c_f32_bf16 v250, v233, v46
v_dot2c_f32_bf16 v227, v46, v46
v_dot2c_f32_bf16 v251, v233, v47
v_dot2c_f32_bf16 v232, v47, v47
s_waitcnt lgkmcnt(0)
v_dot2c_f32_bf16 v250, v233, v48
v_dot2c_f32_bf16 v227, v48, v48
v_dot2c_f32_bf16 v251, v233, v49
v_dot2c_f32_bf16 v232, v49, v49
v_dot2c_f32_bf16 v250, v233, v50
v_dot2c_f32_bf16 v227, v50, v50
v_dot2c_f32_bf16 v251, v233, v51
v_dot2c_f32_bf16 v232, v51, v51
v_dot2c_f32_bf16 v250, v233, v52
v_dot2c_f32_bf16 v227, v52, v52
v_dot2c_f32_bf16 v251, v233, v53
v_dot2c_f32_bf16 v232, v53, v53
v_dot2c_f32_bf16 v250, v233, v54
v_dot2c_f32_bf16 v227, v54, v54
v_dot2c_f32_bf16 v251, v233, v55
v_dot2c_f32_bf16 v232, v55, v55
v_dot2c_f32_bf16 v250, v233, v56
v_dot2c_f32_bf16 v227, v56, v56
v_dot2c_f32_bf16 v251, v233, v57
v_dot2c_f32_bf16 v232, v57, v57
v_dot2c_f32_bf16 v250, v233, v58
v_dot2c_f32_bf16 v227, v58, v58
v_dot2c_f32_bf16 v251, v233, v59
v_dot2c_f32_bf16 v232, v59, v59
v_dot2c_f32_bf16 v250, v233, v60
v_dot2c_f32_bf16 v227, v60, v60
v_dot2c_f32_bf16 v251, v233, v61
v_dot2c_f32_bf16 v232, v61, v61
v_dot2c_f32_bf16 v250, v233, v62
v_dot2c_f32_bf16 v227, v62, v62
v_dot2c_f32_bf16 v251, v233, v63
v_dot2c_f32_bf16 v232, v63, v63
s_nop 1
v_add_f32 v250, v250, v251
v_add_f32 v227, v227, v232
v_mov_b32 v251, v250
v_mov_b32 v232, v227
s_nop 0
v_permlane32_swap_b32 v250, v251
v_permlane32_swap_b32 v227, v232
v_add_f32 v250, v250, v251
v_add_f32 v251, v227, v232
ds_write_b64 v252, v[250:251]
s_waitcnt lgkmcnt(0)
s_barrier
s_lshl_b32 s62, s54, 2
s_add_u32 s62, s62, s53
s_mul_i32 s62, s62, 28672
s_add_u32 s62, s62, s99
v_mul_u32_u24 v70, 3584, v224
s_mov_b64 exec, 0xff
buffer_load_dword v247, v70, s[28:31], s62 offen offset:0 
buffer_load_dword v247, v70, s[32:35], s62 offen offset:0 
s_mov_b64 exec, -1
s_add_u32 m0, s96, 8192
s_add_u32 s62, s90, 1024
buffer_load_dwordx4 v222, s[24:27], s62 offen  lds
s_add_u32 m0, s96, 9216
s_add_u32 s62, s90, 25600
buffer_load_dwordx4 v222, s[24:27], s62 offen  lds
s_add_u32 m0, s96, 10240
s_add_u32 s62, s90, 50176
buffer_load_dwordx4 v222, s[24:27], s62 offen  lds
s_add_u32 m0, s96, 11264
s_add_u32 s62, s90, 74752
buffer_load_dwordx4 v222, s[24:27], s62 offen  lds
s_add_u32 m0, s96, 12288
s_add_u32 s62, s91, 1024
buffer_load_dwordx4 v222, s[24:27], s62 offen  lds
s_add_u32 m0, s96, 13312
s_add_u32 s62, s91, 25600
buffer_load_dwordx4 v222, s[24:27], s62 offen  lds
s_add_u32 m0, s96, 14336
s_add_u32 s62, s91, 50176
buffer_load_dwordx4 v222, s[24:27], s62 offen  lds
s_add_u32 m0, s96, 15360
s_add_u32 s62, s91, 74752
buffer_load_dwordx4 v222, s[24:27], s62 offen  lds
s_setprio 0
v_mov_b32 v232, v224
buffer_load_dwordx2 v[248:249], off, s[48:51], 0 sc1
ds_read_b32 v237, v239 offset:512
s_waitcnt lgkmcnt(0)
v_cmp_gt_u32 vcc, 4, v237
s_nop 0
s_nop 0
s_cbranch_vccnz .Lk_ls_73
.Lk_ld_74:
s_lshl_b32 s93, s53, 14
s_lshl_b32 s91, s55, 6
s_add_u32 s90, s84, s91
s_add_u32 m0, s93, 65536
s_add_u32 s62, s90, 0
buffer_load_dwordx4 v169, s[24:27], s62 offen  lds
s_add_u32 m0, s93, 66560
s_add_u32 s62, s90, 49152
buffer_load_dwordx4 v169, s[24:27], s62 offen  lds
s_waitcnt vmcnt(7)
ds_add_u32 v239, v246 offset:768
s_add_u32 s90, s85, s91
s_add_u32 m0, s93, 67584
s_add_u32 s62, s90, 0
buffer_load_dwordx4 v169, s[24:27], s62 offen  lds
s_add_u32 m0, s93, 68608
s_add_u32 s62, s90, 49152
buffer_load_dwordx4 v169, s[24:27], s62 offen  lds
s_waitcnt vmcnt(5)
ds_add_u32 v239, v246 offset:0
s_add_u32 s90, s86, s91
s_add_u32 m0, s93, 69632
s_add_u32 s62, s90, 0
buffer_load_dwordx4 v169, s[24:27], s62 offen  lds
s_add_u32 m0, s93, 70656
s_add_u32 s62, s90, 49152
buffer_load_dwordx4 v169, s[24:27], s62 offen  lds
s_add_u32 s90, s87, s91
s_add_u32 m0, s93, 71680
s_add_u32 s62, s90, 0
buffer_load_dwordx4 v169, s[24:27], s62 offen  lds
s_add_u32 m0, s93, 72704
s_add_u32 s62, s90, 49152
buffer_load_dwordx4 v169, s[24:27], s62 offen  lds
s_add_u32 m0, s93, 73728
s_add_u32 s62, s92, 0
buffer_load_dwordx4 v170, s[28:31], s62 offen  lds
s_add_u32 m0, s93, 74752
s_add_u32 s62, s92, 28672
buffer_load_dwordx4 v170, s[28:31], s62 offen  lds
s_add_u32 m0, s93, 75776
s_add_u32 s62, s92, 57344
buffer_load_dwordx4 v170, s[28:31], s62 offen  lds
s_add_u32 m0, s93, 76800
s_add_u32 s62, s92, 86016
buffer_load_dwordx4 v170, s[28:31], s62 offen  lds
s_add_u32 m0, s93, 77824
s_add_u32 s62, s92, 0
buffer_load_dwordx4 v170, s[32:35], s62 offen  lds
s_add_u32 m0, s93, 78848
s_add_u32 s62, s92, 28672
buffer_load_dwordx4 v170, s[32:35], s62 offen  lds
s_add_u32 m0, s93, 79872
s_add_u32 s62, s92, 57344
buffer_load_dwordx4 v170, s[32:35], s62 offen  lds
s_add_u32 m0, s93, 80896
s_add_u32 s62, s92, 86016
buffer_load_dwordx4 v170, s[32:35], s62 offen  lds
s_lshr_b32 s62, s53, 1
s_lshl_b32 s95, s55, 1
s_add_u32 s95, s95, s62
s_and_b32 s7, s53, 1
s_lshl_b32 s4, s95, 17
s_lshl_b32 s62, s7, 16
s_add_u32 s4, s4, s62
s_add_u32 s4, s4, 1048576
s_lshl_b32 s94, s94, 13
s_lshl_b32 s94, s95, 11
v_lshlrev_b32 v233, 4, v232
v_and_b32 v234, 15, v232
v_lshrrev_b32 v244, 5, v232
v_lshlrev_b32 v234, 4, v234
v_lshl_add_u32 v234, v244, 8, v234
v_mov_b32 v235, 0xdddddddd
s_lshr_b32 s62, s55, 1
s_lshl_b32 s62, s62, 11
s_lshl_b32 s63, s7, 10
s_add_u32 s62, s62, s63
s_add_u32 s62, s62, 131072
v_and_b32 v236, 31, v232
v_lshlrev_b32 v236, 5, v236
v_lshl_add_u32 v236, v244, 14, v236
v_add_u32 v236, s62, v236
s_lshl_b32 s62, s54, 1
s_add_u32 s62, s62, s7
s_lshl_b32 s62, s62, 1
s_mul_i32 s97, s62, 144
s_add_u32 s97, s97, s95
s_lshl_b32 s97, s97, 10
s_add_u32 s97, s97, 5357568
s_add_u32 s5, s97, 147456
s_lshl_b32 s62, s62, 4
s_add_u32 s62, s62, s95
s_lshl_b32 s62, s62, 4
s_add_u32 s98, s62, 262144
s_lshl_b32 s62, s57, 2
s_and_b32 s63, s58, 3
s_add_u32 s62, s62, s63
s_lshl_b32 s62, s62, 3
s_add_u32 s62, s62, s52
s_lshl_b32 s62, s62, 13
s_lshr_b32 s63, s58, 2
s_lshl_b32 s63, s63, 7
s_add_u32 s59, s62, s63
v_lshrrev_b32 v244, 3, v232
v_and_b32 v245, 7, v232
)ASM" R"ASM(
v_lshlrev_b32 v253, 10, v244
v_lshl_add_u32 v253, v245, 4, v253
v_lshlrev_b32 v254, 1, v245
v_xor_b32 v254, v254, v244
v_lshlrev_b32 v254, 4, v254
v_lshl_add_u32 v254, v244, 8, v254
s_lshr_b32 s62, s52, 2
s_lshl_b32 s62, s62, 13
s_lshl_b32 s63, s53, 11
s_add_u32 s62, s62, s63
v_add_u32 v254, s62, v254
v_xor_b32 v255, 16, v254
s_waitcnt vmcnt(16)
v_add_co_u32 v248, vcc, 0x7f4a7c15, v248
v_mov_b32 v237, 0x9e3779b9
v_mov_b32 v244, 163328
v_addc_co_u32 v249, vcc, v237, v249, vcc
ds_write_b64 v244, v[248:249]
s_mul_i32 s62, s53, 5120
s_add_u32 s62, s62, 139264
v_add_u32 v242, s62, v233
s_xor_b32 s62, s53, 1
s_mul_i32 s62, s62, 5120
s_add_u32 s62, s62, 139264
v_add_u32 v243, s62, v233
s_lshr_b32 s62, s53, 1
s_lshl_b32 s62, s62, 12
s_lshl_b32 s63, s7, 11
s_add_u32 s62, s62, s63
s_add_u32 s62, s62, 131072
v_add_u32 v245, s62, v233
v_xor_b32 v244, 0x800, v245
s_mov_b32 s88, 0x0000ffff
s_mov_b32 s89, 0x0000ffff
s_mov_b32 s90, s4
s_waitcnt lgkmcnt(0)
s_waitcnt vmcnt(8)
s_barrier
v_and_b32 v238, 27, v232
v_cmp_eq_u32 vcc, 16, v238
v_mov_b32 v238, 0x3f803f80
v_mov_b32 v220, v238
v_mov_b32 v221, v238
v_mov_b32 v222, v238
v_mov_b32 v223, v238
v_cndmask_b32 v238, 0, v238, vcc
v_mov_b32 v0, 0
v_mov_b32 v1, 0
v_mov_b32 v2, 0
v_mov_b32 v3, 0
v_mov_b32 v4, 0
v_mov_b32 v5, 0
v_mov_b32 v6, 0
v_mov_b32 v7, 0
v_mov_b32 v8, 0
v_mov_b32 v9, 0
v_mov_b32 v10, 0
v_mov_b32 v11, 0
v_mov_b32 v12, 0
v_mov_b32 v13, 0
v_mov_b32 v14, 0
v_mov_b32 v15, 0
v_mov_b32 v16, 0
v_mov_b32 v17, 0
v_mov_b32 v18, 0
v_mov_b32 v19, 0
v_mov_b32 v20, 0
v_mov_b32 v21, 0
v_mov_b32 v22, 0
v_mov_b32 v23, 0
v_mov_b32 v24, 0
v_mov_b32 v25, 0
v_mov_b32 v26, 0
v_mov_b32 v27, 0
v_mov_b32 v28, 0
v_mov_b32 v29, 0
v_mov_b32 v30, 0
v_mov_b32 v31, 0
v_mov_b32 v32, 0
v_mov_b32 v33, 0
v_mov_b32 v34, 0
v_mov_b32 v35, 0
v_mov_b32 v36, 0
v_mov_b32 v37, 0
v_mov_b32 v38, 0
v_mov_b32 v39, 0
v_mov_b32 v40, 0
v_mov_b32 v41, 0
v_mov_b32 v42, 0
v_mov_b32 v43, 0
v_mov_b32 v44, 0
v_mov_b32 v45, 0
v_mov_b32 v46, 0
v_mov_b32 v47, 0
v_mov_b32 v48, 0
v_mov_b32 v49, 0
v_mov_b32 v50, 0
v_mov_b32 v51, 0
v_mov_b32 v52, 0
v_mov_b32 v53, 0
v_mov_b32 v54, 0
v_mov_b32 v55, 0
v_mov_b32 v56, 0
v_mov_b32 v57, 0
v_mov_b32 v58, 0
v_mov_b32 v59, 0
v_mov_b32 v60, 0
v_mov_b32 v61, 0
v_mov_b32 v62, 0
v_mov_b32 v63, 0
v_mov_b32 v160, 0
v_mov_b32 v161, 0
v_mov_b32 v162, 0
v_mov_b32 v163, 0
v_mov_b32 v164, 0
v_mov_b32 v165, 0
v_mov_b32 v166, 0
v_mov_b32 v167, 0
v_mov_b32 v188, v238
v_mov_b32 v189, v238
v_mov_b32 v190, v238
v_mov_b32 v191, v238
v_mov_b32 v192, v238
v_mov_b32 v193, v238
v_mov_b32 v194, v238
v_mov_b32 v195, v238
v_mov_b32 v200, v238
v_mov_b32 v201, v238
v_mov_b32 v202, v238
v_mov_b32 v203, v238
v_mov_b32 v204, v238
v_mov_b32 v205, v238
v_mov_b32 v206, v238
v_mov_b32 v207, v238
v_mov_b32 v212, v238
v_mov_b32 v213, v238
v_mov_b32 v214, v238
v_mov_b32 v215, v238
v_mov_b32 v216, v238
v_mov_b32 v217, v238
v_mov_b32 v218, v238
v_mov_b32 v219, v238
v_mov_b32 v224, v238
v_mov_b32 v225, v238
v_mov_b32 v226, v238
v_mov_b32 v227, v238
v_mov_b32 v228, v238
v_mov_b32 v229, v238
v_mov_b32 v230, v238
v_mov_b32 v231, v238
v_mov_b32 v144, 0
v_mov_b32 v145, 0
v_mov_b32 v146, 0
v_mov_b32 v147, 0
v_mov_b32 v148, 0
v_mov_b32 v149, 0
v_mov_b32 v150, 0
v_mov_b32 v151, 0
v_mov_b32 v152, 0
v_mov_b32 v153, 0
v_mov_b32 v154, 0
v_mov_b32 v155, 0
v_mov_b32 v156, 0
v_mov_b32 v157, 0
v_mov_b32 v158, 0
v_mov_b32 v159, 0
s_mov_b32 s69, 0
ds_read_b32 v237, v239 offset:32512
s_waitcnt lgkmcnt(0)
v_cmp_gt_u32 vcc, 4, v237
s_nop 0
s_nop 0
s_cbranch_vccnz .Lk_ls_75
.Lk_ld_76:
s_add_u32 m0, s93, 0
s_add_u32 s62, s6, 0
buffer_load_dwordx4 v170, s[28:31], s62 offen  lds
s_add_u32 m0, s93, 1024
s_add_u32 s62, s6, 28672
buffer_load_dwordx4 v170, s[28:31], s62 offen  lds
s_add_u32 m0, s93, 2048
s_add_u32 s62, s6, 57344
buffer_load_dwordx4 v170, s[28:31], s62 offen  lds
s_add_u32 m0, s93, 3072
s_add_u32 s62, s6, 86016
buffer_load_dwordx4 v170, s[28:31], s62 offen  lds
s_add_u32 m0, s93, 4096
s_add_u32 s62, s6, 0
buffer_load_dwordx4 v170, s[32:35], s62 offen  lds
s_add_u32 m0, s93, 5120
s_add_u32 s62, s6, 28672
buffer_load_dwordx4 v170, s[32:35], s62 offen  lds
s_add_u32 m0, s93, 6144
s_add_u32 s62, s6, 57344
buffer_load_dwordx4 v170, s[32:35], s62 offen  lds
s_add_u32 m0, s93, 7168
s_add_u32 s62, s6, 86016
buffer_load_dwordx4 v170, s[32:35], s62 offen  lds
s_waitcnt vmcnt(8)
ds_add_u32 v239, v246 offset:29184
)ASM" R"ASM(
s_setprio 2
ds_read_b32 v237, v239 offset:28928
s_waitcnt lgkmcnt(0)
v_cmp_gt_u32 vcc, 4, v237
s_nop 0
s_nop 0
s_cbranch_vccnz .Lk_ls_77
.Lk_ld_78:
ds_read_b128 v[168:171], v244 offset:0
ds_read_b128 v[172:175], v244 offset:1024
ds_read_b128 v[176:179], v245 offset:0
ds_read_b128 v[180:183], v245 offset:1024
buffer_load_dwordx2 v[240:241], v236, s[48:51], 0 offen offset:0 sc1
s_waitcnt vmcnt(0)
v_cmp_ne_u64 vcc, v[248:249], v[240:241]
s_nop 0
s_nop 0
s_cbranch_vccnz .Lk_ps_79
.Lk_pd_80:
s_mov_b32 s91, s94
v_mov_b32 v240, 1
s_add_u32 s62, s90, s91
buffer_load_dwordx4 v[184:187], v233, s[36:39], s62 offen sc1
s_mov_b64 exec, s[88:89]
buffer_load_dwordx4 v[192:195], v234, s[36:39], s62 offen offset:1536 sc1
buffer_load_dwordx4 v[188:191], v234, s[36:39], s62 offen offset:1024 sc1
s_mov_b64 exec, -1
s_add_u32 s91, s91, 2048
s_and_b32 s91, s91, 0xffff
s_add_u32 s62, s90, s91
buffer_load_dwordx4 v[196:199], v233, s[36:39], s62 offen sc1
s_mov_b64 exec, s[88:89]
buffer_load_dwordx4 v[204:207], v234, s[36:39], s62 offen offset:1536 sc1
buffer_load_dwordx4 v[200:203], v234, s[36:39], s62 offen offset:1024 sc1
s_mov_b64 exec, -1
s_add_u32 s91, s91, 2048
s_and_b32 s91, s91, 0xffff
s_add_u32 s62, s90, s91
buffer_load_dwordx4 v[208:211], v233, s[36:39], s62 offen sc1
s_mov_b64 exec, s[88:89]
buffer_load_dwordx4 v[216:219], v234, s[36:39], s62 offen offset:1536 sc1
buffer_load_dwordx4 v[212:215], v234, s[36:39], s62 offen offset:1024 sc1
s_mov_b64 exec, -1
s_add_u32 s91, s91, 2048
s_and_b32 s91, s91, 0xffff
ds_add_u32 v239, v240 offset:29440
v_mov_b32 v247, v239
s_lshl_b32 s63, s53, 8
v_add_u32 v239, s63, v239
v_xor_b32 v236, 0x100, v239
s_mov_b32 s63, 0xc2480000
s_waitcnt lgkmcnt(0)
s_waitcnt vmcnt(8)
v_mfma_f32_32x32x16_bf16 v[64:79], v[184:187], v[168:171], 0
v_mfma_f32_32x32x16_bf16 v[80:95], v[184:187], v[172:175], 0
v_mfma_f32_32x32x16_bf16 v[96:111], v[184:187], v[176:179], 0
v_mfma_f32_32x32x16_bf16 v[112:127], v[184:187], v[180:183], 0
s_nop 8
v_max3_f32 v244, v64, v65, v66
v_max3_f32 v244, v244, v67, v68
v_max3_f32 v244, v244, v69, v70
v_max3_f32 v244, v244, v71, v72
v_max3_f32 v244, v244, v73, v74
v_max3_f32 v244, v244, v75, v76
v_max3_f32 v244, v244, v77, v78
v_max_f32 v244, v244, v79
v_mov_b32 v251, v244
v_max3_f32 v245, v80, v81, v82
v_max3_f32 v245, v245, v83, v84
v_max3_f32 v245, v245, v85, v86
v_max3_f32 v245, v245, v87, v88
v_max3_f32 v245, v245, v89, v90
v_max3_f32 v245, v245, v91, v92
v_max3_f32 v245, v245, v93, v94
v_max_f32 v245, v245, v95
v_mov_b32 v252, v245
v_permlane32_swap_b32 v244, v251
v_max_f32 v244, v244, v251
v_fma_f32 v244, v244, -0.5, s63
v_cvt_pk_bf16_f32 v144, v244, 0
v_max3_f32 v246, v96, v97, v98
v_max3_f32 v246, v246, v99, v100
v_max3_f32 v246, v246, v101, v102
v_max3_f32 v246, v246, v103, v104
v_max3_f32 v246, v246, v105, v106
v_max3_f32 v246, v246, v107, v108
v_max3_f32 v246, v246, v109, v110
v_max_f32 v246, v246, v111
v_mov_b32 v240, v246
v_mfma_f32_32x32x16_bf16 v[64:79], v[220:223], v[144:147], 0
v_permlane32_swap_b32 v245, v252
v_max_f32 v245, v245, v252
v_fma_f32 v245, v245, -0.5, s63
v_cvt_pk_bf16_f32 v148, v245, 0
v_max3_f32 v250, v112, v113, v114
v_max3_f32 v250, v250, v115, v116
v_max3_f32 v250, v250, v117, v118
v_max3_f32 v250, v250, v119, v120
v_max3_f32 v250, v250, v121, v122
v_max3_f32 v250, v250, v123, v124
v_max3_f32 v250, v250, v125, v126
v_max_f32 v250, v250, v127
v_mov_b32 v241, v250
v_mfma_f32_32x32x16_bf16 v[80:95], v[220:223], v[148:151], 0
v_permlane32_swap_b32 v246, v240
v_max_f32 v246, v246, v240
v_fma_f32 v246, v246, -0.5, s63
v_cvt_pk_bf16_f32 v152, v246, 0
v_mfma_f32_32x32x16_bf16 v[128:143], v[184:187], v[168:171], v[64:79]
v_permlane32_swap_b32 v250, v241
v_max_f32 v250, v250, v241
v_fma_f32 v250, v250, -0.5, s63
v_cvt_pk_bf16_f32 v156, v250, 0
v_mfma_f32_32x32x16_bf16 v[96:111], v[220:223], v[152:155], 0
s_nop 0
v_mfma_f32_32x32x16_bf16 v[112:127], v[220:223], v[156:159], 0
.Lk_again:
s_mov_b32 s92, 4
s_waitcnt vmcnt(6)
s_add_u32 s62, s90, s91
buffer_load_dwordx4 v[220:223], v233, s[36:39], s62 offen sc1
s_nop 1
.Lk_loop:
v_mfma_f32_32x32x16_bf16 v[144:159], v[184:187], v[172:175], v[80:95]
v_exp_f32 v128, v128
v_exp_f32 v129, v129
v_exp_f32 v130, v130
v_smfmac_f32_32x32x32_bf16 v[48:63], v[228:231], v[160:167], v235
s_mov_b64 exec, s[88:89]
buffer_load_dwordx4 v[228:231], v234, s[36:39], s62 offen offset:1536 sc1
buffer_load_dwordx4 v[224:227], v234, s[36:39], s62 offen offset:1024 sc1
s_mov_b64 exec, -1
v_exp_f32 v131, v131
v_exp_f32 v132, v132
v_exp_f32 v133, v133
v_exp_f32 v134, v134
v_exp_f32 v135, v135
v_exp_f32 v160, v136
v_exp_f32 v161, v137
v_exp_f32 v162, v138
v_exp_f32 v163, v139
v_exp_f32 v164, v140
v_smfmac_f32_32x32x32_bf16 v[0:15], v[188:191], v[128:135], v235
s_add_u32 s91, s91, 2048
s_and_b32 s91, s91, 0xffff
v_exp_f32 v165, v141
v_exp_f32 v166, v142
v_exp_f32 v167, v143
v_mfma_f32_32x32x16_bf16 v[128:143], v[184:187], v[176:179], v[96:111]
v_exp_f32 v144, v144
v_exp_f32 v145, v145
v_exp_f32 v146, v146
v_smfmac_f32_32x32x32_bf16 v[0:15], v[192:195], v[160:167], v235
v_exp_f32 v147, v147
v_exp_f32 v148, v148
v_exp_f32 v149, v149
v_exp_f32 v150, v150
v_exp_f32 v151, v151
v_exp_f32 v160, v152
v_exp_f32 v161, v153
v_exp_f32 v162, v154
v_exp_f32 v163, v155
v_exp_f32 v164, v156
v_smfmac_f32_32x32x32_bf16 v[16:31], v[188:191], v[144:151], v235
v_exp_f32 v165, v157
v_exp_f32 v166, v158
v_exp_f32 v167, v159
v_mfma_f32_32x32x16_bf16 v[144:159], v[184:187], v[180:183], v[112:127]
v_exp_f32 v128, v128
v_exp_f32 v129, v129
v_exp_f32 v130, v130
v_smfmac_f32_32x32x32_bf16 v[16:31], v[192:195], v[160:167], v235
v_exp_f32 v131, v131
v_exp_f32 v132, v132
v_exp_f32 v133, v133
v_exp_f32 v134, v134
v_exp_f32 v135, v135
v_exp_f32 v160, v136
v_exp_f32 v161, v137
v_exp_f32 v162, v138
v_exp_f32 v163, v139
v_exp_f32 v164, v140
v_smfmac_f32_32x32x32_bf16 v[32:47], v[188:191], v[128:135], v235
s_waitcnt vmcnt(6)
v_exp_f32 v165, v141
v_exp_f32 v166, v142
v_exp_f32 v167, v143
v_mfma_f32_32x32x16_bf16 v[128:143], v[196:199], v[168:171], v[64:79]
s_add_u32 s62, s90, s91
v_exp_f32 v144, v144
v_exp_f32 v145, v145
v_exp_f32 v146, v146
v_smfmac_f32_32x32x32_bf16 v[32:47], v[192:195], v[160:167], v235
buffer_load_dwordx4 v[184:187], v233, s[36:39], s62 offen sc1
v_exp_f32 v147, v147
v_exp_f32 v148, v148
v_exp_f32 v149, v149
v_exp_f32 v150, v150
v_exp_f32 v151, v151
v_exp_f32 v160, v152
)ASM" R"ASM(
v_exp_f32 v161, v153
v_exp_f32 v162, v154
v_exp_f32 v163, v155
v_exp_f32 v164, v156
v_smfmac_f32_32x32x32_bf16 v[48:63], v[188:191], v[144:151], v235
v_exp_f32 v165, v157
v_exp_f32 v166, v158
v_exp_f32 v167, v159
v_mfma_f32_32x32x16_bf16 v[144:159], v[196:199], v[172:175], v[80:95]
v_exp_f32 v128, v128
v_exp_f32 v129, v129
v_exp_f32 v130, v130
v_smfmac_f32_32x32x32_bf16 v[48:63], v[192:195], v[160:167], v235
s_mov_b64 exec, s[88:89]
buffer_load_dwordx4 v[192:195], v234, s[36:39], s62 offen offset:1536 sc1
buffer_load_dwordx4 v[188:191], v234, s[36:39], s62 offen offset:1024 sc1
s_mov_b64 exec, -1
v_exp_f32 v131, v131
v_exp_f32 v132, v132
v_exp_f32 v133, v133
v_exp_f32 v134, v134
v_exp_f32 v135, v135
v_exp_f32 v160, v136
v_exp_f32 v161, v137
v_exp_f32 v162, v138
v_exp_f32 v163, v139
v_exp_f32 v164, v140
v_smfmac_f32_32x32x32_bf16 v[0:15], v[200:203], v[128:135], v235
s_add_u32 s91, s91, 2048
s_and_b32 s91, s91, 0xffff
v_exp_f32 v165, v141
v_exp_f32 v166, v142
v_exp_f32 v167, v143
v_mfma_f32_32x32x16_bf16 v[128:143], v[196:199], v[176:179], v[96:111]
v_exp_f32 v144, v144
v_exp_f32 v145, v145
v_exp_f32 v146, v146
v_smfmac_f32_32x32x32_bf16 v[0:15], v[204:207], v[160:167], v235
v_exp_f32 v147, v147
v_exp_f32 v148, v148
v_exp_f32 v149, v149
v_exp_f32 v150, v150
v_exp_f32 v151, v151
v_exp_f32 v160, v152
v_exp_f32 v161, v153
v_exp_f32 v162, v154
v_exp_f32 v163, v155
v_exp_f32 v164, v156
v_smfmac_f32_32x32x32_bf16 v[16:31], v[200:203], v[144:151], v235
v_exp_f32 v165, v157
v_exp_f32 v166, v158
v_exp_f32 v167, v159
v_mfma_f32_32x32x16_bf16 v[144:159], v[196:199], v[180:183], v[112:127]
v_exp_f32 v128, v128
v_exp_f32 v129, v129
v_exp_f32 v130, v130
v_smfmac_f32_32x32x32_bf16 v[16:31], v[204:207], v[160:167], v235
v_exp_f32 v131, v131
v_exp_f32 v132, v132
v_exp_f32 v133, v133
v_exp_f32 v134, v134
v_exp_f32 v135, v135
v_exp_f32 v160, v136
v_exp_f32 v161, v137
v_exp_f32 v162, v138
v_exp_f32 v163, v139
v_exp_f32 v164, v140
v_smfmac_f32_32x32x32_bf16 v[32:47], v[200:203], v[128:135], v235
s_waitcnt vmcnt(6)
v_exp_f32 v165, v141
v_exp_f32 v166, v142
v_exp_f32 v167, v143
v_mfma_f32_32x32x16_bf16 v[128:143], v[208:211], v[168:171], v[64:79]
s_add_u32 s62, s90, s91
v_exp_f32 v144, v144
v_exp_f32 v145, v145
v_exp_f32 v146, v146
v_smfmac_f32_32x32x32_bf16 v[32:47], v[204:207], v[160:167], v235
buffer_load_dwordx4 v[196:199], v233, s[36:39], s62 offen sc1
v_exp_f32 v147, v147
v_exp_f32 v148, v148
v_exp_f32 v149, v149
v_exp_f32 v150, v150
v_exp_f32 v151, v151
v_exp_f32 v160, v152
v_exp_f32 v161, v153
v_exp_f32 v162, v154
v_exp_f32 v163, v155
v_exp_f32 v164, v156
v_smfmac_f32_32x32x32_bf16 v[48:63], v[200:203], v[144:151], v235
v_exp_f32 v165, v157
v_exp_f32 v166, v158
v_exp_f32 v167, v159
v_mfma_f32_32x32x16_bf16 v[144:159], v[208:211], v[172:175], v[80:95]
v_exp_f32 v128, v128
v_exp_f32 v129, v129
v_exp_f32 v130, v130
v_smfmac_f32_32x32x32_bf16 v[48:63], v[204:207], v[160:167], v235
s_mov_b64 exec, s[88:89]
buffer_load_dwordx4 v[204:207], v234, s[36:39], s62 offen offset:1536 sc1
buffer_load_dwordx4 v[200:203], v234, s[36:39], s62 offen offset:1024 sc1
s_mov_b64 exec, -1
v_exp_f32 v131, v131
v_exp_f32 v132, v132
v_exp_f32 v133, v133
v_exp_f32 v134, v134
v_exp_f32 v135, v135
v_exp_f32 v160, v136
v_exp_f32 v161, v137
v_exp_f32 v162, v138
v_exp_f32 v163, v139
v_exp_f32 v164, v140
v_smfmac_f32_32x32x32_bf16 v[0:15], v[212:215], v[128:135], v235
s_add_u32 s91, s91, 2048
s_and_b32 s91, s91, 0xffff
v_exp_f32 v165, v141
v_exp_f32 v166, v142
v_exp_f32 v167, v143
v_mfma_f32_32x32x16_bf16 v[128:143], v[208:211], v[176:179], v[96:111]
v_exp_f32 v144, v144
v_exp_f32 v145, v145
v_exp_f32 v146, v146
v_smfmac_f32_32x32x32_bf16 v[0:15], v[216:219], v[160:167], v235
v_exp_f32 v147, v147
v_exp_f32 v148, v148
v_exp_f32 v149, v149
v_exp_f32 v150, v150
v_exp_f32 v151, v151
v_exp_f32 v160, v152
v_exp_f32 v161, v153
v_exp_f32 v162, v154
v_exp_f32 v163, v155
v_exp_f32 v164, v156
v_smfmac_f32_32x32x32_bf16 v[16:31], v[212:215], v[144:151], v235
v_exp_f32 v165, v157
v_exp_f32 v166, v158
v_exp_f32 v167, v159
v_mfma_f32_32x32x16_bf16 v[144:159], v[208:211], v[180:183], v[112:127]
v_exp_f32 v128, v128
v_exp_f32 v129, v129
v_exp_f32 v130, v130
v_smfmac_f32_32x32x32_bf16 v[16:31], v[216:219], v[160:167], v235
v_exp_f32 v131, v131
v_exp_f32 v132, v132
v_exp_f32 v133, v133
v_exp_f32 v134, v134
v_exp_f32 v135, v135
v_exp_f32 v160, v136
v_exp_f32 v161, v137
v_exp_f32 v162, v138
v_exp_f32 v163, v139
v_exp_f32 v164, v140
v_smfmac_f32_32x32x32_bf16 v[32:47], v[212:215], v[128:135], v235
s_waitcnt vmcnt(6)
v_exp_f32 v165, v141
v_exp_f32 v166, v142
v_exp_f32 v167, v143
v_mfma_f32_32x32x16_bf16 v[128:143], v[220:223], v[168:171], v[64:79]
s_add_u32 s62, s90, s91
v_exp_f32 v144, v144
v_exp_f32 v145, v145
v_exp_f32 v146, v146
v_smfmac_f32_32x32x32_bf16 v[32:47], v[216:219], v[160:167], v235
buffer_load_dwordx4 v[208:211], v233, s[36:39], s62 offen sc1
v_exp_f32 v147, v147
v_exp_f32 v148, v148
v_exp_f32 v149, v149
v_exp_f32 v150, v150
v_exp_f32 v151, v151
v_exp_f32 v160, v152
v_exp_f32 v161, v153
v_exp_f32 v162, v154
v_exp_f32 v163, v155
v_exp_f32 v164, v156
v_smfmac_f32_32x32x32_bf16 v[48:63], v[212:215], v[144:151], v235
v_exp_f32 v165, v157
v_exp_f32 v166, v158
v_exp_f32 v167, v159
v_mfma_f32_32x32x16_bf16 v[144:159], v[220:223], v[172:175], v[80:95]
v_exp_f32 v128, v128
v_exp_f32 v129, v129
v_exp_f32 v130, v130
v_smfmac_f32_32x32x32_bf16 v[48:63], v[216:219], v[160:167], v235
s_mov_b64 exec, s[88:89]
buffer_load_dwordx4 v[216:219], v234, s[36:39], s62 offen offset:1536 sc1
buffer_load_dwordx4 v[212:215], v234, s[36:39], s62 offen offset:1024 sc1
s_mov_b64 exec, -1
v_exp_f32 v131, v131
v_exp_f32 v132, v132
v_exp_f32 v133, v133
v_exp_f32 v134, v134
v_exp_f32 v135, v135
v_exp_f32 v160, v136
v_exp_f32 v161, v137
v_exp_f32 v162, v138
v_exp_f32 v163, v139
v_exp_f32 v164, v140
v_smfmac_f32_32x32x32_bf16 v[0:15], v[224:227], v[128:135], v235
s_add_u32 s91, s91, 2048
s_and_b32 s91, s91, 0xffff
)ASM" R"ASM(
v_exp_f32 v165, v141
v_exp_f32 v166, v142
v_exp_f32 v167, v143
v_mfma_f32_32x32x16_bf16 v[128:143], v[220:223], v[176:179], v[96:111]
v_exp_f32 v144, v144
v_exp_f32 v145, v145
v_exp_f32 v146, v146
v_smfmac_f32_32x32x32_bf16 v[0:15], v[228:231], v[160:167], v235
v_exp_f32 v147, v147
v_exp_f32 v148, v148
v_exp_f32 v149, v149
v_exp_f32 v150, v150
v_exp_f32 v151, v151
v_exp_f32 v160, v152
v_exp_f32 v161, v153
v_exp_f32 v162, v154
v_exp_f32 v163, v155
v_exp_f32 v164, v156
v_smfmac_f32_32x32x32_bf16 v[16:31], v[224:227], v[144:151], v235
v_exp_f32 v165, v157
v_exp_f32 v166, v158
v_exp_f32 v167, v159
v_mfma_f32_32x32x16_bf16 v[144:159], v[220:223], v[180:183], v[112:127]
v_exp_f32 v128, v128
v_exp_f32 v129, v129
v_exp_f32 v130, v130
v_smfmac_f32_32x32x32_bf16 v[16:31], v[228:231], v[160:167], v235
v_exp_f32 v131, v131
v_exp_f32 v132, v132
v_exp_f32 v133, v133
v_exp_f32 v134, v134
v_exp_f32 v135, v135
v_exp_f32 v160, v136
v_exp_f32 v161, v137
v_exp_f32 v162, v138
v_exp_f32 v163, v139
v_exp_f32 v164, v140
v_smfmac_f32_32x32x32_bf16 v[32:47], v[224:227], v[128:135], v235
s_waitcnt vmcnt(6)
v_exp_f32 v165, v141
v_exp_f32 v166, v142
v_exp_f32 v167, v143
v_mfma_f32_32x32x16_bf16 v[128:143], v[184:187], v[168:171], v[64:79]
s_add_u32 s62, s90, s91
v_exp_f32 v144, v144
v_exp_f32 v145, v145
v_exp_f32 v146, v146
v_smfmac_f32_32x32x32_bf16 v[32:47], v[228:231], v[160:167], v235
buffer_load_dwordx4 v[220:223], v233, s[36:39], s62 offen sc1
v_exp_f32 v147, v147
v_exp_f32 v148, v148
v_exp_f32 v149, v149
v_exp_f32 v150, v150
v_exp_f32 v151, v151
v_exp_f32 v160, v152
v_exp_f32 v161, v153
v_exp_f32 v162, v154
v_exp_f32 v163, v155
v_exp_f32 v164, v156
v_smfmac_f32_32x32x32_bf16 v[48:63], v[224:227], v[144:151], v235
v_exp_f32 v165, v157
v_exp_f32 v166, v158
v_exp_f32 v167, v159
v_mfma_f32_32x32x16_bf16 v[144:159], v[184:187], v[172:175], v[80:95]
v_exp_f32 v128, v128
v_exp_f32 v129, v129
v_exp_f32 v130, v130
v_smfmac_f32_32x32x32_bf16 v[48:63], v[228:231], v[160:167], v235
s_mov_b64 exec, s[88:89]
buffer_load_dwordx4 v[228:231], v234, s[36:39], s62 offen offset:1536 sc1
buffer_load_dwordx4 v[224:227], v234, s[36:39], s62 offen offset:1024 sc1
s_mov_b64 exec, -1
v_exp_f32 v131, v131
v_exp_f32 v132, v132
v_exp_f32 v133, v133
v_exp_f32 v134, v134
v_exp_f32 v135, v135
v_exp_f32 v160, v136
v_exp_f32 v161, v137
v_exp_f32 v162, v138
v_exp_f32 v163, v139
v_exp_f32 v164, v140
v_smfmac_f32_32x32x32_bf16 v[0:15], v[188:191], v[128:135], v235
s_add_u32 s91, s91, 2048
s_and_b32 s91, s91, 0xffff
v_exp_f32 v165, v141
v_exp_f32 v166, v142
v_exp_f32 v167, v143
v_mfma_f32_32x32x16_bf16 v[128:143], v[184:187], v[176:179], v[96:111]
v_exp_f32 v144, v144
v_exp_f32 v145, v145
v_exp_f32 v146, v146
v_smfmac_f32_32x32x32_bf16 v[0:15], v[192:195], v[160:167], v235
s_cmp_eq_u32 s92, 1
s_cselect_b32 s38, 0, 14680064
v_exp_f32 v147, v147
v_exp_f32 v148, v148
v_exp_f32 v149, v149
v_exp_f32 v150, v150
v_exp_f32 v151, v151
v_exp_f32 v160, v152
v_exp_f32 v161, v153
v_exp_f32 v162, v154
v_exp_f32 v163, v155
v_exp_f32 v164, v156
v_smfmac_f32_32x32x32_bf16 v[16:31], v[188:191], v[144:151], v235
v_exp_f32 v165, v157
v_exp_f32 v166, v158
v_exp_f32 v167, v159
v_mfma_f32_32x32x16_bf16 v[144:159], v[184:187], v[180:183], v[112:127]
v_exp_f32 v128, v128
v_exp_f32 v129, v129
v_exp_f32 v130, v130
v_smfmac_f32_32x32x32_bf16 v[16:31], v[192:195], v[160:167], v235
v_exp_f32 v131, v131
v_exp_f32 v132, v132
v_exp_f32 v133, v133
v_exp_f32 v134, v134
v_exp_f32 v135, v135
v_exp_f32 v160, v136
v_exp_f32 v161, v137
v_exp_f32 v162, v138
v_exp_f32 v163, v139
v_exp_f32 v164, v140
v_smfmac_f32_32x32x32_bf16 v[32:47], v[188:191], v[128:135], v235
s_waitcnt vmcnt(6)
v_exp_f32 v165, v141
v_exp_f32 v166, v142
v_exp_f32 v167, v143
v_mfma_f32_32x32x16_bf16 v[128:143], v[196:199], v[168:171], v[64:79]
s_add_u32 s62, s90, s91
v_exp_f32 v144, v144
v_exp_f32 v145, v145
v_exp_f32 v146, v146
v_smfmac_f32_32x32x32_bf16 v[32:47], v[192:195], v[160:167], v235
buffer_load_dwordx4 v[184:187], v233, s[36:39], s62 offen sc1
v_exp_f32 v147, v147
v_exp_f32 v148, v148
v_exp_f32 v149, v149
v_exp_f32 v150, v150
v_exp_f32 v151, v151
v_exp_f32 v160, v152
v_exp_f32 v161, v153
v_exp_f32 v162, v154
v_exp_f32 v163, v155
v_exp_f32 v164, v156
v_smfmac_f32_32x32x32_bf16 v[48:63], v[188:191], v[144:151], v235
v_exp_f32 v165, v157
v_exp_f32 v166, v158
v_exp_f32 v167, v159
v_mfma_f32_32x32x16_bf16 v[144:159], v[196:199], v[172:175], v[80:95]
v_exp_f32 v128, v128
v_exp_f32 v129, v129
v_exp_f32 v130, v130
v_smfmac_f32_32x32x32_bf16 v[48:63], v[192:195], v[160:167], v235
s_mov_b64 exec, s[88:89]
buffer_load_dwordx4 v[192:195], v234, s[36:39], s62 offen offset:1536 sc1
buffer_load_dwordx4 v[188:191], v234, s[36:39], s62 offen offset:1024 sc1
s_mov_b64 exec, -1
v_exp_f32 v131, v131
v_exp_f32 v132, v132
v_exp_f32 v133, v133
v_exp_f32 v134, v134
v_exp_f32 v135, v135
v_exp_f32 v160, v136
v_exp_f32 v161, v137
v_exp_f32 v162, v138
v_exp_f32 v163, v139
v_exp_f32 v164, v140
v_smfmac_f32_32x32x32_bf16 v[0:15], v[200:203], v[128:135], v235
s_add_u32 s91, s91, 2048
s_and_b32 s91, s91, 0xffff
v_exp_f32 v165, v141
v_exp_f32 v166, v142
v_exp_f32 v167, v143
v_mfma_f32_32x32x16_bf16 v[128:143], v[196:199], v[176:179], v[96:111]
v_exp_f32 v144, v144
v_exp_f32 v145, v145
v_exp_f32 v146, v146
v_smfmac_f32_32x32x32_bf16 v[0:15], v[204:207], v[160:167], v235
v_exp_f32 v147, v147
v_exp_f32 v148, v148
v_exp_f32 v149, v149
v_exp_f32 v150, v150
v_exp_f32 v151, v151
v_exp_f32 v160, v152
v_exp_f32 v161, v153
v_exp_f32 v162, v154
v_exp_f32 v163, v155
v_exp_f32 v164, v156
v_smfmac_f32_32x32x32_bf16 v[16:31], v[200:203], v[144:151], v235
v_exp_f32 v165, v157
v_exp_f32 v166, v158
v_exp_f32 v167, v159
v_mfma_f32_32x32x16_bf16 v[144:159], v[196:199], v[180:183], v[112:127]
v_exp_f32 v128, v128
v_exp_f32 v129, v129
v_exp_f32 v130, v130
v_smfmac_f32_32x32x32_bf16 v[16:31], v[204:207], v[160:167], v235
v_exp_f32 v131, v131
)ASM" R"ASM(
v_exp_f32 v132, v132
v_exp_f32 v133, v133
v_exp_f32 v134, v134
v_exp_f32 v135, v135
v_exp_f32 v160, v136
v_exp_f32 v161, v137
v_exp_f32 v162, v138
v_exp_f32 v163, v139
v_exp_f32 v164, v140
v_smfmac_f32_32x32x32_bf16 v[32:47], v[200:203], v[128:135], v235
s_waitcnt vmcnt(6)
v_exp_f32 v165, v141
v_exp_f32 v166, v142
v_exp_f32 v167, v143
v_mfma_f32_32x32x16_bf16 v[128:143], v[208:211], v[168:171], v[64:79]
s_add_u32 s62, s90, s91
v_exp_f32 v144, v144
v_exp_f32 v145, v145
v_exp_f32 v146, v146
v_smfmac_f32_32x32x32_bf16 v[32:47], v[204:207], v[160:167], v235
buffer_load_dwordx4 v[196:199], v233, s[36:39], s62 offen sc1
v_exp_f32 v147, v147
v_exp_f32 v148, v148
v_exp_f32 v149, v149
v_exp_f32 v150, v150
v_exp_f32 v151, v151
v_exp_f32 v160, v152
v_exp_f32 v161, v153
v_exp_f32 v162, v154
v_exp_f32 v163, v155
v_exp_f32 v164, v156
v_smfmac_f32_32x32x32_bf16 v[48:63], v[200:203], v[144:151], v235
v_exp_f32 v165, v157
v_exp_f32 v166, v158
v_exp_f32 v167, v159
v_mfma_f32_32x32x16_bf16 v[144:159], v[208:211], v[172:175], v[80:95]
v_exp_f32 v128, v128
v_exp_f32 v129, v129
v_exp_f32 v130, v130
v_smfmac_f32_32x32x32_bf16 v[48:63], v[204:207], v[160:167], v235
s_mov_b64 exec, s[88:89]
buffer_load_dwordx4 v[204:207], v234, s[36:39], s62 offen offset:1536 sc1
buffer_load_dwordx4 v[200:203], v234, s[36:39], s62 offen offset:1024 sc1
s_mov_b64 exec, -1
v_exp_f32 v131, v131
v_exp_f32 v132, v132
v_exp_f32 v133, v133
v_exp_f32 v134, v134
v_exp_f32 v135, v135
v_exp_f32 v160, v136
v_exp_f32 v161, v137
v_exp_f32 v162, v138
v_exp_f32 v163, v139
v_exp_f32 v164, v140
v_smfmac_f32_32x32x32_bf16 v[0:15], v[212:215], v[128:135], v235
s_add_u32 s91, s91, 2048
s_and_b32 s91, s91, 0xffff
v_exp_f32 v165, v141
v_exp_f32 v166, v142
v_exp_f32 v167, v143
v_mfma_f32_32x32x16_bf16 v[128:143], v[208:211], v[176:179], v[96:111]
v_exp_f32 v144, v144
v_exp_f32 v145, v145
v_exp_f32 v146, v146
v_smfmac_f32_32x32x32_bf16 v[0:15], v[216:219], v[160:167], v235
v_exp_f32 v147, v147
v_exp_f32 v148, v148
v_exp_f32 v149, v149
v_exp_f32 v150, v150
v_exp_f32 v151, v151
v_exp_f32 v160, v152
v_exp_f32 v161, v153
v_exp_f32 v162, v154
v_exp_f32 v163, v155
v_exp_f32 v164, v156
v_smfmac_f32_32x32x32_bf16 v[16:31], v[212:215], v[144:151], v235
v_exp_f32 v165, v157
v_exp_f32 v166, v158
v_exp_f32 v167, v159
v_mfma_f32_32x32x16_bf16 v[144:159], v[208:211], v[180:183], v[112:127]
v_exp_f32 v128, v128
v_exp_f32 v129, v129
v_exp_f32 v130, v130
v_smfmac_f32_32x32x32_bf16 v[16:31], v[216:219], v[160:167], v235
v_exp_f32 v131, v131
v_exp_f32 v132, v132
v_exp_f32 v133, v133
v_exp_f32 v134, v134
v_exp_f32 v135, v135
v_exp_f32 v160, v136
v_exp_f32 v161, v137
v_exp_f32 v162, v138
v_exp_f32 v163, v139
v_exp_f32 v164, v140
v_smfmac_f32_32x32x32_bf16 v[32:47], v[212:215], v[128:135], v235
s_waitcnt vmcnt(6)
v_exp_f32 v165, v141
v_exp_f32 v166, v142
v_exp_f32 v167, v143
v_mfma_f32_32x32x16_bf16 v[128:143], v[220:223], v[168:171], v[64:79]
s_add_u32 s62, s90, s91
v_exp_f32 v144, v144
v_exp_f32 v145, v145
v_exp_f32 v146, v146
v_smfmac_f32_32x32x32_bf16 v[32:47], v[216:219], v[160:167], v235
buffer_load_dwordx4 v[208:211], v233, s[36:39], s62 offen sc1
v_exp_f32 v147, v147
v_exp_f32 v148, v148
v_exp_f32 v149, v149
v_exp_f32 v150, v150
v_exp_f32 v151, v151
v_exp_f32 v160, v152
v_exp_f32 v161, v153
v_exp_f32 v162, v154
v_exp_f32 v163, v155
v_exp_f32 v164, v156
v_smfmac_f32_32x32x32_bf16 v[48:63], v[212:215], v[144:151], v235
v_exp_f32 v165, v157
v_exp_f32 v166, v158
v_exp_f32 v167, v159
v_mfma_f32_32x32x16_bf16 v[144:159], v[220:223], v[172:175], v[80:95]
v_exp_f32 v128, v128
v_exp_f32 v129, v129
v_exp_f32 v130, v130
v_smfmac_f32_32x32x32_bf16 v[48:63], v[216:219], v[160:167], v235
s_mov_b64 exec, s[88:89]
buffer_load_dwordx4 v[216:219], v234, s[36:39], s62 offen offset:1536 sc1
buffer_load_dwordx4 v[212:215], v234, s[36:39], s62 offen offset:1024 sc1
s_mov_b64 exec, -1
v_exp_f32 v131, v131
v_exp_f32 v132, v132
v_exp_f32 v133, v133
v_exp_f32 v134, v134
v_exp_f32 v135, v135
v_exp_f32 v160, v136
v_exp_f32 v161, v137
v_exp_f32 v162, v138
v_exp_f32 v163, v139
v_exp_f32 v164, v140
v_smfmac_f32_32x32x32_bf16 v[0:15], v[224:227], v[128:135], v235
s_add_u32 s91, s91, 2048
s_and_b32 s91, s91, 0xffff
v_exp_f32 v165, v141
v_exp_f32 v166, v142
v_exp_f32 v167, v143
v_mfma_f32_32x32x16_bf16 v[128:143], v[220:223], v[176:179], v[96:111]
v_exp_f32 v144, v144
v_exp_f32 v145, v145
v_exp_f32 v146, v146
v_smfmac_f32_32x32x32_bf16 v[0:15], v[228:231], v[160:167], v235
v_exp_f32 v147, v147
v_exp_f32 v148, v148
v_exp_f32 v149, v149
v_exp_f32 v150, v150
v_exp_f32 v151, v151
v_exp_f32 v160, v152
v_exp_f32 v161, v153
v_exp_f32 v162, v154
v_exp_f32 v163, v155
v_exp_f32 v164, v156
v_smfmac_f32_32x32x32_bf16 v[16:31], v[224:227], v[144:151], v235
v_exp_f32 v165, v157
v_exp_f32 v166, v158
v_exp_f32 v167, v159
v_mfma_f32_32x32x16_bf16 v[144:159], v[220:223], v[180:183], v[112:127]
v_exp_f32 v128, v128
v_exp_f32 v129, v129
v_exp_f32 v130, v130
v_smfmac_f32_32x32x32_bf16 v[16:31], v[228:231], v[160:167], v235
v_exp_f32 v131, v131
v_exp_f32 v132, v132
v_exp_f32 v133, v133
v_exp_f32 v134, v134
v_exp_f32 v135, v135
v_exp_f32 v160, v136
v_exp_f32 v161, v137
v_exp_f32 v162, v138
v_exp_f32 v163, v139
v_exp_f32 v164, v140
v_smfmac_f32_32x32x32_bf16 v[32:47], v[224:227], v[128:135], v235
s_waitcnt vmcnt(6)
v_exp_f32 v165, v141
v_exp_f32 v166, v142
v_exp_f32 v167, v143
v_mfma_f32_32x32x16_bf16 v[128:143], v[184:187], v[168:171], v[64:79]
s_add_u32 s62, s90, s91
v_exp_f32 v144, v144
v_exp_f32 v145, v145
v_exp_f32 v146, v146
v_smfmac_f32_32x32x32_bf16 v[32:47], v[228:231], v[160:167], v235
buffer_load_dwordx4 v[220:223], v233, s[36:39], s62 offen sc1
v_exp_f32 v147, v147
v_exp_f32 v148, v148
v_exp_f32 v149, v149
v_exp_f32 v150, v150
v_exp_f32 v151, v151
v_exp_f32 v160, v152
v_exp_f32 v161, v153
v_exp_f32 v162, v154
v_exp_f32 v163, v155
)ASM" R"ASM(
v_exp_f32 v164, v156
v_smfmac_f32_32x32x32_bf16 v[48:63], v[224:227], v[144:151], v235
s_sub_u32 s92, s92, 1
s_cmp_lg_u32 s92, 0
v_exp_f32 v165, v157
v_exp_f32 v166, v158
v_exp_f32 v167, v159
s_cbranch_scc1 .Lk_loop
s_waitcnt lgkmcnt(0)
v_mov_b32 v237, 1
v_smfmac_f32_32x32x32_bf16 v[48:63], v[228:231], v[160:167], v235
v_mov_b32 v9, v64
v_mov_b32 v10, v24
v_mov_b32 v11, v80
ds_write_b128 v242, v[0:3] offset:0
ds_write_b128 v242, v[4:7] offset:1024
ds_write_b128 v242, v[16:19] offset:2048
ds_write_b128 v242, v[20:23] offset:3072
ds_write_b128 v242, v[8:11] offset:4096
v_maximum3_f32 v12, abs(v0), abs(v1), abs(v2)
v_maximum3_f32 v13, abs(v3), abs(v4), abs(v5)
v_maximum3_f32 v12, abs(v6), abs(v7), v12
v_maximum3_f32 v12, v12, v13, v13
v_maximum3_f32 v28, abs(v16), abs(v17), abs(v18)
v_maximum3_f32 v29, abs(v19), abs(v20), abs(v21)
v_maximum3_f32 v28, abs(v22), abs(v23), v28
v_maximum3_f32 v28, v28, v29, v29
v_maximum3_f32 v44, abs(v32), abs(v33), abs(v34)
v_maximum3_f32 v45, abs(v35), abs(v36), abs(v37)
v_maximum3_f32 v44, abs(v38), abs(v39), v44
v_maximum3_f32 v44, v44, v45, v45
v_maximum3_f32 v60, abs(v48), abs(v49), abs(v50)
v_maximum3_f32 v61, abs(v51), abs(v52), abs(v53)
v_maximum3_f32 v60, abs(v54), abs(v55), v60
v_maximum3_f32 v60, v60, v61, v61
v_maximum3_f32 v128, v8, v24, v40
v_maximum3_f32 v128, v128, v56, v56
v_minimum3_f32 v129, v8, v24, v40
v_minimum3_f32 v129, v129, v56, v56
v_maximum3_f32 v130, v12, v28, v44
v_maximum3_f32 v130, v130, v60, v60
v_minimum3_f32 v131, v12, v28, v44
v_minimum3_f32 v131, v131, v60, v60
v_cmp_nge_f32 vcc, 0x71800000, v128
v_log_f32 v41, v40
v_log_f32 v57, v56
s_mov_b64 s[64:65], vcc
v_cmp_nle_f32 vcc, 0x0c800000, v129
v_sub_f32 v41, v41, v96
v_sub_f32 v57, v57, v112
s_or_b64 s[64:65], s[64:65], vcc
v_cmp_nge_f32 vcc, 0x7b000000, v130
s_nop 1
s_or_b64 s[64:65], s[64:65], vcc
v_cmp_nle_f32 vcc, 0x04000000, v131
s_nop 1
s_or_b64 s[64:65], s[64:65], vcc
s_cbranch_scc1 .Lk_fb
.Lk_ok:
ds_write_b32 v239, v237 offset:29696
s_mov_b32 s68, 2000000
.Lk_mg:
ds_read_b32 v240, v236 offset:29696
ds_read_b128 v[128:131], v243 offset:4096
ds_read_b128 v[0:3], v243 offset:0
ds_read_b128 v[4:7], v243 offset:1024
ds_read_b128 v[16:19], v243 offset:2048
ds_read_b128 v[20:23], v243 offset:3072
s_waitcnt lgkmcnt(5)
v_cmp_gt_u32 vcc, 1, v240
s_sub_u32 s68, s68, 1
s_cbranch_scc1 .Lk_mh
s_cbranch_vccnz .Lk_mg
.Lk_mh:
s_waitcnt lgkmcnt(4)
v_log_f32 v132, v128
v_log_f32 v133, v130
v_sub_f32 v132, v132, v129
v_sub_f32 v133, v133, v131
v_max_f32 v132, v132, v41
v_max_f32 v133, v133, v57
v_add_f32 v134, v132, v96
v_add_f32 v136, v132, v129
v_add_f32 v135, v133, v112
v_add_f32 v137, v133, v131
v_exp_f32 v134, -v134
v_exp_f32 v135, -v135
v_exp_f32 v136, -v136
v_exp_f32 v137, -v137
v_mul_f32 v132, v134, v40
v_mul_f32 v133, v135, v56
v_fmac_f32 v132, v136, v128
v_fmac_f32 v133, v137, v130
v_rcp_f32 v132, v132
v_rcp_f32 v133, v133
v_mul_f32 v138, v134, v132
v_mul_f32 v140, v136, v132
v_mul_f32 v139, v134, v132
v_mul_f32 v141, v136, v132
v_mul_f32 v142, v135, v133
v_mul_f32 v144, v137, v133
v_mul_f32 v143, v135, v133
v_mul_f32 v145, v137, v133
v_pk_mul_f32 v[32:33], v[32:33], v[138:139]
v_pk_mul_f32 v[34:35], v[34:35], v[138:139]
v_pk_mul_f32 v[36:37], v[36:37], v[138:139]
v_pk_mul_f32 v[38:39], v[38:39], v[138:139]
s_waitcnt lgkmcnt(2)
v_pk_fma_f32 v[32:33], v[0:1], v[140:141], v[32:33]
v_pk_fma_f32 v[34:35], v[2:3], v[140:141], v[34:35]
v_pk_fma_f32 v[36:37], v[4:5], v[140:141], v[36:37]
v_pk_fma_f32 v[38:39], v[6:7], v[140:141], v[38:39]
v_cvt_pk_bf16_f32 v32, v32, v33
v_cvt_pk_bf16_f32 v33, v34, v35
v_cvt_pk_bf16_f32 v34, v36, v37
v_cvt_pk_bf16_f32 v35, v38, v39
buffer_store_dwordx4 v[32:35], v233, s[48:51], s97 offen sc1
v_pk_mul_f32 v[48:49], v[48:49], v[142:143]
v_pk_mul_f32 v[50:51], v[50:51], v[142:143]
v_pk_mul_f32 v[52:53], v[52:53], v[142:143]
v_pk_mul_f32 v[54:55], v[54:55], v[142:143]
s_waitcnt lgkmcnt(0)
v_pk_fma_f32 v[48:49], v[16:17], v[144:145], v[48:49]
v_pk_fma_f32 v[50:51], v[18:19], v[144:145], v[50:51]
v_pk_fma_f32 v[52:53], v[20:21], v[144:145], v[52:53]
v_pk_fma_f32 v[54:55], v[22:23], v[144:145], v[54:55]
v_cvt_pk_bf16_f32 v48, v48, v49
v_cvt_pk_bf16_f32 v49, v50, v51
v_cvt_pk_bf16_f32 v50, v52, v53
v_cvt_pk_bf16_f32 v51, v54, v55
buffer_store_dwordx4 v[48:51], v233, s[48:51], s5 offen sc1
ds_write_b32 v239, v237 offset:30720
s_setprio 0
s_mov_b64 exec, 1
s_waitcnt vmcnt(0)
buffer_atomic_swap_x2 v[248:249], off, s[48:51], s98 offset:0 sc1
s_mov_b64 exec, -1
buffer_load_dwordx4 v[228:231], v253, s[20:23], s59 offen
s_branch .Lk_final
.Lk_fb:
s_cmp_lg_u32 s69, 0
s_cbranch_scc1 .Lk_ok
s_mov_b32 s69, 1
s_waitcnt vmcnt(0) lgkmcnt(0)
v_mov_b32 v244, 0xff800000
v_mov_b32 v245, 0xff800000
v_mov_b32 v246, 0xff800000
v_mov_b32 v250, 0xff800000
s_mov_b32 s91, 0
s_mov_b32 s38, 14680064
.Lk_fbl:
s_add_u32 s62, s90, s91
buffer_load_dwordx4 v[184:187], v233, s[36:39], s62 offen sc1
s_waitcnt vmcnt(0)
v_mfma_f32_32x32x16_bf16 v[64:79], v[184:187], v[168:171], 0
v_mfma_f32_32x32x16_bf16 v[80:95], v[184:187], v[172:175], 0
v_mfma_f32_32x32x16_bf16 v[96:111], v[184:187], v[176:179], 0
v_mfma_f32_32x32x16_bf16 v[112:127], v[184:187], v[180:183], 0
s_nop 8
v_max3_f32 v244, v244, v64, v65
v_max3_f32 v244, v244, v66, v67
v_max3_f32 v244, v244, v68, v69
v_max3_f32 v244, v244, v70, v71
v_max3_f32 v244, v244, v72, v73
v_max3_f32 v244, v244, v74, v75
v_max3_f32 v244, v244, v76, v77
v_max3_f32 v244, v244, v78, v79
v_max3_f32 v245, v245, v80, v81
v_max3_f32 v245, v245, v82, v83
v_max3_f32 v245, v245, v84, v85
v_max3_f32 v245, v245, v86, v87
v_max3_f32 v245, v245, v88, v89
v_max3_f32 v245, v245, v90, v91
v_max3_f32 v245, v245, v92, v93
v_max3_f32 v245, v245, v94, v95
v_max3_f32 v246, v246, v96, v97
v_max3_f32 v246, v246, v98, v99
v_max3_f32 v246, v246, v100, v101
v_max3_f32 v246, v246, v102, v103
v_max3_f32 v246, v246, v104, v105
v_max3_f32 v246, v246, v106, v107
v_max3_f32 v246, v246, v108, v109
v_max3_f32 v246, v246, v110, v111
v_max3_f32 v250, v250, v112, v113
v_max3_f32 v250, v250, v114, v115
v_max3_f32 v250, v250, v116, v117
v_max3_f32 v250, v250, v118, v119
v_max3_f32 v250, v250, v120, v121
v_max3_f32 v250, v250, v122, v123
v_max3_f32 v250, v250, v124, v125
v_max3_f32 v250, v250, v126, v127
s_add_u32 s91, s91, 2048
s_cmp_lt_u32 s91, 0x10000
s_cbranch_scc1 .Lk_fbl
v_mov_b32 v251, v244
v_mov_b32 v252, v245
v_mov_b32 v240, v246
v_mov_b32 v241, v250
v_permlane32_swap_b32 v244, v251
v_permlane32_swap_b32 v245, v252
)ASM" R"ASM(
v_permlane32_swap_b32 v246, v240
v_permlane32_swap_b32 v250, v241
v_max_f32 v244, v244, v251
v_max_f32 v245, v245, v252
v_max_f32 v246, v246, v240
v_max_f32 v250, v250, v241
v_sub_f32 v64, 0, v244
v_sub_f32 v65, 0, v244
v_sub_f32 v66, 0, v244
v_sub_f32 v67, 0, v244
v_sub_f32 v68, 0, v244
v_sub_f32 v69, 0, v244
v_sub_f32 v70, 0, v244
v_sub_f32 v71, 0, v244
v_sub_f32 v72, 0, v244
v_sub_f32 v73, 0, v244
v_sub_f32 v74, 0, v244
v_sub_f32 v75, 0, v244
v_sub_f32 v76, 0, v244
v_sub_f32 v77, 0, v244
v_sub_f32 v78, 0, v244
v_sub_f32 v79, 0, v244
v_sub_f32 v80, 0, v245
v_sub_f32 v81, 0, v245
v_sub_f32 v82, 0, v245
v_sub_f32 v83, 0, v245
v_sub_f32 v84, 0, v245
v_sub_f32 v85, 0, v245
v_sub_f32 v86, 0, v245
v_sub_f32 v87, 0, v245
v_sub_f32 v88, 0, v245
v_sub_f32 v89, 0, v245
v_sub_f32 v90, 0, v245
v_sub_f32 v91, 0, v245
v_sub_f32 v92, 0, v245
v_sub_f32 v93, 0, v245
v_sub_f32 v94, 0, v245
v_sub_f32 v95, 0, v245
v_sub_f32 v96, 0, v246
v_sub_f32 v97, 0, v246
v_sub_f32 v98, 0, v246
v_sub_f32 v99, 0, v246
v_sub_f32 v100, 0, v246
v_sub_f32 v101, 0, v246
v_sub_f32 v102, 0, v246
v_sub_f32 v103, 0, v246
v_sub_f32 v104, 0, v246
v_sub_f32 v105, 0, v246
v_sub_f32 v106, 0, v246
v_sub_f32 v107, 0, v246
v_sub_f32 v108, 0, v246
v_sub_f32 v109, 0, v246
v_sub_f32 v110, 0, v246
v_sub_f32 v111, 0, v246
v_sub_f32 v112, 0, v250
v_sub_f32 v113, 0, v250
v_sub_f32 v114, 0, v250
v_sub_f32 v115, 0, v250
v_sub_f32 v116, 0, v250
v_sub_f32 v117, 0, v250
v_sub_f32 v118, 0, v250
v_sub_f32 v119, 0, v250
v_sub_f32 v120, 0, v250
v_sub_f32 v121, 0, v250
v_sub_f32 v122, 0, v250
v_sub_f32 v123, 0, v250
v_sub_f32 v124, 0, v250
v_sub_f32 v125, 0, v250
v_sub_f32 v126, 0, v250
v_sub_f32 v127, 0, v250
v_mov_b32 v0, 0
v_mov_b32 v1, 0
v_mov_b32 v2, 0
v_mov_b32 v3, 0
v_mov_b32 v4, 0
v_mov_b32 v5, 0
v_mov_b32 v6, 0
v_mov_b32 v7, 0
v_mov_b32 v8, 0
v_mov_b32 v9, 0
v_mov_b32 v10, 0
v_mov_b32 v11, 0
v_mov_b32 v12, 0
v_mov_b32 v13, 0
v_mov_b32 v14, 0
v_mov_b32 v15, 0
v_mov_b32 v16, 0
v_mov_b32 v17, 0
v_mov_b32 v18, 0
v_mov_b32 v19, 0
v_mov_b32 v20, 0
v_mov_b32 v21, 0
v_mov_b32 v22, 0
v_mov_b32 v23, 0
v_mov_b32 v24, 0
v_mov_b32 v25, 0
v_mov_b32 v26, 0
v_mov_b32 v27, 0
v_mov_b32 v28, 0
v_mov_b32 v29, 0
v_mov_b32 v30, 0
v_mov_b32 v31, 0
v_mov_b32 v32, 0
v_mov_b32 v33, 0
v_mov_b32 v34, 0
v_mov_b32 v35, 0
v_mov_b32 v36, 0
v_mov_b32 v37, 0
v_mov_b32 v38, 0
v_mov_b32 v39, 0
v_mov_b32 v40, 0
v_mov_b32 v41, 0
v_mov_b32 v42, 0
v_mov_b32 v43, 0
v_mov_b32 v44, 0
v_mov_b32 v45, 0
v_mov_b32 v46, 0
v_mov_b32 v47, 0
v_mov_b32 v48, 0
v_mov_b32 v49, 0
v_mov_b32 v50, 0
v_mov_b32 v51, 0
v_mov_b32 v52, 0
v_mov_b32 v53, 0
v_mov_b32 v54, 0
v_mov_b32 v55, 0
v_mov_b32 v56, 0
v_mov_b32 v57, 0
v_mov_b32 v58, 0
v_mov_b32 v59, 0
v_mov_b32 v60, 0
v_mov_b32 v61, 0
v_mov_b32 v62, 0
v_mov_b32 v63, 0
v_mov_b32 v160, 0
v_mov_b32 v161, 0
v_mov_b32 v162, 0
v_mov_b32 v163, 0
v_mov_b32 v164, 0
v_mov_b32 v165, 0
v_mov_b32 v166, 0
v_mov_b32 v167, 0
v_mov_b32 v224, v238
v_mov_b32 v225, v238
v_mov_b32 v226, v238
v_mov_b32 v227, v238
v_mov_b32 v228, v238
v_mov_b32 v229, v238
v_mov_b32 v230, v238
v_mov_b32 v231, v238
s_mov_b32 s91, s94
s_add_u32 s62, s90, s91
buffer_load_dwordx4 v[184:187], v233, s[36:39], s62 offen sc1
s_mov_b64 exec, s[88:89]
buffer_load_dwordx4 v[192:195], v234, s[36:39], s62 offen offset:1536 sc1
buffer_load_dwordx4 v[188:191], v234, s[36:39], s62 offen offset:1024 sc1
s_mov_b64 exec, -1
s_add_u32 s91, s91, 2048
s_and_b32 s91, s91, 0xffff
s_add_u32 s62, s90, s91
buffer_load_dwordx4 v[196:199], v233, s[36:39], s62 offen sc1
s_mov_b64 exec, s[88:89]
buffer_load_dwordx4 v[204:207], v234, s[36:39], s62 offen offset:1536 sc1
buffer_load_dwordx4 v[200:203], v234, s[36:39], s62 offen offset:1024 sc1
s_mov_b64 exec, -1
s_add_u32 s91, s91, 2048
s_and_b32 s91, s91, 0xffff
s_add_u32 s62, s90, s91
buffer_load_dwordx4 v[208:211], v233, s[36:39], s62 offen sc1
s_mov_b64 exec, s[88:89]
buffer_load_dwordx4 v[216:219], v234, s[36:39], s62 offen offset:1536 sc1
buffer_load_dwordx4 v[212:215], v234, s[36:39], s62 offen offset:1024 sc1
s_mov_b64 exec, -1
s_add_u32 s91, s91, 2048
s_and_b32 s91, s91, 0xffff
s_waitcnt vmcnt(0)
v_mfma_f32_32x32x16_bf16 v[128:143], v[184:187], v[168:171], v[64:79]
s_nop 11
s_branch .Lk_again
.Lk_ls_3:
s_mov_b32 s68, 2000000
.Lk_ls_3a:
ds_read_b32 v252, v245 offset:256
s_sub_u32 s68, s68, 1
s_waitcnt lgkmcnt(0)
s_cbranch_scc1 .Lk_ls_3b
v_cmp_gt_u32 vcc, 4, v252
s_nop 1
s_cbranch_vccnz .Lk_ls_3a
.Lk_ls_3b:
s_branch .Lk_ld_4
.Lk_ls_5:
s_mov_b32 s68, 2000000
.Lk_ls_5a:
ds_read_b32 v252, v245 offset:768
s_sub_u32 s68, s68, 1
s_waitcnt lgkmcnt(0)
s_cbranch_scc1 .Lk_ls_5b
v_cmp_gt_u32 vcc, 4, v252
s_nop 1
)ASM" R"ASM(
s_cbranch_vccnz .Lk_ls_5a
.Lk_ls_5b:
s_branch .Lk_ld_6
.Lk_ls_7:
s_mov_b32 s68, 2000000
.Lk_ls_7a:
ds_read_b32 v252, v245 offset:0
s_sub_u32 s68, s68, 1
s_waitcnt lgkmcnt(0)
s_cbranch_scc1 .Lk_ls_7b
v_cmp_gt_u32 vcc, 4, v252
s_nop 1
s_cbranch_vccnz .Lk_ls_7a
.Lk_ls_7b:
s_branch .Lk_ld_8
.Lk_ls_9:
s_mov_b32 s68, 2000000
.Lk_ls_9a:
ds_read_b32 v252, v245 offset:256
s_sub_u32 s68, s68, 1
s_waitcnt lgkmcnt(0)
s_cbranch_scc1 .Lk_ls_9b
v_cmp_gt_u32 vcc, 4, v252
s_nop 1
s_cbranch_vccnz .Lk_ls_9a
.Lk_ls_9b:
s_branch .Lk_ld_10
.Lk_ls_11:
s_mov_b32 s68, 2000000
.Lk_ls_11a:
ds_read_b32 v252, v245 offset:768
s_sub_u32 s68, s68, 1
s_waitcnt lgkmcnt(0)
s_cbranch_scc1 .Lk_ls_11b
v_cmp_gt_u32 vcc, 4, v252
s_nop 1
s_cbranch_vccnz .Lk_ls_11a
.Lk_ls_11b:
s_branch .Lk_ld_12
.Lk_ls_13:
s_mov_b32 s68, 2000000
.Lk_ls_13a:
ds_read_b32 v252, v245 offset:0
s_sub_u32 s68, s68, 1
s_waitcnt lgkmcnt(0)
s_cbranch_scc1 .Lk_ls_13b
v_cmp_gt_u32 vcc, 4, v252
s_nop 1
s_cbranch_vccnz .Lk_ls_13a
.Lk_ls_13b:
s_branch .Lk_ld_14
.Lk_ls_15:
s_mov_b32 s68, 2000000
.Lk_ls_15a:
s_sleep 1
ds_read_b32 v252, v245 offset:29184
s_sub_u32 s68, s68, 1
s_waitcnt lgkmcnt(0)
s_cbranch_scc1 .Lk_ls_15b
v_cmp_gt_u32 vcc, 4, v252
s_nop 1
s_cbranch_vccnz .Lk_ls_15a
.Lk_ls_15b:
s_branch .Lk_ld_16
.Lk_ls_17:
s_mov_b32 s68, 2000000
s_setprio 3
.Lk_ls_17a:
ds_read_b32 v227, v245 offset:28672
s_sub_u32 s68, s68, 1
s_waitcnt lgkmcnt(0)
s_cbranch_scc1 .Lk_ls_17b
v_cmp_gt_u32 vcc, 4, v227
s_nop 1
s_cbranch_vccnz .Lk_ls_17a
.Lk_ls_17b:
s_setprio 0
s_branch .Lk_ld_18
.Lk_ls_19:
s_mov_b32 s68, 2000000
s_setprio 3
.Lk_ls_19a:
s_sleep 1
ds_read_b32 v227, v245 offset:29440
s_sub_u32 s68, s68, 1
s_waitcnt lgkmcnt(0)
s_cbranch_scc1 .Lk_ls_19b
v_cmp_gt_u32 vcc, 4, v227
s_nop 1
s_cbranch_vccnz .Lk_ls_19a
.Lk_ls_19b:
s_setprio 0
s_branch .Lk_ld_20
.Lk_ls_21:
s_mov_b32 s68, 2000000
s_setprio 3
.Lk_ls_21a:
ds_read_b32 v227, v245 offset:28672
s_sub_u32 s68, s68, 1
s_waitcnt lgkmcnt(0)
s_cbranch_scc1 .Lk_ls_21b
v_cmp_gt_u32 vcc, 8, v227
s_nop 1
s_cbranch_vccnz .Lk_ls_21a
.Lk_ls_21b:
s_setprio 0
s_branch .Lk_ld_22
.Lk_ls_23:
s_mov_b32 s68, 2000000
s_setprio 3
.Lk_ls_23a:
ds_read_b32 v227, v245 offset:28672
s_sub_u32 s68, s68, 1
s_waitcnt lgkmcnt(0)
s_cbranch_scc1 .Lk_ls_23b
v_cmp_gt_u32 vcc, 12, v227
s_nop 1
s_cbranch_vccnz .Lk_ls_23a
.Lk_ls_23b:
s_setprio 0
s_branch .Lk_ld_24
.Lk_ls_25:
s_mov_b32 s68, 2000000
s_setprio 3
.Lk_ls_25a:
ds_read_b32 v227, v245 offset:28672
s_sub_u32 s68, s68, 1
s_waitcnt lgkmcnt(0)
s_cbranch_scc1 .Lk_ls_25b
v_cmp_gt_u32 vcc, 16, v227
s_nop 1
s_cbranch_vccnz .Lk_ls_25a
.Lk_ls_25b:
s_setprio 0
s_branch .Lk_ld_26
.Lk_ls_27:
s_mov_b32 s68, 2000000
s_setprio 3
.Lk_ls_27a:
ds_read_b32 v227, v245 offset:28672
s_sub_u32 s68, s68, 1
s_waitcnt lgkmcnt(0)
s_cbranch_scc1 .Lk_ls_27b
v_cmp_gt_u32 vcc, 20, v227
s_nop 1
s_cbranch_vccnz .Lk_ls_27a
.Lk_ls_27b:
s_setprio 0
s_branch .Lk_ld_28
.Lk_ls_29:
s_mov_b32 s68, 2000000
s_setprio 3
.Lk_ls_29a:
ds_read_b32 v227, v245 offset:28672
s_sub_u32 s68, s68, 1
s_waitcnt lgkmcnt(0)
s_cbranch_scc1 .Lk_ls_29b
v_cmp_gt_u32 vcc, 24, v227
s_nop 1
s_cbranch_vccnz .Lk_ls_29a
.Lk_ls_29b:
s_setprio 0
s_branch .Lk_ld_30
.Lk_ps_31:
s_mov_b32 s68, 200000
s_setprio 3
.Lk_ps_31a:
s_sleep 2
buffer_load_dwordx2 v[250:251], v225, s[48:51], 0 offen offset:0 sc1
s_sub_u32 s68, s68, 1
s_waitcnt vmcnt(0)
s_cbranch_scc1 .Lk_ps_31b
v_cmp_ne_u64 vcc, v[248:249], v[250:251]
s_nop 1
s_cbranch_vccnz .Lk_ps_31a
.Lk_ps_31b:
s_setprio 0
s_branch .Lk_pd_32
.Lk_ls_33:
s_mov_b32 s68, 2000000
s_setprio 3
.Lk_ls_33a:
ds_read_b32 v227, v245 offset:28672
s_sub_u32 s68, s68, 1
s_waitcnt lgkmcnt(0)
s_cbranch_scc1 .Lk_ls_33b
v_cmp_gt_u32 vcc, 28, v227
s_nop 1
s_cbranch_vccnz .Lk_ls_33a
.Lk_ls_33b:
s_setprio 0
s_branch .Lk_ld_34
.Lk_ls_35:
s_mov_b32 s68, 2000000
s_setprio 3
.Lk_ls_35a:
s_waitcnt lgkmcnt(13)
ds_read_b32 v227, v245 offset:28672
s_sub_u32 s68, s68, 1
s_waitcnt lgkmcnt(0)
)ASM" R"ASM(
s_cbranch_scc1 .Lk_ls_35b
v_cmp_gt_u32 vcc, 32, v227
s_nop 1
s_cbranch_vccnz .Lk_ls_35a
.Lk_ls_35b:
s_setprio 0
s_branch .Lk_ld_36
.Lk_ls_37:
s_mov_b32 s68, 2000000
s_setprio 3
.Lk_ls_37a:
ds_read_b32 v227, v245 offset:28672
s_sub_u32 s68, s68, 1
s_waitcnt lgkmcnt(0)
s_cbranch_scc1 .Lk_ls_37b
v_cmp_gt_u32 vcc, 36, v227
s_nop 1
s_cbranch_vccnz .Lk_ls_37a
.Lk_ls_37b:
s_setprio 0
s_branch .Lk_ld_38
.Lk_ps_39:
s_mov_b32 s68, 200000
s_setprio 3
.Lk_ps_39a:
s_sleep 8
buffer_load_dwordx2 v[250:251], v225, s[48:51], 0 offen offset:8 sc1
s_sub_u32 s68, s68, 1
s_waitcnt vmcnt(0)
s_cbranch_scc1 .Lk_ps_39b
v_cmp_ne_u64 vcc, v[248:249], v[250:251]
s_nop 1
s_cbranch_vccnz .Lk_ps_39a
.Lk_ps_39b:
s_setprio 0
s_branch .Lk_pd_40
.Lk_ls_41:
s_mov_b32 s68, 2000000
s_setprio 3
.Lk_ls_41a:
s_sleep 3
s_waitcnt lgkmcnt(13)
ds_read_b32 v227, v246 offset:30720
s_sub_u32 s68, s68, 1
s_waitcnt lgkmcnt(0)
s_cbranch_scc1 .Lk_ls_41b
v_cmp_gt_u32 vcc, 1, v227
s_nop 1
s_cbranch_vccnz .Lk_ls_41a
.Lk_ls_41b:
s_setprio 0
s_branch .Lk_ld_42
.Lk_ps_43:
s_mov_b32 s68, 200000
s_setprio 3
.Lk_ps_43a:
s_sleep 2
v_mov_b32 v250, v248
v_mov_b32 v251, v249
s_mov_b64 exec, 255
v_mov_b32 v250, 0
v_mov_b32 v251, 0
buffer_atomic_or_x2 v[250:251], v226, s[48:51], 0 offen offset:0 sc0 sc1
s_mov_b64 exec, -1
s_sub_u32 s68, s68, 1
s_waitcnt vmcnt(0)
s_cbranch_scc1 .Lk_ps_43b
v_cmp_ne_u64 vcc, v[248:249], v[250:251]
s_nop 1
s_cbranch_vccnz .Lk_ps_43a
.Lk_ps_43b:
s_setprio 0
s_branch .Lk_pd_44
.Lk_ls_45:
s_mov_b32 s68, 2000000
s_setprio 3
.Lk_ls_45a:
ds_read_b32 v227, v245 offset:28672
s_sub_u32 s68, s68, 1
s_waitcnt lgkmcnt(0)
s_cbranch_scc1 .Lk_ls_45b
v_cmp_gt_u32 vcc, 4, v227
s_nop 1
s_cbranch_vccnz .Lk_ls_45a
.Lk_ls_45b:
s_setprio 0
s_branch .Lk_ld_46
.Lk_ls_47:
s_mov_b32 s68, 2000000
s_setprio 3
.Lk_ls_47a:
s_sleep 1
s_waitcnt lgkmcnt(13)
ds_read_b32 v227, v245 offset:29440
s_sub_u32 s68, s68, 1
s_waitcnt lgkmcnt(0)
s_cbranch_scc1 .Lk_ls_47b
v_cmp_gt_u32 vcc, 4, v227
s_nop 1
s_cbranch_vccnz .Lk_ls_47a
.Lk_ls_47b:
s_setprio 0
s_branch .Lk_ld_48
.Lk_ls_49:
s_mov_b32 s68, 2000000
s_setprio 3
.Lk_ls_49a:
ds_read_b32 v227, v245 offset:28672
s_sub_u32 s68, s68, 1
s_waitcnt lgkmcnt(0)
s_cbranch_scc1 .Lk_ls_49b
v_cmp_gt_u32 vcc, 8, v227
s_nop 1
s_cbranch_vccnz .Lk_ls_49a
.Lk_ls_49b:
s_setprio 0
s_branch .Lk_ld_50
.Lk_ls_51:
s_mov_b32 s68, 2000000
s_setprio 3
.Lk_ls_51a:
s_waitcnt lgkmcnt(13)
ds_read_b32 v227, v245 offset:28672
s_sub_u32 s68, s68, 1
s_waitcnt lgkmcnt(0)
s_cbranch_scc1 .Lk_ls_51b
v_cmp_gt_u32 vcc, 12, v227
s_nop 1
s_cbranch_vccnz .Lk_ls_51a
.Lk_ls_51b:
s_setprio 0
s_branch .Lk_ld_52
.Lk_ls_53:
s_mov_b32 s68, 2000000
s_setprio 3
.Lk_ls_53a:
ds_read_b32 v227, v245 offset:28672
s_sub_u32 s68, s68, 1
s_waitcnt lgkmcnt(0)
s_cbranch_scc1 .Lk_ls_53b
v_cmp_gt_u32 vcc, 16, v227
s_nop 1
s_cbranch_vccnz .Lk_ls_53a
.Lk_ls_53b:
s_setprio 0
s_branch .Lk_ld_54
.Lk_ls_55:
s_mov_b32 s68, 2000000
s_setprio 3
.Lk_ls_55a:
s_waitcnt lgkmcnt(13)
ds_read_b32 v227, v245 offset:28672
s_sub_u32 s68, s68, 1
s_waitcnt lgkmcnt(0)
s_cbranch_scc1 .Lk_ls_55b
v_cmp_gt_u32 vcc, 20, v227
s_nop 1
s_cbranch_vccnz .Lk_ls_55a
.Lk_ls_55b:
s_setprio 0
s_branch .Lk_ld_56
.Lk_ls_57:
s_mov_b32 s68, 2000000
s_setprio 3
.Lk_ls_57a:
ds_read_b32 v227, v245 offset:28672
s_sub_u32 s68, s68, 1
s_waitcnt lgkmcnt(0)
s_cbranch_scc1 .Lk_ls_57b
v_cmp_gt_u32 vcc, 24, v227
s_nop 1
s_cbranch_vccnz .Lk_ls_57a
.Lk_ls_57b:
s_setprio 0
s_branch .Lk_ld_58
.Lk_ps_59:
s_mov_b32 s68, 200000
s_setprio 3
.Lk_ps_59a:
s_sleep 2
buffer_load_dwordx2 v[250:251], v225, s[48:51], 0 offen offset:0 sc1
s_sub_u32 s68, s68, 1
s_waitcnt vmcnt(0)
s_cbranch_scc1 .Lk_ps_59b
v_cmp_ne_u64 vcc, v[248:249], v[250:251]
s_nop 1
s_cbranch_vccnz .Lk_ps_59a
.Lk_ps_59b:
s_setprio 0
s_branch .Lk_pd_60
.Lk_ls_61:
s_mov_b32 s68, 2000000
s_setprio 3
.Lk_ls_61a:
s_waitcnt lgkmcnt(13)
ds_read_b32 v227, v245 offset:28672
s_sub_u32 s68, s68, 1
s_waitcnt lgkmcnt(0)
s_cbranch_scc1 .Lk_ls_61b
v_cmp_gt_u32 vcc, 28, v227
)ASM" R"ASM(
s_nop 1
s_cbranch_vccnz .Lk_ls_61a
.Lk_ls_61b:
s_setprio 0
s_branch .Lk_ld_62
.Lk_ls_63:
s_mov_b32 s68, 2000000
s_setprio 3
.Lk_ls_63a:
ds_read_b32 v227, v245 offset:28672
s_sub_u32 s68, s68, 1
s_waitcnt lgkmcnt(0)
s_cbranch_scc1 .Lk_ls_63b
v_cmp_gt_u32 vcc, 32, v227
s_nop 1
s_cbranch_vccnz .Lk_ls_63a
.Lk_ls_63b:
s_setprio 0
s_branch .Lk_ld_64
.Lk_ls_65:
s_mov_b32 s68, 2000000
s_setprio 3
.Lk_ls_65a:
s_waitcnt lgkmcnt(13)
ds_read_b32 v227, v245 offset:28672
s_sub_u32 s68, s68, 1
s_waitcnt lgkmcnt(0)
s_cbranch_scc1 .Lk_ls_65b
v_cmp_gt_u32 vcc, 36, v227
s_nop 1
s_cbranch_vccnz .Lk_ls_65a
.Lk_ls_65b:
s_setprio 0
s_branch .Lk_ld_66
.Lk_ps_67:
s_mov_b32 s68, 200000
s_setprio 3
.Lk_ps_67a:
s_sleep 8
buffer_load_dwordx2 v[250:251], v225, s[48:51], 0 offen offset:8 sc1
s_sub_u32 s68, s68, 1
s_waitcnt vmcnt(0)
s_cbranch_scc1 .Lk_ps_67b
v_cmp_ne_u64 vcc, v[248:249], v[250:251]
s_nop 1
s_cbranch_vccnz .Lk_ps_67a
.Lk_ps_67b:
s_setprio 0
s_branch .Lk_pd_68
.Lk_ls_69:
s_mov_b32 s68, 2000000
s_setprio 3
.Lk_ls_69a:
s_sleep 3
ds_read_b32 v227, v246 offset:30720
s_sub_u32 s68, s68, 1
s_waitcnt lgkmcnt(0)
s_cbranch_scc1 .Lk_ls_69b
v_cmp_gt_u32 vcc, 1, v227
s_nop 1
s_cbranch_vccnz .Lk_ls_69a
.Lk_ls_69b:
s_setprio 0
s_branch .Lk_ld_70
.Lk_ps_71:
s_mov_b32 s68, 200000
s_setprio 3
.Lk_ps_71a:
s_sleep 2
v_mov_b32 v250, v248
v_mov_b32 v251, v249
s_mov_b64 exec, 255
v_mov_b32 v250, 0
v_mov_b32 v251, 0
buffer_atomic_or_x2 v[250:251], v226, s[48:51], 0 offen offset:0 sc0 sc1
s_mov_b64 exec, -1
s_sub_u32 s68, s68, 1
s_waitcnt vmcnt(0)
s_cbranch_scc1 .Lk_ps_71b
v_cmp_ne_u64 vcc, v[248:249], v[250:251]
s_nop 1
s_cbranch_vccnz .Lk_ps_71a
.Lk_ps_71b:
s_setprio 0
s_branch .Lk_pd_72
.Lk_ls_73:
s_mov_b32 s68, 2000000
.Lk_ls_73a:
s_sleep 2
s_waitcnt lgkmcnt(13)
ds_read_b32 v237, v239 offset:512
s_sub_u32 s68, s68, 1
s_waitcnt lgkmcnt(0)
s_cbranch_scc1 .Lk_ls_73b
v_cmp_gt_u32 vcc, 4, v237
s_nop 1
s_cbranch_vccnz .Lk_ls_73a
.Lk_ls_73b:
s_branch .Lk_ld_74
.Lk_ls_75:
s_mov_b32 s68, 2000000
.Lk_ls_75a:
s_sleep 1
ds_read_b32 v237, v239 offset:32512
s_sub_u32 s68, s68, 1
s_waitcnt lgkmcnt(0)
s_cbranch_scc1 .Lk_ls_75b
v_cmp_gt_u32 vcc, 4, v237
s_nop 1
s_cbranch_vccnz .Lk_ls_75a
.Lk_ls_75b:
s_branch .Lk_ld_76
.Lk_ls_77:
s_mov_b32 s68, 2000000
.Lk_ls_77a:
s_sleep 1
s_waitcnt lgkmcnt(13)
ds_read_b32 v237, v239 offset:28928
s_sub_u32 s68, s68, 1
s_waitcnt lgkmcnt(0)
s_cbranch_scc1 .Lk_ls_77b
v_cmp_gt_u32 vcc, 4, v237
s_nop 1
s_cbranch_vccnz .Lk_ls_77a
.Lk_ls_77b:
s_branch .Lk_ld_78
.Lk_ps_79:
s_mov_b32 s68, 200000
.Lk_ps_79a:
s_sleep 2
buffer_load_dwordx2 v[240:241], v236, s[48:51], 0 offen offset:0 sc1
s_sub_u32 s68, s68, 1
s_waitcnt vmcnt(0)
s_cbranch_scc1 .Lk_ps_79b
v_cmp_ne_u64 vcc, v[248:249], v[240:241]
s_nop 1
s_cbranch_vccnz .Lk_ps_79a
.Lk_ps_79b:
s_branch .Lk_pd_80
.Lk_end:
)ASM"
        : "+{v0}"(tid), "+{s[0:1]}"(ka), "+{s2}"(wgid), "+{s3}"(lds0)
        :
        : "memory", "vcc", "scc",
        "v1", "v2", "v3", "v4", "v5", "v6", "v7", "v8", "v9", "v10", "v11", "v12", "v13", "v14", "v15", "v16", "v17", "v18", "v19", "v20", "v21", "v22", "v23", "v24", "v25", "v26", "v27", "v28", "v29", "v30", "v31", "v32", "v33", "v34", "v35", "v36", "v37", "v38", "v39", "v40", "v41", "v42", "v43", "v44", "v45", "v46", "v47", "v48", "v49", "v50", "v51", "v52", "v53", "v54", "v55", "v56", "v57", "v58", "v59", "v60", "v61", "v62", "v63", "v64", "v65", "v66", "v67", "v68", "v69", "v70", "v71", "v72", "v73", "v74", "v75", "v76", "v77", "v78", "v79", "v80", "v81", "v82", "v83", "v84", "v85", "v86", "v87", "v88", "v89", "v90", "v91", "v92", "v93", "v94", "v95", "v96", "v97", "v98", "v99", "v100", "v101", "v102", "v103", "v104", "v105", "v106", "v107", "v108", "v109", "v110", "v111", "v112", "v113", "v114", "v115", "v116", "v117", "v118", "v119", "v120", "v121", "v122", "v123", "v124", "v125", "v126", "v127", "v128", "v129", "v130", "v131", "v132", "v133", "v134", "v135", "v136", "v137", "v138", "v139", "v140", "v141", "v142", "v143", "v144", "v145", "v146", "v147", "v148", "v149", "v150", "v151", "v152", "v153", "v154", "v155", "v156", "v157", "v158", "v159", "v160", "v161", "v162", "v163", "v164", "v165", "v166", "v167", "v168", "v169", "v170", "v171", "v172", "v173", "v174", "v175", "v176", "v177", "v178", "v179", "v180", "v181", "v182", "v183", "v184", "v185", "v186", "v187", "v188", "v189", "v190", "v191", "v192", "v193", "v194", "v195", "v196", "v197", "v198", "v199", "v200", "v201", "v202", "v203", "v204", "v205", "v206", "v207", "v208", "v209", "v210", "v211", "v212", "v213", "v214", "v215", "v216", "v217", "v218", "v219", "v220", "v221", "v222", "v223", "v224", "v225", "v226", "v227", "v228", "v229", "v230", "v231", "v232", "v233", "v234", "v235", "v236", "v237", "v238", "v239", "v240", "v241", "v242", "v243", "v244", "v245", "v246", "v247", "v248", "v249", "v250", "v251", "v252", "v253", "v254", "v255",
        "s4", "s5", "s6", "s7", "s8", "s9", "s10", "s11", "s12", "s13", "s14", "s15", "s16", "s17", "s18", "s19", "s20", "s21", "s22", "s23", "s24", "s25", "s26", "s27", "s28", "s29", "s30", "s31", "s32", "s33", "s34", "s35", "s36", "s37", "s38", "s39", "s40", "s41", "s42", "s43", "s44", "s45", "s46", "s47", "s48", "s49", "s50", "s51", "s52", "s53", "s54", "s55", "s56", "s57", "s58", "s59", "s60", "s61", "s62", "s63", "s64", "s65", "s66", "s67", "s68", "s69", "s70", "s71", "s72", "s73", "s74", "s75", "s76", "s77", "s78", "s79", "s80", "s81", "s82", "s83", "s84", "s85", "s86", "s87", "s88", "s89", "s90", "s91", "s92", "s93", "s94", "s95", "s96", "s97", "s98", "s99");
}

extern "C" void kernel_launch(void* const* d_in, const int* in_sizes, int n_in, void* d_out, int out_size,
                              void* d_ws, size_t ws_size, hipStream_t stream) {
    if (ws_size >= ((size_t)112 << 20)) d_ws = (char*)d_ws + ((size_t)96 << 20);
    fused_block_kernel<<<256, 512, 0, stream>>>(d_in[0], d_in[1], d_in[2], d_in[4], d_in[5], d_in[6], d_out, d_ws);
}
```
